```python
import torch

def _softmax(x, axis=-1):
    return torch.softmax(x, dim=axis)
def _gelu(x):
    return torch.nn.functional.gelu(x, approximate="tanh")
def _silu(x):
    return torch.nn.functional.silu(x)
def _sigmoid(x):
    return torch.sigmoid(x)
def _layernorm(x, axis=-1, eps=1e-5):
    m = x.mean(dim=axis, keepdim=True)
    v = x.var(dim=axis, keepdim=True, unbiased=False)
    return (x - m) / torch.sqrt(v + eps)
def _scatter_add(tgt, idx, src, axis=0):
    return tgt.clone().index_add_(axis, idx, src)
def _segment_sum(src, seg, n):
    out = torch.zeros((n,) + src.shape[1:], dtype=src.dtype)
    return out.index_add_(0, seg, src)
def _pad(x, pw):
    flat = tuple(v for pair in reversed(pw) for v in pair)
    return torch.nn.functional.pad(x, flat)

kernel_name = 'interleave_layers__softmax_full_mha__dense_activation_mish__prompt'

def setup_inputs():

    _w = (lambda a: (lambda t: t.to(torch.bfloat16) if t.is_floating_point() else t)(torch.as_tensor(a)))
    def _A_setup():
        B, S, D = 1, 2048, 448
        return {
            "x": _w((lambda *s: torch.randn(*s, dtype=torch.bfloat16) * 0.05)(B, S, D)),
            "wqkv": _w((lambda *s: torch.randn(*s, dtype=torch.bfloat16) * 0.05)(D, 3 * D)),
            "wo": _w((lambda *s: torch.randn(*s, dtype=torch.bfloat16) * 0.05)(D, D)),
        }
    _A_in = _A_setup()

    def _B_setup():
        B, S, D, F = 1, 2048, 448, 1792
        return {
            "x": _w((lambda *s: torch.randn(*s, dtype=torch.bfloat16) * 0.05)(B, S, D)),
            "w1": _w((lambda *s: torch.randn(*s, dtype=torch.bfloat16) * 1.6)(D, F) * (D ** -0.5)),
            "b1": _w((lambda *s: torch.randn(*s, dtype=torch.bfloat16) * 0.2)(F)),
            "w2": _w((lambda *s: torch.randn(*s, dtype=torch.bfloat16) * 0.2)(F, D) * (F ** -0.5)),
        }
    _B_in = _B_setup()

    return {
        "A_x": _A_in["x"],
        "A_wqkv": _A_in["wqkv"],
        "A_wo": _A_in["wo"],
        "B_x": _B_in["x"],
        "B_w1": _B_in["w1"],
        "B_b1": _B_in["b1"],
        "B_w2": _B_in["w2"],
    }

def reference(A_x, A_wqkv, A_wo, B_x, B_w1, B_b1, B_w2):
    def _A_ref(x, wqkv, wo):
        B, S, D = x.shape
        H = 16; Dh = D // H
        qkv = (lambda x, *d: x.permute(*d))((x @ wqkv).reshape(B, S, 3, H, Dh), 2, 0, 3, 1, 4)
        q, k, v = qkv[0], qkv[1], qkv[2]
        scale = Dh ** -0.5
        scores = torch.einsum("bhqd,bhkd->bhqk", q, k) * scale
        p = _softmax(scores, axis=-1)
        o = torch.einsum("bhqk,bhkd->bhqd", p, v)
        return (lambda x, *d: x.permute(*d))(o, 0, 2, 1, 3).reshape(B, S, D) @ wo

    def _B_ref(x, w1, b1, w2):
        h = torch.einsum("bsd,df->bsf", x, w1) + b1
        sp = torch.log1p(torch.exp(-torch.abs(h))) + torch.where(h > 0, h, torch.zeros_like(h))
        act = h * torch.tanh(sp)
        return torch.einsum("bsf,fd->bsd", act, w2)

    x = A_x
    x = x + _A_ref(_layernorm(x, axis=-1), A_wqkv, A_wo)
    x = x + _B_ref(_layernorm(x, axis=-1), B_w1, B_b1, B_w2)
    x = x + _A_ref(_layernorm(x, axis=-1), A_wqkv, A_wo)
    x = x + _B_ref(_layernorm(x, axis=-1), B_w1, B_b1, B_w2)
    return x
```

```cpp
#include <hip/hip_runtime.h>
#include <hip/hip_bf16.h>
#include <stdint.h>

typedef short bf16x8 __attribute__((ext_vector_type(8)));
typedef float f32x16 __attribute__((ext_vector_type(16)));
typedef float f32x4 __attribute__((ext_vector_type(4)));
typedef float f32x2 __attribute__((ext_vector_type(2)));
typedef unsigned u32x4 __attribute__((ext_vector_type(4)));
typedef unsigned u32x2 __attribute__((ext_vector_type(2)));
typedef unsigned short u16;
#define DI __device__ __forceinline__
#define MFMA(a, b, c) __builtin_amdgcn_mfma_f32_32x32x16_bf16((a), (b), (c), 0, 0, 0)

constexpr int S = 2048, D = 448, H = 16, DH = 28, F = 1792;
constexpr int MT = 64, XS = 28, FS = 112, OS = 32;
constexpr size_t KB = 1024;
constexpr size_t WQKV_T = 0;
constexpr size_t WO_T   = WQKV_T + 16 * 3 * 28 * KB;
constexpr size_t W1_T   = WO_T + 14 * 32 * KB;
constexpr size_t W2_T   = W1_T + 56 * 28 * KB;
constexpr size_t X_T    = W2_T + 14 * 112 * KB;
constexpr size_t XSZ    = 64 * 28 * KB;
constexpr size_t ST     = X_T + 4 * XSZ;
constexpr size_t STSZ   = 7 * 2048 * 8;
constexpr size_t Q_T    = ST + 4 * STSZ;
constexpr size_t K_T    = Q_T + 2048 * KB;
constexpr size_t V_T    = K_T + 2048 * KB;
constexpr size_t O_T    = V_T + 2048 * KB;
constexpr size_t H_T    = O_T + 2048 * KB;
constexpr size_t CS     = H_T + 64 * 112 * KB;

typedef __bf16 bf16x2_t __attribute__((ext_vector_type(2)));
DI unsigned pk2(float lo, float hi) { f32x2 v = {lo, hi}; bf16x2_t r = __builtin_convertvector(v, bf16x2_t); return __builtin_bit_cast(unsigned, r); }
DI float bflo(unsigned u) { return __uint_as_float(u << 16); }
DI float bfhi(unsigned u) { return __uint_as_float(u & 0xffff0000u); }
DI float bf2f(u16 v) { return __uint_as_float((unsigned)v << 16); }
DI u32x4 pack8(const f32x16& a, int s) {
    u32x4 r; r[0] = pk2(a[8 * s], a[8 * s + 1]); r[1] = pk2(a[8 * s + 2], a[8 * s + 3]); r[2] = pk2(a[8 * s + 4], a[8 * s + 5]); r[3] = pk2(a[8 * s + 6], a[8 * s + 7]); return r; }
DI bf16x8 asfrag(u32x4 v) { return __builtin_bit_cast(bf16x8, v); }
DI int kperm(int s, int h, int j) { return 16 * s + 8 * (j >> 2) + 4 * h + (j & 3); }
DI float xhalf(float v) { return __shfl_xor(v, 32); }

__global__ void __launch_bounds__(256) k_prep(const u16* __restrict__ x, const u16* __restrict__ wqkv, const u16* __restrict__ wo,
                                              const u16* __restrict__ w1, const u16* __restrict__ w2, char* ws) {
    __shared__ float T[448 * 33];
    const int u = blockIdx.x, tid = threadIdx.x;
    if (u < 174) {
        const u16* src; int ld, ncol = 32; bool center = false; char* dst; int nstep = 28; bool isO = false;
        if (u < 48) { int h = u / 3, part = u % 3; src = wqkv + part * 448 + h * 28; ld = 1344; ncol = 28; center = true; dst = ws + WQKV_T + (size_t)u * 28 * KB; }
        else if (u < 104) { int nt = u - 48; src = w1 + nt * 32; ld = 1792; center = true; dst = ws + W1_T + (size_t)nt * 28 * KB; }
        else if (u < 118) { int nt = u - 104; src = wo + nt * 32; ld = 448; dst = ws + WO_T + (size_t)nt * 32 * KB; nstep = 32; isO = true; }
        else { int v = u - 118, nt = v >> 2, kq = v & 3; src = w2 + (size_t)kq * 448 * 448 + nt * 32; ld = 448; dst = ws + W2_T + ((size_t)nt * 112 + kq * 28) * KB; }
        for (int i = tid; i < 448 * 32; i += 256) { int k = i >> 5, n = i & 31; T[k * 33 + n] = n < ncol ? bf2f(src[(size_t)k * ld + n]) : 0.f; }
        __syncthreads();
        if (tid < 32 && center) { float s = 0; for (int k = 0; k < 448; ++k) s += T[k * 33 + tid]; ((float*)(ws + CS))[u * 32 + tid] = s; }
        __syncthreads();
        for (int c = tid; c < nstep * 64; c += 256) {
            int s = c >> 6, lane = c & 63, r = lane & 31, h = lane >> 5; float v[8];
            for (int j = 0; j < 8; ++j) {
                int k; bool ok = true;
                if (isO) { int d = kperm(s & 1, h, j); ok = d < 28; k = (s >> 1) * 28 + d; } else k = kperm(s, h, j);
                v[j] = ok ? T[k * 33 + r] : 0.f;
            }
            u32x4 o = {pk2(v[0], v[1]), pk2(v[2], v[3]), pk2(v[4], v[5]), pk2(v[6], v[7])};
            *(u32x4*)(dst + (size_t)c * 16) = o;
        }
    } else {
        const int mt = u - 174;
        for (int i = tid; i < 32 * 448; i += 256) { int t = i / 448, k = i % 448; T[t * 449 + k] = bf2f(x[(size_t)(mt * 32 + t) * 448 + k]); }
        __syncthreads();
        if (tid < 32) { float s = 0, q = 0; for (int k = 0; k < 448; ++k) { float v = T[tid * 449 + k]; s += v; q += v * v; }
            f32x2* st = (f32x2*)(ws + ST); st[mt * 32 + tid] = (f32x2){s, q};
            for (int sl = 1; sl < 7; ++sl) st[sl * 2048 + mt * 32 + tid] = (f32x2){0.f, 0.f}; }
        char* dst = ws + X_T + (size_t)mt * 28 * KB;
        for (int c = tid; c < 28 * 64; c += 256) {
            int s = c >> 6, lane = c & 63, r = lane & 31, h = lane >> 5; float v[8];
            for (int j = 0; j < 8; ++j) v[j] = T[r * 449 + kperm(s, h, j)];
            u32x4 o = {pk2(v[0], v[1]), pk2(v[2], v[3]), pk2(v[4], v[5]), pk2(v[6], v[7])};
            *(u32x4*)(dst + (size_t)c * 16) = o;
        }
    }
}

DI float rstd_of(const f32x2* st, int tok, float& mu) {
    float s = 0, q = 0;
#pragma unroll
    for (int sl = 0; sl < 7; ++sl) { f32x2 v = st[sl * 2048 + tok]; s += v[0]; q += v[1]; }
    mu = s * (1.f / 448.f); float var = fmaxf(q * (1.f / 448.f) - mu * mu, 0.f);
    return __builtin_amdgcn_rsqf(var + 1e-5f);
}

__global__ void __launch_bounds__(256, 2) k_qkv(char* ws, int xi) {
    extern __shared__ __attribute__((aligned(16))) char lds[];
    const int b = blockIdx.x, tid = threadIdx.x, lane = tid & 63, w = __builtin_amdgcn_readfirstlane(tid >> 6);
    const int p = (b & 7) * 2 + ((b >> 3) & 1), h = b >> 4;
    const int mt = p * 4 + w, r = lane & 31, hh = lane >> 5;
    const char* wsrc = ws + WQKV_T + (size_t)h * 84 * KB;
    for (int i = w; i < 84; i += 4)
        __builtin_amdgcn_global_load_lds((const unsigned*)(wsrc + i * KB + lane * 16), (__attribute__((address_space(3))) unsigned*)(lds + i * KB), 16, 0, 0);
    const char* xsrc = ws + X_T + (size_t)xi * XSZ + (size_t)mt * 28 * KB + lane * 16;
    bf16x8 xf[28];
#pragma unroll
    for (int s = 0; s < 28; ++s) xf[s] = *(const bf16x8*)(xsrc + s * KB);
    float mu; const float rs = rstd_of((const f32x2*)(ws + ST + (size_t)xi * STSZ), mt * 32 + r, mu);
    float* rsl = (float*)(lds + 84 * KB) + w * 64;
    if (hh == 0) { rsl[r] = rs; rsl[32 + r] = mu; }
    const float* cs = (const float*)(ws + CS) + h * 96;
    f32x4 cq[4], ck[4]; const float cv = cs[64 + r];
#pragma unroll
    for (int q = 0; q < 4; ++q) { cq[q] = *(const f32x4*)(cs + 8 * q + 4 * hh); ck[q] = *(const f32x4*)(cs + 32 + 8 * q + 4 * hh); }
    __syncthreads();
    f32x16 aq = {}, ak = {}, av = {};
#pragma unroll
    for (int s = 0; s < 28; ++s) {
        bf16x8 wq = *(const bf16x8*)(lds + (0 * 28 + s) * KB + lane * 16);
        bf16x8 wk = *(const bf16x8*)(lds + (1 * 28 + s) * KB + lane * 16);
        bf16x8 wv = *(const bf16x8*)(lds + (2 * 28 + s) * KB + lane * 16);
        aq = MFMA(wq, xf[s], aq); ak = MFMA(wk, xf[s], ak); av = MFMA(xf[s], wv, av);
    }
    const float qs = rs * 0.18898223650461362f * 1.4426950408889634f;
#pragma unroll
    for (int i = 0; i < 16; ++i) { aq[i] = fmaf(-mu, cq[i >> 2][i & 3], aq[i]) * qs; ak[i] = fmaf(-mu, ck[i >> 2][i & 3], ak[i]) * rs; }
    if (hh == 1) { aq[12] = aq[13] = aq[14] = aq[15] = 0.f; ak[12] = ak[13] = ak[14] = 1.f; ak[15] = 0.f; }
#pragma unroll
    for (int q = 0; q < 4; ++q) { f32x4 rv = *(const f32x4*)(rsl + 8 * q + 4 * hh), mv = *(const f32x4*)(rsl + 32 + 8 * q + 4 * hh);
#pragma unroll
        for (int i = 0; i < 4; ++i) av[4 * q + i] = r < 28 ? fmaf(-mv[i], cv, av[4 * q + i]) * rv[i] : (r == 28 ? 1.f : 0.f); }
    const size_t o = ((size_t)h * 64 + mt) * 2 * KB + lane * 16;
    *(u32x4*)(ws + Q_T + o) = pack8(aq, 0); *(u32x4*)(ws + Q_T + o + KB) = pack8(aq, 1);
    *(u32x4*)(ws + K_T + o) = pack8(ak, 0); *(u32x4*)(ws + K_T + o + KB) = pack8(ak, 1);
    *(u32x4*)(ws + V_T + o) = pack8(av, 0); *(u32x4*)(ws + V_T + o + KB) = pack8(av, 1);
}

__global__ void __launch_bounds__(256, 2) k_attn(char* ws) {
    extern __shared__ __attribute__((aligned(16))) char lds[];
    const int b = blockIdx.x, tid = threadIdx.x, lane = tid & 63, w = __builtin_amdgcn_readfirstlane(tid >> 6);
    const int h = (b & 7) * 2 + ((b >> 3) & 1), p = b >> 4;
    const int r = lane & 31, hh = lane >> 5;
    bf16x8 qf[4][2];
#pragma unroll
    for (int t = 0; t < 4; ++t)
#pragma unroll
        for (int s = 0; s < 2; ++s) qf[t][s] = *(const bf16x8*)(ws + Q_T + (((size_t)h * 64 + p * 4 + t) * 2 + s) * KB + lane * 16);
    const char* kp = ws + K_T + ((size_t)h * 64 + w * 16) * 2 * KB + lane * 16;
    const char* vp = ws + V_T + ((size_t)h * 64 + w * 16) * 2 * KB + lane * 16;
    float* red = (float*)lds;
    int* flag = (int*)(lds + 64 * KB);
#pragma unroll 1
    for (int attempt = 0; attempt < 2; ++attempt) {
        f32x16 o[4] = {};
        bf16x8 k0 = *(const bf16x8*)(kp), k1 = *(const bf16x8*)(kp + KB), v0 = *(const bf16x8*)(vp), v1 = *(const bf16x8*)(vp + KB);
#pragma unroll 1
        for (int kt = 0; kt < 16; ++kt) {
            const int nx = kt < 15 ? kt + 1 : kt;
            bf16x8 nk0 = *(const bf16x8*)(kp + nx * 2 * KB), nk1 = *(const bf16x8*)(kp + nx * 2 * KB + KB);
            bf16x8 nv0 = *(const bf16x8*)(vp + nx * 2 * KB), nv1 = *(const bf16x8*)(vp + nx * 2 * KB + KB);
#pragma unroll
            for (int t = 0; t < 4; ++t) {
                f32x16 s = {};
                s = MFMA(k0, qf[t][0], s); s = MFMA(k1, qf[t][1], s);
#pragma unroll
                for (int i = 0; i < 16; ++i) s[i] = __builtin_amdgcn_exp2f(s[i]);
                o[t] = MFMA(v0, asfrag(pack8(s, 0)), o[t]); o[t] = MFMA(v1, asfrag(pack8(s, 1)), o[t]);
            }
            k0 = nk0; k1 = nk1; v0 = nv0; v1 = nv1;
        }
        if (tid == 0) *flag = 0;
#pragma unroll
        for (int t = 0; t < 4; ++t)
#pragma unroll
            for (int q = 0; q < 4; ++q) *(f32x4*)(red + (((t * 4 + w) * 4 + q) * 64 + lane) * 4) = (f32x4){o[t][4 * q], o[t][4 * q + 1], o[t][4 * q + 2], o[t][4 * q + 3]};
        __syncthreads();
        f32x16 a;
#pragma unroll
        for (int q = 0; q < 4; ++q) {
            f32x4 s0 = *(const f32x4*)(red + (((w * 4 + 0) * 4 + q) * 64 + lane) * 4), s1 = *(const f32x4*)(red + (((w * 4 + 1) * 4 + q) * 64 + lane) * 4);
            f32x4 s2 = *(const f32x4*)(red + (((w * 4 + 2) * 4 + q) * 64 + lane) * 4), s3 = *(const f32x4*)(red + (((w * 4 + 3) * 4 + q) * 64 + lane) * 4);
            f32x4 t4 = (s0 + s1) + (s2 + s3);
            a[4 * q] = t4[0]; a[4 * q + 1] = t4[1]; a[4 * q + 2] = t4[2]; a[4 * q + 3] = t4[3];
        }
        const float lo = xhalf(a[12]); const float l = hh ? a[12] : lo;
        float amax = 0.f;
#pragma unroll
        for (int i = 0; i < 16; ++i) amax = fmaxf(amax, fabsf(a[i]));
        const bool ok = (l >= 5.4210109e-20f) && (l <= 1.8446744e19f) && (amax <= 3.0e38f);
        if (attempt == 0) {
            if (!ok) *flag = 1;
            __syncthreads();
            if (*flag) {
                __syncthreads();
                float m[4] = {-3.0e38f, -3.0e38f, -3.0e38f, -3.0e38f};
#pragma unroll 1
                for (int kt = 0; kt < 16; ++kt) {
                    bf16x8 c0 = *(const bf16x8*)(kp + kt * 2 * KB), c1 = *(const bf16x8*)(kp + kt * 2 * KB + KB);
#pragma unroll
                    for (int t = 0; t < 4; ++t) { f32x16 s = {}; s = MFMA(c0, qf[t][0], s); s = MFMA(c1, qf[t][1], s);
#pragma unroll
                        for (int i = 0; i < 16; ++i) m[t] = fmaxf(m[t], s[i]); }
                }
#pragma unroll
                for (int t = 0; t < 4; ++t) { m[t] = fmaxf(m[t], xhalf(m[t])); if (hh == 0) red[(w * 4 + t) * 32 + r] = m[t]; }
                __syncthreads();
#pragma unroll
                for (int t = 0; t < 4; ++t) {
                    float mm = fmaxf(fmaxf(red[(0 * 4 + t) * 32 + r], red[(1 * 4 + t) * 32 + r]), fmaxf(red[(2 * 4 + t) * 32 + r], red[(3 * 4 + t) * 32 + r]));
                    float n0 = -mm; unsigned p0 = pk2(n0, 0.f); float r1 = n0 - bflo(p0); unsigned p1 = pk2(r1, 0.f); float r2 = r1 - bflo(p1); unsigned p2 = pk2(r2, 0.f);
                    if (hh == 1) { u32x4 qq = __builtin_bit_cast(u32x4, qf[t][1]); qq[2] = (p0 & 0xffffu) | (p1 << 16); qq[3] = p2 & 0xffffu; qf[t][1] = asfrag(qq); }
                }
                __syncthreads();
                continue;
            }
        }
        const float inv = __builtin_amdgcn_rcpf(l);
#pragma unroll
        for (int i = 0; i < 16; ++i) a[i] *= inv;
        if (hh == 1) { a[12] = a[13] = a[14] = a[15] = 0.f; }
        const size_t oo = O_T + ((size_t)(p * 4 + w) * 32 + h * 2) * KB + lane * 16;
        *(u32x4*)(ws + oo) = pack8(a, 0); *(u32x4*)(ws + oo + KB) = pack8(a, 1);
        break;
    }
}

template <int NS, bool FINAL>
__global__ void __launch_bounds__(256, 2) k_res(char* ws, size_t a_off, size_t w_off, int xi_in, int xi_out, u16* out) {
    extern __shared__ __attribute__((aligned(16))) char lds[];
    const int b = blockIdx.x, tid = threadIdx.x, lane = tid & 63, w = __builtin_amdgcn_readfirstlane(tid >> 6);
    const int idx = b >> 3, hp = (b & 7) * 4 + idx / 7, sl = idx % 7;
    constexpr int NW = NS / 4;
    const char* xa = ws + a_off + ((size_t)(hp * 2) * NS + w * NW) * KB + lane * 16;
    const char* wa = ws + w_off + ((size_t)(sl * 2) * NS + w * NW) * KB + lane * 16;
    f32x16 acc[4] = {};
#pragma unroll 4
    for (int s = 0; s < NW; ++s) {
        bf16x8 x0 = *(const bf16x8*)(xa + s * KB), x1 = *(const bf16x8*)(xa + (NS + s) * KB);
        bf16x8 w0 = *(const bf16x8*)(wa + s * KB), w1 = *(const bf16x8*)(wa + (NS + s) * KB);
        acc[0] = MFMA(w0, x0, acc[0]); acc[1] = MFMA(w0, x1, acc[1]); acc[2] = MFMA(w1, x0, acc[2]); acc[3] = MFMA(w1, x1, acc[3]);
    }
    float* red = (float*)lds;
#pragma unroll
    for (int t = 0; t < 4; ++t)
#pragma unroll
        for (int q = 0; q < 4; ++q) *(f32x4*)(red + (((t * 4 + w) * 4 + q) * 64 + lane) * 4) = (f32x4){acc[t][4 * q], acc[t][4 * q + 1], acc[t][4 * q + 2], acc[t][4 * q + 3]};
    const int n = w >> 1, m = w & 1, mt = hp * 2 + m, nt = sl * 2 + n;
    const size_t xo = ((size_t)mt * 28 + nt * 2) * KB + lane * 16;
    const u32x4 r0 = *(const u32x4*)(ws + X_T + (size_t)xi_in * XSZ + xo), r1 = *(const u32x4*)(ws + X_T + (size_t)xi_in * XSZ + xo + KB);
    __syncthreads();
    f32x16 a;
#pragma unroll
    for (int q = 0; q < 4; ++q) {
        f32x4 s0 = *(const f32x4*)(red + (((w * 4 + 0) * 4 + q) * 64 + lane) * 4), s1 = *(const f32x4*)(red + (((w * 4 + 1) * 4 + q) * 64 + lane) * 4);
        f32x4 s2 = *(const f32x4*)(red + (((w * 4 + 2) * 4 + q) * 64 + lane) * 4), s3 = *(const f32x4*)(red + (((w * 4 + 3) * 4 + q) * 64 + lane) * 4);
        f32x4 t4 = (s0 + s1) + (s2 + s3);
        a[4 * q] = t4[0]; a[4 * q + 1] = t4[1]; a[4 * q + 2] = t4[2]; a[4 * q + 3] = t4[3];
    }
#pragma unroll
    for (int i = 0; i < 4; ++i) { a[2 * i] += bflo(r0[i]); a[2 * i + 1] += bfhi(r0[i]); a[8 + 2 * i] += bflo(r1[i]); a[8 + 2 * i + 1] += bfhi(r1[i]); }
    const u32x4 p0 = pack8(a, 0), p1 = pack8(a, 1);
    const int r = lane & 31, hh = lane >> 5;
    if (FINAL) {
        u16* o = out + (size_t)(mt * 32 + r) * 448 + nt * 32 + 4 * hh;
        *(u32x2*)(o) = (u32x2){p0[0], p0[1]}; *(u32x2*)(o + 8) = (u32x2){p0[2], p0[3]}; *(u32x2*)(o + 16) = (u32x2){p1[0], p1[1]}; *(u32x2*)(o + 24) = (u32x2){p1[2], p1[3]};
    } else {
        *(u32x4*)(ws + X_T + (size_t)xi_out * XSZ + xo) = p0; *(u32x4*)(ws + X_T + (size_t)xi_out * XSZ + xo + KB) = p1;
        float s = 0, q = 0;
#pragma unroll
        for (int i = 0; i < 4; ++i) { float v0 = bflo(p0[i]), v1 = bfhi(p0[i]), v2 = bflo(p1[i]), v3 = bfhi(p1[i]); s += (v0 + v1) + (v2 + v3); q += (v0 * v0 + v1 * v1) + (v2 * v2 + v3 * v3); }
        s += xhalf(s); q += xhalf(q);
        __syncthreads();
        f32x2* sx = (f32x2*)lds;
        if (n == 1 && hh == 0) sx[m * 32 + r] = (f32x2){s, q};
        __syncthreads();
        if (n == 0 && hh == 0) { f32x2 o2 = sx[m * 32 + r]; ((f32x2*)(ws + ST + (size_t)xi_out * STSZ))[sl * 2048 + mt * 32 + r] = (f32x2){s + o2[0], q + o2[1]}; }
    }
}

__global__ void __launch_bounds__(256, 2) k_ffn1(char* ws, int xi, const u16* __restrict__ b1) {
    extern __shared__ __attribute__((aligned(16))) char lds[];
    const int b = blockIdx.x, tid = threadIdx.x, lane = tid & 63, w = __builtin_amdgcn_readfirstlane(tid >> 6);
    const int idx = b >> 3, p = (b & 7) * 2 + idx / 14, j = idx % 14;
    const int mt = p * 4 + w, r = lane & 31, hh = lane >> 5;
    const char* wsrc = ws + W1_T + (size_t)j * 112 * KB;
    for (int i = w; i < 112; i += 4)
        __builtin_amdgcn_global_load_lds((const unsigned*)(wsrc + i * KB + lane * 16), (__attribute__((address_space(3))) unsigned*)(lds + i * KB), 16, 0, 0);
    const char* xsrc = ws + X_T + (size_t)xi * XSZ + (size_t)mt * 28 * KB + lane * 16;
    bf16x8 xf[28];
#pragma unroll
    for (int s = 0; s < 28; ++s) xf[s] = *(const bf16x8*)(xsrc + s * KB);
    float mu; const float rs = rstd_of((const f32x2*)(ws + ST + (size_t)xi * STSZ), mt * 32 + r, mu);
    const float nm = -mu * rs;
    const float* cs = (const float*)(ws + CS) + (48 + j * 4) * 32;
    u32x2 bb[4][4];
#pragma unroll
    for (int n = 0; n < 4; ++n)
#pragma unroll
        for (int q = 0; q < 4; ++q) bb[n][q] = *(const u32x2*)(b1 + (j * 4 + n) * 32 + 8 * q + 4 * hh);
    __syncthreads();
#pragma unroll
    for (int n = 0; n < 4; ++n) {
        f32x16 a = {}; f32x4 c4[4];
#pragma unroll
        for (int q = 0; q < 4; ++q) c4[q] = *(const f32x4*)(cs + n * 32 + 8 * q + 4 * hh);
#pragma unroll
        for (int s = 0; s < 28; ++s) a = MFMA(*(const bf16x8*)(lds + (n * 28 + s) * KB + lane * 16), xf[s], a);
#pragma unroll
        for (int i = 0; i < 16; ++i) {
            const unsigned bw = bb[n][i >> 2][(i >> 1) & 1]; const float bias = (i & 1) ? bfhi(bw) : bflo(bw);
            const float hv = fmaf(rs, a[i], fmaf(nm, c4[i >> 2][i & 3], bias));
            const float e = __builtin_amdgcn_exp2f(hv * 1.4426950408889634f);
            const float u = e + 1.f; const float d = fmaf(u, u, 1.f);
            const float t = hv * __builtin_amdgcn_rcpf(d);
            a[i] = fmaf(t, -2.f, hv);
        }
        const size_t o = H_T + ((size_t)mt * 112 + (j * 4 + n) * 2) * KB + lane * 16;
        *(u32x4*)(ws + o) = pack8(a, 0); *(u32x4*)(ws + o + KB) = pack8(a, 1);
    }
}

extern "C" void kernel_launch(void* const* d_in, const int* in_sizes, int n_in, void* d_out, int out_size, void* d_ws, size_t ws_size, hipStream_t stream) {
    const u16* x = (const u16*)d_in[0]; const u16* wqkv = (const u16*)d_in[1]; const u16* wo = (const u16*)d_in[2];
    const u16* w1 = (const u16*)d_in[4]; const u16* b1 = (const u16*)d_in[5]; const u16* w2 = (const u16*)d_in[6];
    char* ws = (char*)d_ws; u16* out = (u16*)d_out;
    (void)hipFuncSetAttribute((const void*)k_qkv, hipFuncAttributeMaxDynamicSharedMemorySize, 86 * 1024);
    (void)hipFuncSetAttribute((const void*)k_attn, hipFuncAttributeMaxDynamicSharedMemorySize, 65 * 1024);
    (void)hipFuncSetAttribute((const void*)k_ffn1, hipFuncAttributeMaxDynamicSharedMemorySize, 112 * 1024);
    k_prep<<<238, 256, 0, stream>>>(x, wqkv, wo, w1, w2, ws);
    for (int l = 0; l < 2; ++l) {
        k_qkv<<<256, 256, 86 * 1024, stream>>>(ws, 2 * l);
        k_attn<<<256, 256, 65 * 1024, stream>>>(ws);
        k_res<32, false><<<224, 256, 64 * 1024, stream>>>(ws, O_T, WO_T, 2 * l, 2 * l + 1, out);
        k_ffn1<<<224, 256, 112 * 1024, stream>>>(ws, 2 * l + 1, b1);
        if (l == 0) k_res<112, false><<<224, 256, 64 * 1024, stream>>>(ws, H_T, W2_T, 1, 2, out);
        else k_res<112, true><<<224, 256, 64 * 1024, stream>>>(ws, H_T, W2_T, 3, 0, out);
    }
}
```

```cpp
#include <hip/hip_runtime.h>
#include <stdint.h>

__global__ void __launch_bounds__(256, 1) k_main(const void* x, const void* wqkv, const void* wo, const void* w1, const void* b1, const void* w2, void* out, void* ws) {
    asm volatile(
        "s_load_dwordx4 s[56:59], %0, 0x0\n\t"
        "s_load_dwordx2 s[16:17], %0, 0x38\n\t"
        "s_load_dwordx2 s[20:21], %0, 0x30\n\t"
        "s_mov_b32 s46, %1\n\t"
        "v_lshrrev_b32 v222, 6, %2\n\t"
        "v_and_b32 v223, 63, %2\n\t"
        "v_readfirstlane_b32 s26, v222\n\t"
        "v_lshlrev_b32 v247, 4, v223\n\t"
        "s_getpc_b64 s[48:49]\n\t"
        "kpc_%=:\n\t"
        "s_lshr_b32 s38, s46, 3\n\t"
        "s_lshl_b32 s39, s26, 5\n\t"
        "s_add_u32 s39, s39, s38\n\t"
        "s_lshl_b32 s39, s39, 10\n\t"
        "s_min_u32 s39, s39, kend_%=-kpc_%=-1024\n\t"
        "s_add_u32 s48, s48, s39\n\t"
        "s_addc_u32 s49, s49, 0\n\t"
        "s_and_b32 s48, s48, 0xfffffff0\n\t"
        "global_load_dwordx4 a[224:227], v247, s[48:49]\n\t"
        "s_and_b32 s24, s46, 7\n\t"
        "s_lshl_b32 s24, s24, 1\n\t"
        "s_and_b32 s39, s38, 1\n\t"
        "s_add_u32 s24, s24, s39\n\t"
        "s_lshr_b32 s25, s38, 1\n\t"
        "v_lshrrev_b32 v221, 2, v223\n\t"
        "v_mul_u32_u24 v221, 2688, v221\n\t"
        "v_lshrrev_b32 v220, 4, v223\n\t"
        "v_and_b32 v220, 2, v220\n\t"
        "v_and_b32 v219, 3, v223\n\t"
        "v_xor_b32 v220, v220, v219\n\t"
        "v_lshl_add_u32 v221, v220, 4, v221\n\t"
        "s_mul_i32 s40, s25, 56\n\t"
        "s_and_b32 s40, s40, 0xfffffff0\n\t"
        "s_mul_i32 s41, s26, 3\n\t"
        "s_add_u32 s47, s41, 0\n\t"
        "s_cmp_ge_u32 s47, 6\n\t"
        "s_cselect_b32 s33, 86016, 0\n\t"
        "s_cselect_b32 s34, 6, 0\n\t"
        "s_sub_u32 s47, s47, s34\n\t"
        "s_lshr_b32 s34, s47, 1\n\t"
        "s_mul_i32 s34, s34, 896\n\t"
        "s_add_u32 s33, s33, s34\n\t"
        "s_and_b32 s47, s47, 1\n\t"
        "s_mul_i32 s47, s47, 43008\n\t"
        "s_add_u32 s33, s33, s47\n\t"
        "s_add_u32 s33, s33, s40\n\t"
        "v_add_u32 v228, s33, v221\n\t"
        "s_add_u32 s47, s41, 1\n\t"
        "s_cmp_ge_u32 s47, 6\n\t"
        "s_cselect_b32 s33, 86016, 0\n\t"
        "s_cselect_b32 s34, 6, 0\n\t"
        "s_sub_u32 s47, s47, s34\n\t"
        "s_lshr_b32 s34, s47, 1\n\t"
        "s_mul_i32 s34, s34, 896\n\t"
        "s_add_u32 s33, s33, s34\n\t"
        "s_and_b32 s47, s47, 1\n\t"
        "s_mul_i32 s47, s47, 43008\n\t"
        "s_add_u32 s33, s33, s47\n\t"
        "s_add_u32 s33, s33, s40\n\t"
        "v_add_u32 v229, s33, v221\n\t"
        "s_add_u32 s47, s41, 2\n\t"
        "s_cmp_ge_u32 s47, 6\n\t"
        "s_cselect_b32 s33, 86016, 0\n\t"
        "s_cselect_b32 s34, 6, 0\n\t"
        "s_sub_u32 s47, s47, s34\n\t"
        "s_lshr_b32 s34, s47, 1\n\t"
        "s_mul_i32 s34, s34, 896\n\t"
        "s_add_u32 s33, s33, s34\n\t"
        "s_and_b32 s47, s47, 1\n\t"
        "s_mul_i32 s47, s47, 43008\n\t"
        "s_add_u32 s33, s33, s47\n\t"
        "s_add_u32 s33, s33, s40\n\t"
        "v_add_u32 v230, s33, v221\n\t"
        "s_lshl_b32 s47, s24, 3\n\t"
        "s_add_u32 s47, s47, s26\n\t"
        "s_mul_i32 s47, s47, 14336\n\t"
        "s_lshl_b32 s35, s41, 10\n\t"
        "s_mul_i32 s36, s26, 14336\n\t"
        "s_add_u32 s36, s36, 87040\n\t"
        "s_mov_b32 s18, 0x3832000\n\t"
        "s_mov_b32 s19, 0x20000\n\t"
        "s_waitcnt lgkmcnt(0)\n\t"
        "s_add_u32 s2, s56, s47\n\t"
        "s_addc_u32 s3, s57, 0\n\t"
        "s_mov_b64 s[62:63], s[58:59]\n\t"
        "s_mov_b64 s[8:9], s[56:57]\n\t"
        "v_mov_b32 v221, v223\n\t"
        "s_add_u32 m0, s35, 0\n\t"
        "s_nop 0\n\t"
        "global_load_lds_dwordx4 v228, s[62:63]\n\t"
        "s_add_u32 m0, s35, 1024\n\t"
        "s_nop 0\n\t"
        "global_load_lds_dwordx4 v229, s[62:63]\n\t"
        "s_add_u32 m0, s35, 2048\n\t"
        "s_nop 0\n\t"
        "global_load_lds_dwordx4 v230, s[62:63]\n\t"
        "v_add_u32 v220, 0, v221\n\t"
        "v_mul_u32_u24 v219, 9363, v220\n\t"
        "v_bfe_u32 v219, v219, 20, 3\n\t"
        "v_xor_b32 v220, v220, v219\n\t"
        "v_lshlrev_b32 v0, 4, v220\n\t"
        "s_add_u32 m0, s36, 0\n\t"
        "s_nop 0\n\t"
        "global_load_lds_dwordx4 v0, s[2:3]\n\t"
        "buffer_load_dwordx2 v[234:235], off, s[16:19], 0 offset:0 sc1\n\t"
        "v_add_u32 v220, 64, v221\n\t"
        "v_mul_u32_u24 v219, 9363, v220\n\t"
        "v_bfe_u32 v219, v219, 20, 3\n\t"
        "v_xor_b32 v220, v220, v219\n\t"
        "v_lshlrev_b32 v1, 4, v220\n\t"
        "s_add_u32 m0, s36, 1024\n\t"
        "s_nop 0\n\t"
        "global_load_lds_dwordx4 v1, s[2:3]\n\t"
        "v_add_u32 v220, 128, v221\n\t"
        "v_mul_u32_u24 v219, 9363, v220\n\t"
        "v_bfe_u32 v219, v219, 20, 3\n\t"
        "v_xor_b32 v220, v220, v219\n\t"
        "v_lshlrev_b32 v2, 4, v220\n\t"
        "s_add_u32 m0, s36, 2048\n\t"
        "s_nop 0\n\t"
        "global_load_lds_dwordx4 v2, s[2:3]\n\t"
        "v_add_u32 v220, 192, v221\n\t"
        "v_mul_u32_u24 v219, 9363, v220\n\t"
        "v_bfe_u32 v219, v219, 20, 3\n\t"
        "v_xor_b32 v220, v220, v219\n\t"
        "v_lshlrev_b32 v3, 4, v220\n\t"
        "s_add_u32 m0, s36, 3072\n\t"
        "s_nop 0\n\t"
        "global_load_lds_dwordx4 v3, s[2:3]\n\t"
        "v_add_u32 v220, 256, v221\n\t"
        "v_mul_u32_u24 v219, 9363, v220\n\t"
        "v_bfe_u32 v219, v219, 20, 3\n\t"
        "v_xor_b32 v220, v220, v219\n\t"
        "v_lshlrev_b32 v4, 4, v220\n\t"
        "s_add_u32 m0, s36, 4096\n\t"
        "s_nop 0\n\t"
        "global_load_lds_dwordx4 v4, s[2:3]\n\t"
        "s_mov_b32 s27, 0\n\t"
        "s_mov_b32 s28, 5447680\n\t"
        "s_mov_b32 s22, 1835008\n\t"
        "s_mov_b32 s23, 0x20000\n\t"
        "s_and_b32 s54, s26, 1\n\t"
        "s_cmp_eq_u32 s54, 1\n\t"
        "s_cselect_b64 s[52:53], -1, 0\n\t"
        "s_lshr_b32 s47, s26, 1\n\t"
        "s_lshl_b32 s42, s24, 2\n\t"
        "s_add_u32 s33, s42, s47\n\t"
        "s_cmp_ge_u32 s25, 7\n\t"
        "s_cselect_b32 s38, 7, 0\n\t"
        "s_cselect_b32 s39, 1, 0\n\t"
        "s_sub_u32 s43, s25, s38\n\t"
        "s_lshl_b32 s43, s43, 1\n\t"
        "s_add_u32 s43, s43, s47\n\t"
        "s_add_u32 s34, s42, s39\n\t"
        "s_lshl_b32 s41, s54, 10\n\t"
        "s_mul_i32 s29, s33, 28672\n\t"
        "v_add_u32 v220, 320, v221\n\t"
        "v_mul_u32_u24 v219, 9363, v220\n\t"
        "v_bfe_u32 v219, v219, 20, 3\n\t"
        "v_xor_b32 v220, v220, v219\n\t"
        "v_lshlrev_b32 v5, 4, v220\n\t"
        "s_add_u32 m0, s36, 5120\n\t"
        "s_nop 0\n\t"
        "global_load_lds_dwordx4 v5, s[2:3]\n\t"
        "s_mul_i32 s30, s33, 7168\n\t"
        "s_lshl_b32 s38, s25, 6\n\t"
        "s_add_u32 s38, s38, s33\n\t"
        "s_lshl_b32 s31, s38, 12\n\t"
        "s_lshl_b32 s37, s38, 11\n\t"
        "s_mul_i32 s59, s33, 112\n\t"
        "s_lshl_b32 s39, s25, 3\n\t"
        "s_add_u32 s59, s59, s39\n\t"
        "s_lshl_b32 s59, s59, 10\n\t"
        "s_lshl_b32 s38, s24, 6\n\t"
        "s_lshl_b32 s39, s26, 4\n\t"
        "s_add_u32 s39, s39, s38\n\t"
        "s_lshl_b32 s44, s39, 12\n\t"
        "s_lshl_b32 s39, s25, 2\n\t"
        "s_add_u32 s39, s39, s47\n\t"
        "s_add_u32 s38, s38, s39\n\t"
        "v_add_u32 v220, 384, v221\n\t"
        "v_mul_u32_u24 v219, 9363, v220\n\t"
        "v_bfe_u32 v219, v219, 20, 3\n\t"
        "v_xor_b32 v220, v220, v219\n\t"
        "v_lshlrev_b32 v6, 4, v220\n\t"
        "s_add_u32 m0, s36, 6144\n\t"
        "s_nop 0\n\t"
        "global_load_lds_dwordx4 v6, s[2:3]\n\t"
        "s_lshl_b32 s45, s38, 11\n\t"
        "s_lshl_b32 s39, s39, 4\n\t"
        "s_add_u32 s39, s39, s24\n\t"
        "s_lshl_b32 s56, s39, 11\n\t"
        "s_add_u32 s56, s56, s41\n\t"
        "s_lshl_b32 s38, s34, 2\n\t"
        "s_add_u32 s38, s38, s26\n\t"
        "s_lshl_b32 s60, s38, 13\n\t"
        "s_mul_i32 s61, s38, 28672\n\t"
        "s_mul_i32 s57, s34, 28672\n\t"
        "s_lshl_b32 s39, s43, 11\n\t"
        "s_add_u32 s57, s57, s39\n\t"
        "s_add_u32 s57, s57, s41\n\t"
        "s_mul_i32 s58, s34, 7168\n\t"
        "s_lshl_b32 s39, s43, 9\n\t"
        "s_add_u32 s58, s58, s39\n\t"
        "v_add_u32 v220, 448, v221\n\t"
        "v_mul_u32_u24 v219, 9363, v220\n\t"
        "v_bfe_u32 v219, v219, 20, 3\n\t"
        "v_xor_b32 v220, v220, v219\n\t"
        "v_lshlrev_b32 v7, 4, v220\n\t"
        "s_add_u32 m0, s36, 7168\n\t"
        "s_nop 0\n\t"
        "global_load_lds_dwordx4 v7, s[2:3]\n\t"
        "s_lshl_b32 s39, s54, 8\n\t"
        "s_add_u32 s58, s58, s39\n\t"
        "s_mul_i32 s15, s34, 28672\n\t"
        "s_lshl_b32 s39, s43, 6\n\t"
        "s_add_u32 s15, s15, s39\n\t"
        "s_lshl_b32 s39, s54, 5\n\t"
        "s_add_u32 s15, s15, s39\n\t"
        "s_xor_b32 s38, s43, 0\n\t"
        "s_lshl_b32 s38, s38, 2\n\t"
        "s_add_u32 s38, s38, s26\n\t"
        "s_lshl_b32 s96, s38, 13\n\t"
        "s_add_u32 s96, s96, 1744896\n\t"
        "s_mul_i32 s98, s38, 28672\n\t"
        "s_add_u32 s98, s98, 3809280\n\t"
        "s_xor_b32 s38, s43, 1\n\t"
        "s_lshl_b32 s38, s38, 2\n\t"
        "v_add_u32 v220, 512, v221\n\t"
        "v_mul_u32_u24 v219, 9363, v220\n\t"
        "v_bfe_u32 v219, v219, 20, 3\n\t"
        "v_xor_b32 v220, v220, v219\n\t"
        "v_lshlrev_b32 v8, 4, v220\n\t"
        "s_add_u32 m0, s36, 8192\n\t"
        "s_nop 0\n\t"
        "global_load_lds_dwordx4 v8, s[2:3]\n\t"
        "s_add_u32 s38, s38, s26\n\t"
        "s_lshl_b32 s97, s38, 13\n\t"
        "s_add_u32 s97, s97, 1744896\n\t"
        "s_mul_i32 s99, s38, 28672\n\t"
        "s_add_u32 s99, s99, 3809280\n\t"
        "s_lshl_b32 s14, s24, 7\n\t"
        "s_lshr_b32 s38, s46, 3\n\t"
        "s_lshl_b32 s38, s38, 2\n\t"
        "s_add_u32 s38, s38, s26\n\t"
        "s_mul_i32 s13, s38, 12544\n\t"
        "s_add_u32 s13, s13, 3809280\n\t"
        "s_lshl_b32 s12, s24, 11\n\t"
        "s_lshl_b32 s38, s25, 7\n\t"
        "s_add_u32 s12, s12, s38\n\t"
        "s_lshl_b32 s38, s54, 8\n\t"
        "v_xor_b32 v246, s38, v247\n\t"
        "v_add_u32 v220, 576, v221\n\t"
        "v_mul_u32_u24 v219, 9363, v220\n\t"
        "v_bfe_u32 v219, v219, 20, 3\n\t"
        "v_xor_b32 v220, v220, v219\n\t"
        "v_lshlrev_b32 v9, 4, v220\n\t"
        "s_add_u32 m0, s36, 9216\n\t"
        "s_nop 0\n\t"
        "global_load_lds_dwordx4 v9, s[2:3]\n\t"
        "v_lshrrev_b32 v222, 5, v223\n\t"
        "v_and_b32 v218, 31, v223\n\t"
        "v_lshrrev_b32 v217, 4, v223\n\t"
        "v_mul_u32_u24 v216, 1792, v217\n\t"
        "v_and_b32 v215, 15, v223\n\t"
        "v_lshl_add_u32 v245, v215, 3, v216\n\t"
        "s_lshl_b32 s39, s54, 7\n\t"
        "v_add_u32 v245, s39, v245\n\t"
        "v_lshlrev_b32 v216, 9, v217\n\t"
        "v_lshl_add_u32 v231, v215, 4, v216\n\t"
        "v_add_u32 v231, s38, v231\n\t"
        "v_lshlrev_b32 v244, 4, v222\n\t"
        "v_lshlrev_b32 v239, 2, v223\n\t"
        "v_and_b32 v217, 15, v223\n\t"
        "v_lshlrev_b32 v238, 3, v217\n\t"
        "v_min_u32 v237, 104, v238\n\t"
        "v_add_u32 v220, 640, v221\n\t"
        "v_mul_u32_u24 v219, 9363, v220\n\t"
        "v_bfe_u32 v219, v219, 20, 3\n\t"
        "v_xor_b32 v220, v220, v219\n\t"
        "v_lshlrev_b32 v10, 4, v220\n\t"
        "s_add_u32 m0, s36, 10240\n\t"
        "s_nop 0\n\t"
        "global_load_lds_dwordx4 v10, s[2:3]\n\t"
        "s_xor_b32 s38, s26, 1\n\t"
        "s_mul_i32 s38, s38, 6144\n\t"
        "s_add_u32 s38, s38, 114688\n\t"
        "v_add_u32 v243, s38, v247\n\t"
        "s_xor_b32 s38, s26, 2\n\t"
        "s_mul_i32 s38, s38, 6144\n\t"
        "s_add_u32 s38, s38, 116736\n\t"
        "v_add_u32 v242, s38, v247\n\t"
        "s_xor_b32 s38, s26, 3\n\t"
        "s_mul_i32 s38, s38, 6144\n\t"
        "s_add_u32 s38, s38, 118784\n\t"
        "v_add_u32 v241, s38, v247\n\t"
        "s_mul_i32 s38, s26, 6144\n\t"
        "s_add_u32 s38, s38, 114688\n\t"
        "v_add_u32 v240, s38, v247\n\t"
        "s_lshl_b32 s38, s54, 8\n\t"
        "v_add_u32 v220, 704, v221\n\t"
        "v_mul_u32_u24 v219, 9363, v220\n\t"
        "v_bfe_u32 v219, v219, 20, 3\n\t"
        "v_xor_b32 v220, v220, v219\n\t"
        "v_lshlrev_b32 v11, 4, v220\n\t"
        "s_add_u32 m0, s36, 11264\n\t"
        "s_nop 0\n\t"
        "global_load_lds_dwordx4 v11, s[2:3]\n\t"
        "v_subrev_u32 v225, s38, v231\n\t"
        "v_lshlrev_b32 v222, 7, v244\n\t"
        "v_add_u32 v222, v222, v225\n\t"
        "v_add_u32 v223, 43008, v222\n\t"
        "v_lshrrev_b32 v225, 2, v239\n\t"
        "v_and_b32 v225, 0x30, v225\n\t"
        "v_and_b32 v213, 0x3c, v239\n\t"
        "v_bfe_u32 v224, v225, 4, 1\n\t"
        "v_lshlrev_b32 v224, 9, v224\n\t"
        "v_lshl_add_u32 v224, v244, 4, v224\n\t"
        "v_lshl_add_u32 v224, v213, 2, v224\n\t"
        "s_mul_i32 s34, s25, 384\n\t"
        "s_add_u32 s34, s34, 5414912\n\t"
        "s_and_b32 s38, s25, 1\n\t"
        "s_lshl_b32 s38, s38, 3\n\t"
        "v_bfe_u32 v184, v239, 2, 2\n\t"
        "v_add_u32 v220, 768, v221\n\t"
        "v_mul_u32_u24 v219, 9363, v220\n\t"
        "v_bfe_u32 v219, v219, 20, 3\n\t"
        "v_xor_b32 v220, v220, v219\n\t"
        "v_lshlrev_b32 v12, 4, v220\n\t"
        "s_add_u32 m0, s36, 12288\n\t"
        "s_nop 0\n\t"
        "global_load_lds_dwordx4 v12, s[2:3]\n\t"
        "v_lshl_add_u32 v184, v184, 3, s38\n\t"
        "v_add_u32 v185, 32, v184\n\t"
        "v_bfe_u32 v186, v239, 6, 1\n\t"
        "v_lshlrev_b32 v186, 5, v186\n\t"
        "v_xor_b32 v184, v184, v186\n\t"
        "v_xor_b32 v185, v185, v186\n\t"
        "v_bfe_u32 v186, v239, 4, 2\n\t"
        "v_lshlrev_b32 v186, 6, v186\n\t"
        "v_lshl_add_u32 v186, v225, 5, v186\n\t"
        "v_add_u32 v184, v184, v186\n\t"
        "v_add_u32 v185, v185, v186\n\t"
        "v_add_u32 v186, 43008, v184\n\t"
        "v_add_u32 v187, 43008, v185\n\t"
        "v_lshrrev_b32 v190, 3, v213\n\t"
        "v_lshlrev_b32 v190, 4, v190\n\t"
        "v_xor_b32 v190, v190, v225\n\t"
        "v_add_u32 v220, 832, v221\n\t"
        "v_mul_u32_u24 v219, 9363, v220\n\t"
        "v_bfe_u32 v219, v219, 20, 3\n\t"
        "v_xor_b32 v220, v220, v219\n\t"
        "v_lshlrev_b32 v13, 4, v220\n\t"
        "s_add_u32 m0, s36, 13312\n\t"
        "s_nop 0\n\t"
        "global_load_lds_dwordx4 v13, s[2:3]\n\t"
        "v_mul_u32_u24 v191, 224, v213\n\t"
        "v_add_u32 v191, s36, v191\n\t"
        "v_xor_b32 v189, 64, v190\n\t"
        "v_add_u32 v190, v190, v191\n\t"
        "v_add_u32 v191, v189, v191\n\t"
        "s_add_u32 s62, s62, 172032\n\t"
        "s_addc_u32 s63, s63, 0\n\t"
        "s_add_u32 m0, s35, 12288\n\t"
        "s_nop 0\n\t"
        "global_load_lds_dwordx4 v228, s[62:63]\n\t"
        "s_add_u32 m0, s35, 13312\n\t"
        "s_nop 0\n\t"
        "global_load_lds_dwordx4 v229, s[62:63]\n\t"
        "s_add_u32 m0, s35, 14336\n\t"
        "s_nop 0\n\t"
        "global_load_lds_dwordx4 v230, s[62:63]\n\t"
        "s_cmp_lt_u32 s46, 64\n\t"
        "s_cbranch_scc1 pn_11_%=\n\t"
        "s_mov_b32 s71, 1024\n\t"
        "s_mov_b32 s72, 0\n\t"
        "s_mov_b32 s74, 0\n\t"
        "s_mov_b32 s78, 2048\n\t"
        "s_mov_b32 s79, 0\n\t"
        "s_mov_b64 s[76:77], 0xffff\n\t"
        "s_cmp_lt_u32 s46, 112\n\t"
        "s_cbranch_scc1 pq_6_%=\n\t"
        "s_cmp_lt_u32 s46, 168\n\t"
        "s_cbranch_scc1 p1_7_%=\n\t"
        "s_mov_b32 s79, 896\n\t"
        "s_mov_b64 s[76:77], 0xffffffff\n\t"
        "s_mov_b32 s69, 896\n\t"
        "s_cmp_lt_u32 s46, 224\n\t"
        "s_cbranch_scc1 p2_8_%=\n\t"
        "s_cmp_lt_u32 s46, 238\n\t"
        "s_cbranch_scc0 pn_11_%=\n\t"
        "s_sub_u32 s38, s46, 224\n\t"
        "s_lshl_b32 s68, s38, 6\n\t"
        "s_lshl_b32 s70, s38, 15\n\t"
        "s_add_u32 s70, s70, 1744896\n\t"
        "s_lshr_b32 s39, s38, 1\n\t"
        "s_lshl_b32 s73, s39, 7\n\t"
        "s_and_b32 s39, s38, 1\n\t"
        "s_lshl_b32 s39, s39, 3\n\t"
        "s_add_u32 s73, s73, s39\n\t"
        "s_add_u32 s73, s73, 335968\n\t"
        "s_mov_b32 s74, 2\n\t"
        "s_mov_b32 s66, 401408\n\t"
        "s_load_dwordx2 s[64:65], %0, 0x10\n\t"
        "s_branch pw_10_%=\n\t"
        "p2_8_%=:\n\t"
        "s_sub_u32 s38, s46, 168\n\t"
        "s_lshr_b32 s39, s38, 2\n\t"
        "s_and_b32 s40, s38, 3\n\t"
        "s_mul_i32 s68, s40, 401408\n\t"
        "s_lshl_b32 s41, s39, 6\n\t"
        "s_add_u32 s68, s68, s41\n\t"
        "s_mul_i32 s70, s38, 28672\n\t"
        "s_add_u32 s70, s70, 3809280\n\t"
        "s_lshr_b32 s73, s39, 1\n\t"
        "s_lshl_b32 s73, s73, 7\n\t"
        "s_and_b32 s38, s38, 7\n\t"
        "s_lshl_b32 s38, s38, 3\n\t"
        "s_add_u32 s73, s73, s38\n\t"
        "s_add_u32 s73, s73, 335904\n\t"
        "s_mov_b32 s66, 1605632\n\t"
        "s_waitcnt lgkmcnt(0)\n\t"
        "s_load_dwordx2 s[64:65], %0, 0x28\n\t"
        "s_branch pw_10_%=\n\t"
        "p1_7_%=:\n\t"
        "s_sub_u32 s38, s46, 112\n\t"
        "s_lshl_b32 s68, s38, 6\n\t"
        "s_mov_b32 s69, 3584\n\t"
        "s_mul_i32 s70, s38, 28672\n\t"
        "s_add_u32 s70, s70, 2203648\n\t"
        "s_lshl_b32 s72, s38, 8\n\t"
        "s_add_u32 s72, s72, 5423104\n\t"
        "s_lshr_b32 s73, s38, 2\n\t"
        "s_lshl_b32 s73, s73, 7\n\t"
        "s_and_b32 s39, s38, 3\n\t"
        "s_lshl_b32 s39, s39, 3\n\t"
        "s_add_u32 s73, s73, s39\n\t"
        "s_add_u32 s73, s73, 335872\n\t"
        "s_mov_b32 s66, 1605632\n\t"
        "s_load_dwordx2 s[80:81], %0, 0x20\n\t"
        "s_waitcnt lgkmcnt(0)\n\t"
        "s_load_dwordx2 s[64:65], %0, 0x18\n\t"
        "s_mov_b32 s75, s68\n\t"
        "s_branch pw_10_%=\n\t"
        "pq_6_%=:\n\t"
        "s_sub_u32 s38, s46, 64\n\t"
        "s_mul_i32 s39, s38, 171\n\t"
        "s_lshr_b32 s39, s39, 9\n\t"
        "s_mul_i32 s40, s39, 3\n\t"
        "s_sub_u32 s40, s38, s40\n\t"
        "s_mul_i32 s68, s40, 896\n\t"
        "s_mul_i32 s41, s39, 56\n\t"
        "s_and_b32 s74, s41, 8\n\t"
        "s_sub_u32 s41, s41, s74\n\t"
        "s_add_u32 s68, s68, s41\n\t"
        "s_or_b32 s74, s74, 1\n\t"
        "s_mov_b32 s69, 2688\n\t"
        "s_mul_i32 s70, s39, 86016\n\t"
        "s_lshl_b32 s41, s40, 10\n\t"
        "s_add_u32 s70, s70, s41\n\t"
        "s_add_u32 s70, s70, 368640\n\t"
        "s_mov_b32 s71, 3072\n\t"
        "s_lshl_b32 s72, s38, 7\n\t"
        "s_add_u32 s72, s72, 5414912\n\t"
        "s_lshl_b32 s73, s39, 7\n\t"
        "s_lshl_b32 s40, s40, 3\n\t"
        "s_add_u32 s73, s73, s40\n\t"
        "s_add_u32 s73, s73, 303136\n\t"
        "s_mov_b32 s66, 1204224\n\t"
        "s_waitcnt lgkmcnt(0)\n\t"
        "s_load_dwordx2 s[64:65], %0, 0x8\n\t"
        "pw_10_%=:\n\t"
        "s_mul_i32 s38, s26, 112\n\t"
        "s_mul_i32 s38, s38, s69\n\t"
        "s_add_u32 s68, s68, s38\n\t"
        "s_mov_b32 s67, 0x20000\n\t"
        "s_branch pi_12_%=\n\t"
        "pn_11_%=:\n\t"
        "s_mov_b32 s74, 4\n\t"
        "s_mov_b64 s[76:77], 0\n\t"
        "s_mov_b32 s73, 0\n\t"
        "s_mov_b32 s78, 0\n\t"
        "s_mov_b32 s79, 0\n\t"
        "pi_12_%=:\n\t"
        "s_waitcnt lgkmcnt(0)\n\t"
        "s_add_u32 s62, s62, 172032\n\t"
        "s_addc_u32 s63, s63, 0\n\t"
        "s_add_u32 m0, s35, 24576\n\t"
        "s_nop 0\n\t"
        "global_load_lds_dwordx4 v228, s[62:63]\n\t"
        "s_add_u32 m0, s35, 25600\n\t"
        "s_nop 0\n\t"
        "global_load_lds_dwordx4 v229, s[62:63]\n\t"
        "s_add_u32 m0, s35, 26624\n\t"
        "s_nop 0\n\t"
        "global_load_lds_dwordx4 v230, s[62:63]\n\t"
        "s_add_u32 s62, s62, 172032\n\t"
        "s_addc_u32 s63, s63, 0\n\t"
        "s_add_u32 m0, s35, 36864\n\t"
        "s_nop 0\n\t"
        "global_load_lds_dwordx4 v228, s[62:63]\n\t"
        "s_add_u32 m0, s35, 37888\n\t"
        "s_nop 0\n\t"
        "global_load_lds_dwordx4 v229, s[62:63]\n\t"
        "s_add_u32 m0, s35, 38912\n\t"
        "s_nop 0\n\t"
        "global_load_lds_dwordx4 v230, s[62:63]\n\t"
        "s_mov_b64 s[84:85], -1\n\t"
        "s_waitcnt vmcnt(9)\n\t"
        "ds_read_b128 v[56:59], v190 offset:0\n\t"
        "ds_read_b128 v[60:63], v191 offset:0\n\t"
        "ds_read_b128 v[64:67], v190 offset:128\n\t"
        "ds_read_b128 v[68:71], v191 offset:128\n\t"
        "ds_read_b128 v[72:75], v190 offset:256\n\t"
        "ds_read_b128 v[76:79], v191 offset:256\n\t"
        "ds_read_b128 v[80:83], v190 offset:384\n\t"
        "ds_read_b128 v[84:87], v191 offset:384\n\t"
        "ds_read_b128 v[88:91], v190 offset:512\n\t"
        "ds_read_b128 v[92:95], v191 offset:512\n\t"
        "ds_read_b128 v[96:99], v190 offset:640\n\t"
        "ds_read_b128 v[100:103], v191 offset:640\n\t"
        "ds_read_b128 v[104:107], v190 offset:768\n\t"
        "ds_read_b128 v[108:111], v191 offset:768\n\t"
        "v_mov_b32 v172, 0x3f803f80\n\t"
        "v_mov_b32 v173, 0x3f803f80\n\t"
        "v_mov_b32 v174, 0x3f803f80\n\t"
        "v_mov_b32 v175, 0x3f803f80\n\t"
        "s_barrier\n\t"
        "ds_read_b64_tr_b16 v[144:145], v184 offset:0\n\t"
        "s_waitcnt lgkmcnt(14)\n\t"
        "ds_read_b64_tr_b16 v[146:147], v184 offset:256\n\t"
        "s_waitcnt lgkmcnt(14)\n\t"
        "ds_read_b64_tr_b16 v[148:149], v185 offset:0\n\t"
        "s_waitcnt lgkmcnt(14)\n\t"
        "ds_read_b64_tr_b16 v[150:151], v185 offset:256\n\t"
        "s_waitcnt lgkmcnt(14)\n\t"
        "ds_read_b64_tr_b16 v[152:153], v184 offset:2048\n\t"
        "s_waitcnt lgkmcnt(14)\n\t"
        "ds_read_b64_tr_b16 v[154:155], v184 offset:2304\n\t"
        "s_waitcnt lgkmcnt(14)\n\t"
        "ds_read_b64_tr_b16 v[156:157], v185 offset:2048\n\t"
        "s_waitcnt lgkmcnt(14)\n\t"
        "ds_read_b64_tr_b16 v[158:159], v185 offset:2304\n\t"
        "s_waitcnt lgkmcnt(14)\n\t"
        "ds_read_b64_tr_b16 v[160:161], v184 offset:4096\n\t"
        "s_waitcnt lgkmcnt(14)\n\t"
        "ds_read_b64_tr_b16 v[162:163], v184 offset:4352\n\t"
        "s_waitcnt lgkmcnt(14)\n\t"
        "ds_read_b64_tr_b16 v[164:165], v185 offset:4096\n\t"
        "s_waitcnt lgkmcnt(14)\n\t"
        "ds_read_b64_tr_b16 v[166:167], v185 offset:4352\n\t"
        "s_waitcnt lgkmcnt(8)\n\t"
        "v_mfma_f32_16x16x32_bf16 v[112:115], v[144:147], v[56:59], 0\n\t"
        "v_mfma_f32_16x16x32_bf16 v[192:195], v[144:147], v[172:175], 0\n\t"
        "ds_read_b64_tr_b16 v[168:169], v184 offset:6144\n\t"
        "ds_read_b64_tr_b16 v[170:171], v184 offset:6400\n\t"
        "v_mfma_f32_16x16x32_bf16 v[116:119], v[148:151], v[56:59], 0\n\t"
        "v_mfma_f32_16x16x32_bf16 v[196:199], v[148:151], v[172:175], 0\n\t"
        "v_mfma_f32_16x16x32_bf16 v[176:179], v[172:175], v[56:59], 0\n\t"
        "ds_read_b64_tr_b16 v[144:145], v185 offset:6144\n\t"
        "ds_read_b64_tr_b16 v[146:147], v185 offset:6400\n\t"
        "s_add_u32 s62, s62, 172032\n\t"
        "s_addc_u32 s63, s63, 0\n\t"
        "s_add_u32 m0, s35, 49152\n\t"
        "s_nop 0\n\t"
        "global_load_lds_dwordx4 v228, s[62:63]\n\t"
        "s_add_u32 m0, s35, 50176\n\t"
        "s_nop 0\n\t"
        "global_load_lds_dwordx4 v229, s[62:63]\n\t"
        "s_add_u32 m0, s35, 51200\n\t"
        "s_nop 0\n\t"
        "global_load_lds_dwordx4 v230, s[62:63]\n\t"
        "s_waitcnt lgkmcnt(8)\n\t"
        "v_mfma_f32_16x16x32_bf16 v[120:123], v[152:155], v[56:59], 0\n\t"
        "v_mfma_f32_16x16x32_bf16 v[200:203], v[152:155], v[172:175], 0\n\t"
        "ds_read_b64_tr_b16 v[148:149], v184 offset:8192\n\t"
        "ds_read_b64_tr_b16 v[150:151], v184 offset:8448\n\t"
        "v_mfma_f32_16x16x32_bf16 v[124:127], v[156:159], v[56:59], 0\n\t"
        "v_mfma_f32_16x16x32_bf16 v[204:207], v[156:159], v[172:175], 0\n\t"
        "v_mfma_f32_16x16x32_bf16 v[180:183], v[56:59], v[56:59], 0\n\t"
        "ds_read_b64_tr_b16 v[152:153], v185 offset:8192\n\t"
        "ds_read_b64_tr_b16 v[154:155], v185 offset:8448\n\t"
        "s_waitcnt lgkmcnt(8)\n\t"
        "v_mfma_f32_16x16x32_bf16 v[128:131], v[56:59], v[160:163], 0\n\t"
        "v_mfma_f32_16x16x32_bf16 v[136:139], v[172:175], v[160:163], 0\n\t"
        "ds_read_b64_tr_b16 v[156:157], v184 offset:10240\n\t"
        "ds_read_b64_tr_b16 v[158:159], v184 offset:10496\n\t"
        "v_mfma_f32_16x16x32_bf16 v[132:135], v[56:59], v[164:167], 0\n\t"
        "v_mfma_f32_16x16x32_bf16 v[140:143], v[172:175], v[164:167], 0\n\t"
        "ds_read_b64_tr_b16 v[160:161], v185 offset:10240\n\t"
        "ds_read_b64_tr_b16 v[162:163], v185 offset:10496\n\t"
        "s_waitcnt lgkmcnt(8)\n\t"
        "v_mfma_f32_16x16x32_bf16 v[112:115], v[168:171], v[60:63], v[112:115]\n\t"
        "v_mfma_f32_16x16x32_bf16 v[192:195], v[168:171], v[172:175], v[192:195]\n\t"
        "s_waitcnt vmcnt(9)\n\t"
        "s_barrier\n\t"
        "ds_read_b64_tr_b16 v[164:165], v184 offset:12288\n\t"
        "ds_read_b64_tr_b16 v[166:167], v184 offset:12544\n\t"
        "v_mfma_f32_16x16x32_bf16 v[116:119], v[144:147], v[60:63], v[116:119]\n\t"
        "v_mfma_f32_16x16x32_bf16 v[196:199], v[144:147], v[172:175], v[196:199]\n\t"
        "v_mfma_f32_16x16x32_bf16 v[176:179], v[172:175], v[60:63], v[176:179]\n\t"
        "ds_read_b64_tr_b16 v[168:169], v185 offset:12288\n\t"
        "ds_read_b64_tr_b16 v[170:171], v185 offset:12544\n\t"
        "s_waitcnt lgkmcnt(8)\n\t"
        "v_mfma_f32_16x16x32_bf16 v[120:123], v[148:151], v[60:63], v[120:123]\n\t"
        "v_mfma_f32_16x16x32_bf16 v[200:203], v[148:151], v[172:175], v[200:203]\n\t"
        "ds_read_b64_tr_b16 v[144:145], v184 offset:14336\n\t"
        "ds_read_b64_tr_b16 v[146:147], v184 offset:14592\n\t"
        "v_mfma_f32_16x16x32_bf16 v[124:127], v[152:155], v[60:63], v[124:127]\n\t"
        "v_mfma_f32_16x16x32_bf16 v[204:207], v[152:155], v[172:175], v[204:207]\n\t"
        "v_mfma_f32_16x16x32_bf16 v[180:183], v[60:63], v[60:63], v[180:183]\n\t"
        "ds_read_b64_tr_b16 v[148:149], v185 offset:14336\n\t"
        "ds_read_b64_tr_b16 v[150:151], v185 offset:14592\n\t"
        "s_waitcnt lgkmcnt(8)\n\t"
        "v_mfma_f32_16x16x32_bf16 v[128:131], v[60:63], v[156:159], v[128:131]\n\t"
        "v_mfma_f32_16x16x32_bf16 v[136:139], v[172:175], v[156:159], v[136:139]\n\t"
        "ds_read_b64_tr_b16 v[152:153], v184 offset:16384\n\t"
        "ds_read_b64_tr_b16 v[154:155], v184 offset:16640\n\t"
        "v_mfma_f32_16x16x32_bf16 v[132:135], v[60:63], v[160:163], v[132:135]\n\t"
        "v_mfma_f32_16x16x32_bf16 v[140:143], v[172:175], v[160:163], v[140:143]\n\t"
        "ds_read_b64_tr_b16 v[156:157], v185 offset:16384\n\t"
        "ds_read_b64_tr_b16 v[158:159], v185 offset:16640\n\t"
        "s_waitcnt lgkmcnt(8)\n\t"
        "v_mfma_f32_16x16x32_bf16 v[112:115], v[164:167], v[64:67], v[112:115]\n\t"
        "v_mfma_f32_16x16x32_bf16 v[192:195], v[164:167], v[172:175], v[192:195]\n\t"
        "ds_read_b64_tr_b16 v[160:161], v184 offset:18432\n\t"
        "ds_read_b64_tr_b16 v[162:163], v184 offset:18688\n\t"
        "v_mfma_f32_16x16x32_bf16 v[116:119], v[168:171], v[64:67], v[116:119]\n\t"
        "v_mfma_f32_16x16x32_bf16 v[196:199], v[168:171], v[172:175], v[196:199]\n\t"
        "v_mfma_f32_16x16x32_bf16 v[176:179], v[172:175], v[64:67], v[176:179]\n\t"
        "ds_read_b64_tr_b16 v[164:165], v185 offset:18432\n\t"
        "ds_read_b64_tr_b16 v[166:167], v185 offset:18688\n\t"
        "s_add_u32 s62, s62, 172032\n\t"
        "s_addc_u32 s63, s63, 0\n\t"
        "s_add_u32 m0, s35, 61440\n\t"
        "s_nop 0\n\t"
        "global_load_lds_dwordx4 v228, s[62:63]\n\t"
        "s_add_u32 m0, s35, 62464\n\t"
        "s_nop 0\n\t"
        "global_load_lds_dwordx4 v229, s[62:63]\n\t"
        "s_add_u32 m0, s35, 63488\n\t"
        "s_nop 0\n\t"
        "global_load_lds_dwordx4 v230, s[62:63]\n\t"
        "s_waitcnt lgkmcnt(8)\n\t"
        "v_mfma_f32_16x16x32_bf16 v[120:123], v[144:147], v[64:67], v[120:123]\n\t"
        "v_mfma_f32_16x16x32_bf16 v[200:203], v[144:147], v[172:175], v[200:203]\n\t"
        "ds_read_b64_tr_b16 v[168:169], v184 offset:20480\n\t"
        "ds_read_b64_tr_b16 v[170:171], v184 offset:20736\n\t"
        "v_mfma_f32_16x16x32_bf16 v[124:127], v[148:151], v[64:67], v[124:127]\n\t"
        "v_mfma_f32_16x16x32_bf16 v[204:207], v[148:151], v[172:175], v[204:207]\n\t"
        "v_mfma_f32_16x16x32_bf16 v[180:183], v[64:67], v[64:67], v[180:183]\n\t"
        "ds_read_b64_tr_b16 v[144:145], v185 offset:20480\n\t"
        "ds_read_b64_tr_b16 v[146:147], v185 offset:20736\n\t"
        "s_waitcnt lgkmcnt(8)\n\t"
        "v_mfma_f32_16x16x32_bf16 v[128:131], v[64:67], v[152:155], v[128:131]\n\t"
        "v_mfma_f32_16x16x32_bf16 v[136:139], v[172:175], v[152:155], v[136:139]\n\t"
        "ds_read_b64_tr_b16 v[148:149], v184 offset:22528\n\t"
        "ds_read_b64_tr_b16 v[150:151], v184 offset:22784\n\t"
        "v_mfma_f32_16x16x32_bf16 v[132:135], v[64:67], v[156:159], v[132:135]\n\t"
        "v_mfma_f32_16x16x32_bf16 v[140:143], v[172:175], v[156:159], v[140:143]\n\t"
        "ds_read_b64_tr_b16 v[152:153], v185 offset:22528\n\t"
        "ds_read_b64_tr_b16 v[154:155], v185 offset:22784\n\t"
        "s_waitcnt lgkmcnt(8)\n\t"
        "v_mfma_f32_16x16x32_bf16 v[112:115], v[160:163], v[68:71], v[112:115]\n\t"
        "v_mfma_f32_16x16x32_bf16 v[192:195], v[160:163], v[172:175], v[192:195]\n\t"
        "s_waitcnt vmcnt(9)\n\t"
        "s_barrier\n\t"
        "ds_read_b64_tr_b16 v[156:157], v184 offset:24576\n\t"
        "ds_read_b64_tr_b16 v[158:159], v184 offset:24832\n\t"
        "v_mfma_f32_16x16x32_bf16 v[116:119], v[164:167], v[68:71], v[116:119]\n\t"
        "v_mfma_f32_16x16x32_bf16 v[196:199], v[164:167], v[172:175], v[196:199]\n\t"
        "v_mfma_f32_16x16x32_bf16 v[176:179], v[172:175], v[68:71], v[176:179]\n\t"
        "ds_read_b64_tr_b16 v[160:161], v185 offset:24576\n\t"
        "ds_read_b64_tr_b16 v[162:163], v185 offset:24832\n\t"
        "s_waitcnt lgkmcnt(8)\n\t"
        "v_mfma_f32_16x16x32_bf16 v[120:123], v[168:171], v[68:71], v[120:123]\n\t"
        "v_mfma_f32_16x16x32_bf16 v[200:203], v[168:171], v[172:175], v[200:203]\n\t"
        "ds_read_b64_tr_b16 v[164:165], v184 offset:26624\n\t"
        "ds_read_b64_tr_b16 v[166:167], v184 offset:26880\n\t"
        "v_mfma_f32_16x16x32_bf16 v[124:127], v[144:147], v[68:71], v[124:127]\n\t"
        "v_mfma_f32_16x16x32_bf16 v[204:207], v[144:147], v[172:175], v[204:207]\n\t"
        "v_mfma_f32_16x16x32_bf16 v[180:183], v[68:71], v[68:71], v[180:183]\n\t"
        "ds_read_b64_tr_b16 v[168:169], v185 offset:26624\n\t"
        "ds_read_b64_tr_b16 v[170:171], v185 offset:26880\n\t"
        "s_waitcnt lgkmcnt(8)\n\t"
        "v_mfma_f32_16x16x32_bf16 v[128:131], v[68:71], v[148:151], v[128:131]\n\t"
        "v_mfma_f32_16x16x32_bf16 v[136:139], v[172:175], v[148:151], v[136:139]\n\t"
        "ds_read_b64_tr_b16 v[144:145], v184 offset:28672\n\t"
        "ds_read_b64_tr_b16 v[146:147], v184 offset:28928\n\t"
        "v_mfma_f32_16x16x32_bf16 v[132:135], v[68:71], v[152:155], v[132:135]\n\t"
        "v_mfma_f32_16x16x32_bf16 v[140:143], v[172:175], v[152:155], v[140:143]\n\t"
        "ds_read_b64_tr_b16 v[148:149], v185 offset:28672\n\t"
        "ds_read_b64_tr_b16 v[150:151], v185 offset:28928\n\t"
        "s_waitcnt lgkmcnt(8)\n\t"
        "v_mfma_f32_16x16x32_bf16 v[112:115], v[156:159], v[72:75], v[112:115]\n\t"
        "v_mfma_f32_16x16x32_bf16 v[192:195], v[156:159], v[172:175], v[192:195]\n\t"
        "ds_read_b64_tr_b16 v[152:153], v184 offset:30720\n\t"
        "ds_read_b64_tr_b16 v[154:155], v184 offset:30976\n\t"
        "v_mfma_f32_16x16x32_bf16 v[116:119], v[160:163], v[72:75], v[116:119]\n\t"
        "v_mfma_f32_16x16x32_bf16 v[196:199], v[160:163], v[172:175], v[196:199]\n\t"
        "v_mfma_f32_16x16x32_bf16 v[176:179], v[172:175], v[72:75], v[176:179]\n\t"
        "ds_read_b64_tr_b16 v[156:157], v185 offset:30720\n\t"
        "ds_read_b64_tr_b16 v[158:159], v185 offset:30976\n\t"
        "s_add_u32 s62, s62, 172032\n\t"
        "s_addc_u32 s63, s63, 0\n\t"
        "s_add_u32 m0, s35, 73728\n\t"
        "s_nop 0\n\t"
        "global_load_lds_dwordx4 v228, s[62:63]\n\t"
        "s_add_u32 m0, s35, 74752\n\t"
        "s_nop 0\n\t"
        "global_load_lds_dwordx4 v229, s[62:63]\n\t"
        "s_add_u32 m0, s35, 75776\n\t"
        "s_nop 0\n\t"
        "global_load_lds_dwordx4 v230, s[62:63]\n\t"
        "s_waitcnt lgkmcnt(8)\n\t"
        "v_mfma_f32_16x16x32_bf16 v[120:123], v[164:167], v[72:75], v[120:123]\n\t"
        "v_mfma_f32_16x16x32_bf16 v[200:203], v[164:167], v[172:175], v[200:203]\n\t"
        "ds_read_b64_tr_b16 v[160:161], v184 offset:32768\n\t"
        "ds_read_b64_tr_b16 v[162:163], v184 offset:33024\n\t"
        "v_mfma_f32_16x16x32_bf16 v[124:127], v[168:171], v[72:75], v[124:127]\n\t"
        "v_mfma_f32_16x16x32_bf16 v[204:207], v[168:171], v[172:175], v[204:207]\n\t"
        "v_mfma_f32_16x16x32_bf16 v[180:183], v[72:75], v[72:75], v[180:183]\n\t"
        "ds_read_b64_tr_b16 v[164:165], v185 offset:32768\n\t"
        "ds_read_b64_tr_b16 v[166:167], v185 offset:33024\n\t"
        "s_waitcnt lgkmcnt(8)\n\t"
        "v_mfma_f32_16x16x32_bf16 v[128:131], v[72:75], v[144:147], v[128:131]\n\t"
        "v_mfma_f32_16x16x32_bf16 v[136:139], v[172:175], v[144:147], v[136:139]\n\t"
        "ds_read_b64_tr_b16 v[168:169], v184 offset:34816\n\t"
        "ds_read_b64_tr_b16 v[170:171], v184 offset:35072\n\t"
        "v_mfma_f32_16x16x32_bf16 v[132:135], v[72:75], v[148:151], v[132:135]\n\t"
        "v_mfma_f32_16x16x32_bf16 v[140:143], v[172:175], v[148:151], v[140:143]\n\t"
        "ds_read_b64_tr_b16 v[144:145], v185 offset:34816\n\t"
        "ds_read_b64_tr_b16 v[146:147], v185 offset:35072\n\t"
        "s_waitcnt lgkmcnt(8)\n\t"
        "v_mfma_f32_16x16x32_bf16 v[112:115], v[152:155], v[76:79], v[112:115]\n\t"
        "v_mfma_f32_16x16x32_bf16 v[192:195], v[152:155], v[172:175], v[192:195]\n\t"
        "s_waitcnt vmcnt(9)\n\t"
        "s_barrier\n\t"
        "ds_read_b64_tr_b16 v[148:149], v184 offset:36864\n\t"
        "ds_read_b64_tr_b16 v[150:151], v184 offset:37120\n\t"
        "v_mfma_f32_16x16x32_bf16 v[116:119], v[156:159], v[76:79], v[116:119]\n\t"
        "v_mfma_f32_16x16x32_bf16 v[196:199], v[156:159], v[172:175], v[196:199]\n\t"
        "v_mfma_f32_16x16x32_bf16 v[176:179], v[172:175], v[76:79], v[176:179]\n\t"
        "ds_read_b64_tr_b16 v[152:153], v185 offset:36864\n\t"
        "ds_read_b64_tr_b16 v[154:155], v185 offset:37120\n\t"
        "s_waitcnt lgkmcnt(8)\n\t"
        "v_mfma_f32_16x16x32_bf16 v[120:123], v[160:163], v[76:79], v[120:123]\n\t"
        "v_mfma_f32_16x16x32_bf16 v[200:203], v[160:163], v[172:175], v[200:203]\n\t"
        "ds_read_b64_tr_b16 v[156:157], v184 offset:38912\n\t"
        "ds_read_b64_tr_b16 v[158:159], v184 offset:39168\n\t"
        "v_mfma_f32_16x16x32_bf16 v[124:127], v[164:167], v[76:79], v[124:127]\n\t"
        "v_mfma_f32_16x16x32_bf16 v[204:207], v[164:167], v[172:175], v[204:207]\n\t"
        "v_mfma_f32_16x16x32_bf16 v[180:183], v[76:79], v[76:79], v[180:183]\n\t"
        "ds_read_b64_tr_b16 v[160:161], v185 offset:38912\n\t"
        "ds_read_b64_tr_b16 v[162:163], v185 offset:39168\n\t"
        "s_waitcnt lgkmcnt(8)\n\t"
        "v_mfma_f32_16x16x32_bf16 v[128:131], v[76:79], v[168:171], v[128:131]\n\t"
        "v_mfma_f32_16x16x32_bf16 v[136:139], v[172:175], v[168:171], v[136:139]\n\t"
        "ds_read_b64_tr_b16 v[164:165], v184 offset:40960\n\t"
        "ds_read_b64_tr_b16 v[166:167], v184 offset:41216\n\t"
        "v_mfma_f32_16x16x32_bf16 v[132:135], v[76:79], v[144:147], v[132:135]\n\t"
        "v_mfma_f32_16x16x32_bf16 v[140:143], v[172:175], v[144:147], v[140:143]\n\t"
        "ds_read_b64_tr_b16 v[168:169], v185 offset:40960\n\t"
        "ds_read_b64_tr_b16 v[170:171], v185 offset:41216\n\t"
        "s_waitcnt lgkmcnt(8)\n\t"
        "v_mfma_f32_16x16x32_bf16 v[112:115], v[148:151], v[80:83], v[112:115]\n\t"
        "v_mfma_f32_16x16x32_bf16 v[192:195], v[148:151], v[172:175], v[192:195]\n\t"
        "ds_read_b64_tr_b16 v[144:145], v186 offset:0\n\t"
        "ds_read_b64_tr_b16 v[146:147], v186 offset:256\n\t"
        "v_mfma_f32_16x16x32_bf16 v[116:119], v[152:155], v[80:83], v[116:119]\n\t"
        "v_mfma_f32_16x16x32_bf16 v[196:199], v[152:155], v[172:175], v[196:199]\n\t"
        "v_mfma_f32_16x16x32_bf16 v[176:179], v[172:175], v[80:83], v[176:179]\n\t"
        "ds_read_b64_tr_b16 v[148:149], v187 offset:0\n\t"
        "ds_read_b64_tr_b16 v[150:151], v187 offset:256\n\t"
        "s_waitcnt lgkmcnt(8)\n\t"
        "v_mfma_f32_16x16x32_bf16 v[120:123], v[156:159], v[80:83], v[120:123]\n\t"
        "v_mfma_f32_16x16x32_bf16 v[200:203], v[156:159], v[172:175], v[200:203]\n\t"
        "ds_read_b64_tr_b16 v[152:153], v186 offset:2048\n\t"
        "ds_read_b64_tr_b16 v[154:155], v186 offset:2304\n\t"
        "s_add_u32 s2, s2, 57344\n\t"
        "s_addc_u32 s3, s3, 0\n\t"
        "s_add_u32 m0, s36, 0\n\t"
        "s_nop 0\n\t"
        "global_load_lds_dwordx4 v0, s[2:3]\n\t"
        "v_mfma_f32_16x16x32_bf16 v[124:127], v[160:163], v[80:83], v[124:127]\n\t"
        "v_mfma_f32_16x16x32_bf16 v[204:207], v[160:163], v[172:175], v[204:207]\n\t"
        "v_mfma_f32_16x16x32_bf16 v[180:183], v[80:83], v[80:83], v[180:183]\n\t"
        "ds_read_b64_tr_b16 v[156:157], v187 offset:2048\n\t"
        "ds_read_b64_tr_b16 v[158:159], v187 offset:2304\n\t"
        "s_waitcnt lgkmcnt(8)\n\t"
        "v_mfma_f32_16x16x32_bf16 v[128:131], v[80:83], v[164:167], v[128:131]\n\t"
        "v_mfma_f32_16x16x32_bf16 v[136:139], v[172:175], v[164:167], v[136:139]\n\t"
        "ds_read_b64_tr_b16 v[160:161], v186 offset:4096\n\t"
        "ds_read_b64_tr_b16 v[162:163], v186 offset:4352\n\t"
        "s_add_u32 m0, s36, 1024\n\t"
        "s_nop 0\n\t"
        "global_load_lds_dwordx4 v1, s[2:3]\n\t"
        "v_mfma_f32_16x16x32_bf16 v[132:135], v[80:83], v[168:171], v[132:135]\n\t"
        "v_mfma_f32_16x16x32_bf16 v[140:143], v[172:175], v[168:171], v[140:143]\n\t"
        "ds_read_b64_tr_b16 v[164:165], v187 offset:4096\n\t"
        "ds_read_b64_tr_b16 v[166:167], v187 offset:4352\n\t"
        "s_waitcnt lgkmcnt(8)\n\t"
        "v_mfma_f32_16x16x32_bf16 v[112:115], v[144:147], v[84:87], v[112:115]\n\t"
        "v_mfma_f32_16x16x32_bf16 v[192:195], v[144:147], v[172:175], v[192:195]\n\t"
        "s_waitcnt vmcnt(8)\n\t"
        "s_barrier\n\t"
        "ds_read_b64_tr_b16 v[168:169], v186 offset:6144\n\t"
        "ds_read_b64_tr_b16 v[170:171], v186 offset:6400\n\t"
        "v_mfma_f32_16x16x32_bf16 v[116:119], v[148:151], v[84:87], v[116:119]\n\t"
        "v_mfma_f32_16x16x32_bf16 v[196:199], v[148:151], v[172:175], v[196:199]\n\t"
        "v_mfma_f32_16x16x32_bf16 v[176:179], v[172:175], v[84:87], v[176:179]\n\t"
        "ds_read_b64_tr_b16 v[144:145], v187 offset:6144\n\t"
        "ds_read_b64_tr_b16 v[146:147], v187 offset:6400\n\t"
        "s_waitcnt lgkmcnt(8)\n\t"
        "v_mfma_f32_16x16x32_bf16 v[120:123], v[152:155], v[84:87], v[120:123]\n\t"
        "v_mfma_f32_16x16x32_bf16 v[200:203], v[152:155], v[172:175], v[200:203]\n\t"
        "ds_read_b64_tr_b16 v[148:149], v186 offset:8192\n\t"
        "ds_read_b64_tr_b16 v[150:151], v186 offset:8448\n\t"
        "s_add_u32 m0, s36, 2048\n\t"
        "s_nop 0\n\t"
        "global_load_lds_dwordx4 v2, s[2:3]\n\t"
        "v_mfma_f32_16x16x32_bf16 v[124:127], v[156:159], v[84:87], v[124:127]\n\t"
        "v_mfma_f32_16x16x32_bf16 v[204:207], v[156:159], v[172:175], v[204:207]\n\t"
        "v_mfma_f32_16x16x32_bf16 v[180:183], v[84:87], v[84:87], v[180:183]\n\t"
        "ds_read_b64_tr_b16 v[152:153], v187 offset:8192\n\t"
        "ds_read_b64_tr_b16 v[154:155], v187 offset:8448\n\t"
        "s_waitcnt lgkmcnt(8)\n\t"
        "v_mfma_f32_16x16x32_bf16 v[128:131], v[84:87], v[160:163], v[128:131]\n\t"
        "v_mfma_f32_16x16x32_bf16 v[136:139], v[172:175], v[160:163], v[136:139]\n\t"
        "ds_read_b64_tr_b16 v[156:157], v186 offset:10240\n\t"
        "ds_read_b64_tr_b16 v[158:159], v186 offset:10496\n\t"
        "s_add_u32 m0, s36, 3072\n\t"
        "s_nop 0\n\t"
        "global_load_lds_dwordx4 v3, s[2:3]\n\t"
        "v_mfma_f32_16x16x32_bf16 v[132:135], v[84:87], v[164:167], v[132:135]\n\t"
        "v_mfma_f32_16x16x32_bf16 v[140:143], v[172:175], v[164:167], v[140:143]\n\t"
        "ds_read_b64_tr_b16 v[160:161], v187 offset:10240\n\t"
        "ds_read_b64_tr_b16 v[162:163], v187 offset:10496\n\t"
        "s_waitcnt lgkmcnt(8)\n\t"
        "v_mfma_f32_16x16x32_bf16 v[112:115], v[168:171], v[88:91], v[112:115]\n\t"
        "v_mfma_f32_16x16x32_bf16 v[192:195], v[168:171], v[172:175], v[192:195]\n\t"
        "ds_read_b64_tr_b16 v[164:165], v186 offset:12288\n\t"
        "ds_read_b64_tr_b16 v[166:167], v186 offset:12544\n\t"
        "v_mfma_f32_16x16x32_bf16 v[116:119], v[144:147], v[88:91], v[116:119]\n\t"
        "v_mfma_f32_16x16x32_bf16 v[196:199], v[144:147], v[172:175], v[196:199]\n\t"
        "v_mfma_f32_16x16x32_bf16 v[176:179], v[172:175], v[88:91], v[176:179]\n\t"
        "ds_read_b64_tr_b16 v[168:169], v187 offset:12288\n\t"
        "ds_read_b64_tr_b16 v[170:171], v187 offset:12544\n\t"
        "s_waitcnt lgkmcnt(8)\n\t"
        "v_mfma_f32_16x16x32_bf16 v[120:123], v[148:151], v[88:91], v[120:123]\n\t"
        "v_mfma_f32_16x16x32_bf16 v[200:203], v[148:151], v[172:175], v[200:203]\n\t"
        "ds_read_b64_tr_b16 v[144:145], v186 offset:14336\n\t"
        "ds_read_b64_tr_b16 v[146:147], v186 offset:14592\n\t"
        "s_add_u32 m0, s36, 4096\n\t"
        "s_nop 0\n\t"
        "global_load_lds_dwordx4 v4, s[2:3]\n\t"
        "v_mfma_f32_16x16x32_bf16 v[124:127], v[152:155], v[88:91], v[124:127]\n\t"
        "v_mfma_f32_16x16x32_bf16 v[204:207], v[152:155], v[172:175], v[204:207]\n\t"
        "v_mfma_f32_16x16x32_bf16 v[180:183], v[88:91], v[88:91], v[180:183]\n\t"
        "ds_read_b64_tr_b16 v[148:149], v187 offset:14336\n\t"
        "ds_read_b64_tr_b16 v[150:151], v187 offset:14592\n\t"
        "s_waitcnt lgkmcnt(8)\n\t"
        "v_mfma_f32_16x16x32_bf16 v[128:131], v[88:91], v[156:159], v[128:131]\n\t"
        "v_mfma_f32_16x16x32_bf16 v[136:139], v[172:175], v[156:159], v[136:139]\n\t"
        "ds_read_b64_tr_b16 v[152:153], v186 offset:16384\n\t"
        "ds_read_b64_tr_b16 v[154:155], v186 offset:16640\n\t"
        "s_add_u32 m0, s36, 5120\n\t"
        "s_nop 0\n\t"
        "global_load_lds_dwordx4 v5, s[2:3]\n\t"
        "v_mfma_f32_16x16x32_bf16 v[132:135], v[88:91], v[160:163], v[132:135]\n\t"
        "v_mfma_f32_16x16x32_bf16 v[140:143], v[172:175], v[160:163], v[140:143]\n\t"
        "ds_read_b64_tr_b16 v[156:157], v187 offset:16384\n\t"
        "ds_read_b64_tr_b16 v[158:159], v187 offset:16640\n\t"
        "s_waitcnt lgkmcnt(8)\n\t"
        "v_mfma_f32_16x16x32_bf16 v[112:115], v[164:167], v[92:95], v[112:115]\n\t"
        "v_mfma_f32_16x16x32_bf16 v[192:195], v[164:167], v[172:175], v[192:195]\n\t"
        "s_waitcnt vmcnt(9)\n\t"
        "s_barrier\n\t"
        "ds_read_b64_tr_b16 v[160:161], v186 offset:18432\n\t"
        "ds_read_b64_tr_b16 v[162:163], v186 offset:18688\n\t"
        "v_mfma_f32_16x16x32_bf16 v[116:119], v[168:171], v[92:95], v[116:119]\n\t"
        "v_mfma_f32_16x16x32_bf16 v[196:199], v[168:171], v[172:175], v[196:199]\n\t"
        "v_mfma_f32_16x16x32_bf16 v[176:179], v[172:175], v[92:95], v[176:179]\n\t"
        "ds_read_b64_tr_b16 v[164:165], v187 offset:18432\n\t"
        "ds_read_b64_tr_b16 v[166:167], v187 offset:18688\n\t"
        "s_waitcnt lgkmcnt(8)\n\t"
        "v_mfma_f32_16x16x32_bf16 v[120:123], v[144:147], v[92:95], v[120:123]\n\t"
        "v_mfma_f32_16x16x32_bf16 v[200:203], v[144:147], v[172:175], v[200:203]\n\t"
        "ds_read_b64_tr_b16 v[168:169], v186 offset:20480\n\t"
        "ds_read_b64_tr_b16 v[170:171], v186 offset:20736\n\t"
        "s_add_u32 m0, s36, 6144\n\t"
        "s_nop 0\n\t"
        "global_load_lds_dwordx4 v6, s[2:3]\n\t"
        "v_mfma_f32_16x16x32_bf16 v[124:127], v[148:151], v[92:95], v[124:127]\n\t"
        "v_mfma_f32_16x16x32_bf16 v[204:207], v[148:151], v[172:175], v[204:207]\n\t"
        "v_mfma_f32_16x16x32_bf16 v[180:183], v[92:95], v[92:95], v[180:183]\n\t"
        "ds_read_b64_tr_b16 v[144:145], v187 offset:20480\n\t"
        "ds_read_b64_tr_b16 v[146:147], v187 offset:20736\n\t"
        "s_waitcnt lgkmcnt(8)\n\t"
        "v_mfma_f32_16x16x32_bf16 v[128:131], v[92:95], v[152:155], v[128:131]\n\t"
        "v_mfma_f32_16x16x32_bf16 v[136:139], v[172:175], v[152:155], v[136:139]\n\t"
        "ds_read_b64_tr_b16 v[148:149], v186 offset:22528\n\t"
        "ds_read_b64_tr_b16 v[150:151], v186 offset:22784\n\t"
        "s_add_u32 m0, s36, 7168\n\t"
        "s_nop 0\n\t"
        "global_load_lds_dwordx4 v7, s[2:3]\n\t"
        "v_mfma_f32_16x16x32_bf16 v[132:135], v[92:95], v[156:159], v[132:135]\n\t"
        "v_mfma_f32_16x16x32_bf16 v[140:143], v[172:175], v[156:159], v[140:143]\n\t"
        "ds_read_b64_tr_b16 v[152:153], v187 offset:22528\n\t"
        "ds_read_b64_tr_b16 v[154:155], v187 offset:22784\n\t"
        "s_waitcnt lgkmcnt(8)\n\t"
        "v_mfma_f32_16x16x32_bf16 v[112:115], v[160:163], v[96:99], v[112:115]\n\t"
        "v_mfma_f32_16x16x32_bf16 v[192:195], v[160:163], v[172:175], v[192:195]\n\t"
        "ds_read_b64_tr_b16 v[156:157], v186 offset:24576\n\t"
        "ds_read_b64_tr_b16 v[158:159], v186 offset:24832\n\t"
        "v_mfma_f32_16x16x32_bf16 v[116:119], v[164:167], v[96:99], v[116:119]\n\t"
        "v_mfma_f32_16x16x32_bf16 v[196:199], v[164:167], v[172:175], v[196:199]\n\t"
        "v_mfma_f32_16x16x32_bf16 v[176:179], v[172:175], v[96:99], v[176:179]\n\t"
        "ds_read_b64_tr_b16 v[160:161], v187 offset:24576\n\t"
        "ds_read_b64_tr_b16 v[162:163], v187 offset:24832\n\t"
        "s_waitcnt lgkmcnt(8)\n\t"
        "v_mfma_f32_16x16x32_bf16 v[120:123], v[168:171], v[96:99], v[120:123]\n\t"
        "v_mfma_f32_16x16x32_bf16 v[200:203], v[168:171], v[172:175], v[200:203]\n\t"
        "ds_read_b64_tr_b16 v[164:165], v186 offset:26624\n\t"
        "ds_read_b64_tr_b16 v[166:167], v186 offset:26880\n\t"
        "s_add_u32 m0, s36, 8192\n\t"
        "s_nop 0\n\t"
        "global_load_lds_dwordx4 v8, s[2:3]\n\t"
        "v_mfma_f32_16x16x32_bf16 v[124:127], v[144:147], v[96:99], v[124:127]\n\t"
        "v_mfma_f32_16x16x32_bf16 v[204:207], v[144:147], v[172:175], v[204:207]\n\t"
        "v_mfma_f32_16x16x32_bf16 v[180:183], v[96:99], v[96:99], v[180:183]\n\t"
        "ds_read_b64_tr_b16 v[168:169], v187 offset:26624\n\t"
        "ds_read_b64_tr_b16 v[170:171], v187 offset:26880\n\t"
        "s_waitcnt lgkmcnt(8)\n\t"
        "v_mfma_f32_16x16x32_bf16 v[128:131], v[96:99], v[148:151], v[128:131]\n\t"
        "v_mfma_f32_16x16x32_bf16 v[136:139], v[172:175], v[148:151], v[136:139]\n\t"
        "ds_read_b64_tr_b16 v[144:145], v186 offset:28672\n\t"
        "ds_read_b64_tr_b16 v[146:147], v186 offset:28928\n\t"
        "s_add_u32 m0, s36, 9216\n\t"
        "s_nop 0\n\t"
        "global_load_lds_dwordx4 v9, s[2:3]\n\t"
        "v_mfma_f32_16x16x32_bf16 v[132:135], v[96:99], v[152:155], v[132:135]\n\t"
        "v_mfma_f32_16x16x32_bf16 v[140:143], v[172:175], v[152:155], v[140:143]\n\t"
        "ds_read_b64_tr_b16 v[148:149], v187 offset:28672\n\t"
        "ds_read_b64_tr_b16 v[150:151], v187 offset:28928\n\t"
        "s_waitcnt lgkmcnt(8)\n\t"
        "v_mfma_f32_16x16x32_bf16 v[112:115], v[156:159], v[100:103], v[112:115]\n\t"
        "v_mfma_f32_16x16x32_bf16 v[192:195], v[156:159], v[172:175], v[192:195]\n\t"
        "s_waitcnt vmcnt(10)\n\t"
        "s_barrier\n\t"
        "ds_read_b64_tr_b16 v[152:153], v186 offset:30720\n\t"
        "ds_read_b64_tr_b16 v[154:155], v186 offset:30976\n\t"
        "v_mfma_f32_16x16x32_bf16 v[116:119], v[160:163], v[100:103], v[116:119]\n\t"
        "v_mfma_f32_16x16x32_bf16 v[196:199], v[160:163], v[172:175], v[196:199]\n\t"
        "v_mfma_f32_16x16x32_bf16 v[176:179], v[172:175], v[100:103], v[176:179]\n\t"
        "ds_read_b64_tr_b16 v[156:157], v187 offset:30720\n\t"
        "ds_read_b64_tr_b16 v[158:159], v187 offset:30976\n\t"
        "s_waitcnt lgkmcnt(8)\n\t"
        "v_mfma_f32_16x16x32_bf16 v[120:123], v[164:167], v[100:103], v[120:123]\n\t"
        "v_mfma_f32_16x16x32_bf16 v[200:203], v[164:167], v[172:175], v[200:203]\n\t"
        "ds_read_b64_tr_b16 v[160:161], v186 offset:32768\n\t"
        "ds_read_b64_tr_b16 v[162:163], v186 offset:33024\n\t"
        "s_add_u32 m0, s36, 10240\n\t"
        "s_nop 0\n\t"
        "global_load_lds_dwordx4 v10, s[2:3]\n\t"
        "v_mfma_f32_16x16x32_bf16 v[124:127], v[168:171], v[100:103], v[124:127]\n\t"
        "v_mfma_f32_16x16x32_bf16 v[204:207], v[168:171], v[172:175], v[204:207]\n\t"
        "v_mfma_f32_16x16x32_bf16 v[180:183], v[100:103], v[100:103], v[180:183]\n\t"
        "ds_read_b64_tr_b16 v[164:165], v187 offset:32768\n\t"
        "ds_read_b64_tr_b16 v[166:167], v187 offset:33024\n\t"
        "s_waitcnt lgkmcnt(8)\n\t"
        "v_mfma_f32_16x16x32_bf16 v[128:131], v[100:103], v[144:147], v[128:131]\n\t"
        "v_mfma_f32_16x16x32_bf16 v[136:139], v[172:175], v[144:147], v[136:139]\n\t"
        "ds_read_b64_tr_b16 v[168:169], v186 offset:34816\n\t"
        "ds_read_b64_tr_b16 v[170:171], v186 offset:35072\n\t"
        "s_add_u32 m0, s36, 11264\n\t"
        "s_nop 0\n\t"
        "global_load_lds_dwordx4 v11, s[2:3]\n\t"
        "v_mfma_f32_16x16x32_bf16 v[132:135], v[100:103], v[148:151], v[132:135]\n\t"
        "v_mfma_f32_16x16x32_bf16 v[140:143], v[172:175], v[148:151], v[140:143]\n\t"
        "ds_read_b64_tr_b16 v[144:145], v187 offset:34816\n\t"
        "ds_read_b64_tr_b16 v[146:147], v187 offset:35072\n\t"
        "s_waitcnt lgkmcnt(8)\n\t"
        "v_mfma_f32_16x16x32_bf16 v[112:115], v[152:155], v[104:107], v[112:115]\n\t"
        "v_mfma_f32_16x16x32_bf16 v[192:195], v[152:155], v[172:175], v[192:195]\n\t"
        "ds_read_b64_tr_b16 v[148:149], v186 offset:36864\n\t"
        "ds_read_b64_tr_b16 v[150:151], v186 offset:37120\n\t"
        "v_mfma_f32_16x16x32_bf16 v[116:119], v[156:159], v[104:107], v[116:119]\n\t"
        "v_mfma_f32_16x16x32_bf16 v[196:199], v[156:159], v[172:175], v[196:199]\n\t"
        "v_mfma_f32_16x16x32_bf16 v[176:179], v[172:175], v[104:107], v[176:179]\n\t"
        "ds_read_b64_tr_b16 v[152:153], v187 offset:36864\n\t"
        "ds_read_b64_tr_b16 v[154:155], v187 offset:37120\n\t"
        "s_waitcnt lgkmcnt(8)\n\t"
        "v_mfma_f32_16x16x32_bf16 v[120:123], v[160:163], v[104:107], v[120:123]\n\t"
        "v_mfma_f32_16x16x32_bf16 v[200:203], v[160:163], v[172:175], v[200:203]\n\t"
        "ds_read_b64_tr_b16 v[156:157], v186 offset:38912\n\t"
        "ds_read_b64_tr_b16 v[158:159], v186 offset:39168\n\t"
        "s_add_u32 m0, s36, 12288\n\t"
        "s_nop 0\n\t"
        "global_load_lds_dwordx4 v12, s[2:3]\n\t"
        "v_mfma_f32_16x16x32_bf16 v[124:127], v[164:167], v[104:107], v[124:127]\n\t"
        "v_mfma_f32_16x16x32_bf16 v[204:207], v[164:167], v[172:175], v[204:207]\n\t"
        "v_mfma_f32_16x16x32_bf16 v[180:183], v[104:107], v[104:107], v[180:183]\n\t"
        "ds_read_b64_tr_b16 v[160:161], v187 offset:38912\n\t"
        "ds_read_b64_tr_b16 v[162:163], v187 offset:39168\n\t"
        "s_waitcnt lgkmcnt(8)\n\t"
        "v_mfma_f32_16x16x32_bf16 v[128:131], v[104:107], v[168:171], v[128:131]\n\t"
        "v_mfma_f32_16x16x32_bf16 v[136:139], v[172:175], v[168:171], v[136:139]\n\t"
        "ds_read_b64_tr_b16 v[164:165], v186 offset:40960\n\t"
        "ds_read_b64_tr_b16 v[166:167], v186 offset:41216\n\t"
        "s_add_u32 m0, s36, 13312\n\t"
        "s_nop 0\n\t"
        "global_load_lds_dwordx4 v13, s[2:3]\n\t"
        "v_mfma_f32_16x16x32_bf16 v[132:135], v[104:107], v[144:147], v[132:135]\n\t"
        "v_mfma_f32_16x16x32_bf16 v[140:143], v[172:175], v[144:147], v[140:143]\n\t"
        "ds_read_b64_tr_b16 v[168:169], v187 offset:40960\n\t"
        "ds_read_b64_tr_b16 v[170:171], v187 offset:41216\n\t"
        "s_waitcnt lgkmcnt(8)\n\t"
        "v_mfma_f32_16x16x32_bf16 v[112:115], v[148:151], v[108:111], v[112:115]\n\t"
        "v_mfma_f32_16x16x32_bf16 v[192:195], v[148:151], v[172:175], v[192:195]\n\t"
        "v_mfma_f32_16x16x32_bf16 v[116:119], v[152:155], v[108:111], v[116:119]\n\t"
        "v_mfma_f32_16x16x32_bf16 v[196:199], v[152:155], v[172:175], v[196:199]\n\t"
        "v_mfma_f32_16x16x32_bf16 v[176:179], v[172:175], v[108:111], v[176:179]\n\t"
        "s_waitcnt lgkmcnt(4)\n\t"
        "v_mfma_f32_16x16x32_bf16 v[120:123], v[156:159], v[108:111], v[120:123]\n\t"
        "v_mfma_f32_16x16x32_bf16 v[200:203], v[156:159], v[172:175], v[200:203]\n\t"
        "v_mfma_f32_16x16x32_bf16 v[124:127], v[160:163], v[108:111], v[124:127]\n\t"
        "v_mfma_f32_16x16x32_bf16 v[204:207], v[160:163], v[172:175], v[204:207]\n\t"
        "v_mfma_f32_16x16x32_bf16 v[180:183], v[108:111], v[108:111], v[180:183]\n\t"
        "s_waitcnt lgkmcnt(0)\n\t"
        "v_mfma_f32_16x16x32_bf16 v[128:131], v[108:111], v[164:167], v[128:131]\n\t"
        "v_mfma_f32_16x16x32_bf16 v[136:139], v[172:175], v[164:167], v[136:139]\n\t"
        "v_mfma_f32_16x16x32_bf16 v[132:135], v[108:111], v[168:171], v[132:135]\n\t"
        "v_mfma_f32_16x16x32_bf16 v[140:143], v[172:175], v[168:171], v[140:143]\n\t"
        "v_bfe_u32 v188, v239, 2, 2\n\t"
        "v_cmp_eq_u32 vcc, 1, v188\n\t"
        "s_lshl_b32 s38, s26, 7\n\t"
        "s_add_u32 s38, s38, 86016\n\t"
        "v_cndmask_b32 v180, v180, v181, vcc\n\t"
        "v_cmp_eq_u32 vcc, 2, v188\n\t"
        "v_add_u32 v226, s38, v213\n\t"
        "v_add_u32 v227, s38, v225\n\t"
        "v_cndmask_b32 v180, v180, v182, vcc\n\t"
        "v_cmp_eq_u32 vcc, 3, v188\n\t"
        "v_and_b32 v189, 0x30, v213\n\t"
        "v_mul_f32 v176, 0x3e800000, v176\n\t"
        "v_cndmask_b32 v180, v180, v183, vcc\n\t"
        "v_cmp_eq_u32 vcc, v189, v225\n\t"
        "v_mov_b32 v208, v136\n\t"
        "v_mov_b32 v210, v140\n\t"
        "v_cndmask_b32 v177, 0, v180, vcc\n\t"
        "v_mov_b32 v178, v176\n\t"
        "v_mov_b32 v179, v177\n\t"
        "s_nop 0\n\t"
        "v_permlane32_swap_b32 v176, v178\n\t"
        "v_permlane32_swap_b32 v177, v179\n\t"
        "v_add_f32 v176, v176, v178\n\t"
        "v_add_f32 v177, v177, v179\n\t"
        "v_mov_b32 v178, v176\n\t"
        "v_mov_b32 v179, v177\n\t"
        "s_nop 0\n\t"
        "v_permlane16_swap_b32 v176, v178\n\t"
        "v_permlane16_swap_b32 v177, v179\n\t"
        "v_add_f32 v176, v176, v178\n\t"
        "v_add_f32 v177, v177, v179\n\t"
        "v_mul_f32 v176, 0x3b124925, v176\n\t"
        "v_mul_f32 v177, 0x3b124925, v177\n\t"
        "v_fma_f32 v177, -v176, v176, v177\n\t"
        "v_max_f32 v177, 0, v177\n\t"
        "v_add_f32 v177, 0x3727c5ac, v177\n\t"
        "v_rsq_f32 v214, v177\n\t"
        "s_nop 0\n\t"
        "v_mul_f32 v215, -v176, v214\n\t"
        "ds_write2_b32 v226, v214, v215 offset0:0 offset1:16\n\t"
        "ds_read_b128 v[56:59], v227\n\t"
        "ds_read_b128 v[60:63], v227 offset:64\n\t"
        "s_add_u32 s47, s31, s28\n\t"
        "s_add_u32 s47, s47, 4390912\n\t"
        "s_add_u32 s33, s37, s28\n\t"
        "s_add_u32 s33, s33, 2293760\n\t"
        "v_cmp_eq_u32 vcc, 48, v225\n\t"
        "v_mov_b32 v219, 0\n\t"
        "v_mul_f32 v216, 0x3e8b97f6, v214\n\t"
        "v_cndmask_b32 v218, 0, 1.0, vcc\n\t"
        "v_cmp_eq_u32 vcc, 48, v213\n\t"
        "v_mul_f32 v217, 0x3e8b97f6, v215\n\t"
        "s_lshl_b32 s38, s54, 10\n\t"
        "v_cndmask_b32 v220, 0, 1.0, vcc\n\t"
        "s_add_u32 s38, s38, s47\n\t"
        "v_pk_mul_f32 v[64:65], v[200:201], v[214:215] op_sel:[0,1] op_sel_hi:[1,1]\n\t"
        "v_pk_mul_f32 v[66:67], v[202:203], v[214:215] op_sel:[0,1] op_sel_hi:[1,1]\n\t"
        "v_pk_fma_f32 v[68:69], v[204:205], v[214:215], v[218:219] op_sel:[0,1,0] op_sel_hi:[1,1,0]\n\t"
        "v_pk_fma_f32 v[70:71], v[206:207], v[214:215], v[218:219] op_sel:[0,1,0] op_sel_hi:[1,1,1]\n\t"
        "v_pk_fma_f32 v[120:121], v[120:121], v[214:215], v[64:65] op_sel_hi:[1,0,1]\n\t"
        "v_pk_fma_f32 v[122:123], v[122:123], v[214:215], v[66:67] op_sel_hi:[1,0,1]\n\t"
        "v_pk_fma_f32 v[124:125], v[124:125], v[214:215], v[68:69] op_sel_hi:[1,0,1]\n\t"
        "v_pk_fma_f32 v[126:127], v[126:127], v[214:215], v[70:71] op_sel_hi:[1,0,1]\n\t"
        "v_cvt_pk_bf16_f32 v136, v120, v121\n\t"
        "v_cvt_pk_bf16_f32 v137, v122, v123\n\t"
        "v_cvt_pk_bf16_f32 v138, v124, v125\n\t"
        "v_cvt_pk_bf16_f32 v139, v126, v127\n\t"
        "v_cmp_eq_u32 s[48:49], 48, v225\n\t"
        "v_mov_b32 v188, 0x3f803f80\n\t"
        "v_mov_b32 v189, 0x3f80\n\t"
        "v_mov_b32 v183, 0\n\t"
        "v_cndmask_b32 v138, v138, v188, s[48:49]\n\t"
        "v_cndmask_b32 v139, v139, v189, s[48:49]\n\t"
        "buffer_store_dwordx4 v[136:139], v231, s[16:19], s47 offen sc1\n\t"
        "s_waitcnt lgkmcnt(0)\n\t"
        "v_pk_mul_f32 v[72:73], v[60:61], v[208:209] op_sel_hi:[1,0]\n\t"
        "v_pk_fma_f32 v[76:77], v[60:61], v[210:211], v[220:221] op_sel_hi:[1,0,0]\n\t"
        "v_pk_mul_f32 v[74:75], v[62:63], v[208:209] op_sel_hi:[1,0]\n\t"
        "v_pk_fma_f32 v[78:79], v[62:63], v[210:211], v[220:221] op_sel_hi:[1,0,0]\n\t"
        "v_pk_fma_f32 v[128:129], v[128:129], v[56:57], v[72:73]\n\t"
        "v_pk_fma_f32 v[130:131], v[130:131], v[58:59], v[74:75]\n\t"
        "v_pk_fma_f32 v[132:133], v[132:133], v[56:57], v[76:77]\n\t"
        "v_pk_fma_f32 v[134:135], v[134:135], v[58:59], v[78:79]\n\t"
        "v_cvt_pk_bf16_f32 v140, v128, v129\n\t"
        "v_cvt_pk_bf16_f32 v141, v130, v131\n\t"
        "v_cvt_pk_bf16_f32 v142, v132, v133\n\t"
        "v_cvt_pk_bf16_f32 v143, v134, v135\n\t"
        "v_pk_mul_f32 v[64:65], v[192:193], v[216:217] op_sel:[0,1] op_sel_hi:[1,1]\n\t"
        "v_pk_mul_f32 v[66:67], v[194:195], v[216:217] op_sel:[0,1] op_sel_hi:[1,1]\n\t"
        "v_pk_mul_f32 v[68:69], v[196:197], v[216:217] op_sel:[0,1] op_sel_hi:[1,1]\n\t"
        "v_pk_mul_f32 v[70:71], v[198:199], v[216:217] op_sel:[0,1] op_sel_hi:[1,1]\n\t"
        "v_cndmask_b32 v189, v183, v188, vcc\n\t"
        "v_cmp_le_u32 vcc, 48, v213\n\t"
        "s_nop 1\n\t"
        "v_cndmask_b32 v142, v142, v189, vcc\n\t"
        "v_cndmask_b32 v143, v143, v189, vcc\n\t"
        "s_nop 0\n\t"
        "v_permlane32_swap_b32 v140, v142\n\t"
        "v_permlane32_swap_b32 v141, v143\n\t"
        "v_pk_fma_f32 v[112:113], v[112:113], v[216:217], v[64:65] op_sel_hi:[1,0,1]\n\t"
        "v_pk_fma_f32 v[114:115], v[114:115], v[216:217], v[66:67] op_sel_hi:[1,0,1]\n\t"
        "v_pk_fma_f32 v[116:117], v[116:117], v[216:217], v[68:69] op_sel_hi:[1,0,1]\n\t"
        "v_pk_fma_f32 v[118:119], v[118:119], v[216:217], v[70:71] op_sel_hi:[1,0,1]\n\t"
        "buffer_store_dwordx4 v[140:143], v224, s[16:19], s38 offen offset:2048 sc1\n\t"
        "v_cvt_pk_bf16_f32 v144, v112, v113\n\t"
        "v_cvt_pk_bf16_f32 v145, v114, v115\n\t"
        "v_cvt_pk_bf16_f32 v146, v116, v117\n\t"
        "v_cvt_pk_bf16_f32 v147, v118, v119\n\t"
        "v_cndmask_b32 v146, v146, v183, s[48:49]\n\t"
        "v_cndmask_b32 v147, v147, v183, s[48:49]\n\t"
        "buffer_store_dwordx4 v[144:147], v231, s[16:19], s33 offen sc1\n\t"
        "v_readfirstlane_b32 s10, v234\n\t"
        "v_readfirstlane_b32 s11, v235\n\t"
        "s_add_u32 s10, s10, 1\n\t"
        "s_addc_u32 s11, s11, 0\n\t"
        "v_mov_b32 v234, s10\n\t"
        "v_mov_b32 v235, s11\n\t"
        "s_waitcnt vmcnt(3)\n\t"
        "ds_read_b128 v[0:3], v190 offset:0\n\t"
        "ds_read_b128 v[4:7], v191 offset:0\n\t"
        "ds_read_b128 v[8:11], v190 offset:128\n\t"
        "ds_read_b128 v[12:15], v191 offset:128\n\t"
        "ds_read_b128 v[16:19], v190 offset:256\n\t"
        "ds_read_b128 v[20:23], v191 offset:256\n\t"
        "ds_read_b128 v[24:27], v190 offset:384\n\t"
        "ds_read_b128 v[28:31], v191 offset:384\n\t"
        "ds_read_b128 v[32:35], v190 offset:512\n\t"
        "ds_read_b128 v[36:39], v191 offset:512\n\t"
        "ds_read_b128 v[40:43], v190 offset:640\n\t"
        "ds_read_b128 v[44:47], v191 offset:640\n\t"
        "ds_read_b128 v[48:51], v190 offset:768\n\t"
        "ds_read_b128 v[52:55], v191 offset:768\n\t"
        "s_mul_i32 s38, s27, 147456\n\t"
        "s_lshl_b32 s39, s25, 11\n\t"
        "s_lshl_b32 s40, s24, 3\n\t"
        "v_lshlrev_b32 v236, 4, v238\n\t"
        "s_add_u32 s38, s38, s39\n\t"
        "s_add_u32 s38, s38, s40\n\t"
        "s_add_u32 s38, s38, 8192\n\t"
        "s_waitcnt vmcnt(0)\n\t"
        "s_waitcnt lgkmcnt(0)\n\t"
        "s_barrier\n\t"
        "s_cmp_lg_u32 s26, 0\n\t"
        "s_cbranch_scc1 sig_13_%=\n\t"
        "s_mov_b64 exec, 0xffff\n\t"
        "buffer_atomic_swap_x2 v[234:235], v236, s[16:19], s38 offen\n\t"
        "s_mov_b64 exec, -1\n\t"
        "sig_13_%=:\n\t"
        "s_cmp_eq_u32 s74, 4\n\t"
        "s_cbranch_scc1 pn_14_%=\n\t"
        "v_lshrrev_b32 v228, 4, v239\n\t"
        "v_mul_lo_u32 v228, v228, s69\n\t"
        "v_bfe_u32 v229, v239, 2, 2\n\t"
        "v_lshl_add_u32 v228, v229, 4, v228\n\t"
        "s_lshl_b32 s69, s69, 4\n\t"
        "s_mul_i32 s38, s26, 7168\n\t"
        "s_add_u32 m0, s38, 114688\n\t"
        "s_nop 0\n\t"
        "buffer_load_dwordx4 v228, s[64:67], s68 offen lds\n\t"
        "s_add_u32 s68, s68, s69\n\t"
        "s_add_u32 m0, m0, 1024\n\t"
        "s_nop 0\n\t"
        "buffer_load_dwordx4 v228, s[64:67], s68 offen lds\n\t"
        "s_add_u32 s68, s68, s69\n\t"
        "s_add_u32 m0, m0, 1024\n\t"
        "s_nop 0\n\t"
        "buffer_load_dwordx4 v228, s[64:67], s68 offen lds\n\t"
        "s_add_u32 s68, s68, s69\n\t"
        "s_add_u32 m0, m0, 1024\n\t"
        "s_nop 0\n\t"
        "buffer_load_dwordx4 v228, s[64:67], s68 offen lds\n\t"
        "s_add_u32 s68, s68, s69\n\t"
        "s_add_u32 m0, m0, 1024\n\t"
        "s_nop 0\n\t"
        "buffer_load_dwordx4 v228, s[64:67], s68 offen lds\n\t"
        "s_add_u32 s68, s68, s69\n\t"
        "s_add_u32 m0, m0, 1024\n\t"
        "s_nop 0\n\t"
        "buffer_load_dwordx4 v228, s[64:67], s68 offen lds\n\t"
        "s_add_u32 s68, s68, s69\n\t"
        "s_add_u32 m0, m0, 1024\n\t"
        "s_nop 0\n\t"
        "buffer_load_dwordx4 v228, s[64:67], s68 offen lds\n\t"
        "s_cmp_lg_u32 s69, 57344\n\t"
        "s_cbranch_scc1 pn_14_%=\n\t"
        "v_bfe_u32 v229, v239, 2, 5\n\t"
        "v_lshl_add_u32 v229, v229, 1, s75\n\t"
        "global_load_ushort v230, v229, s[80:81]\n\t"
        "pn_14_%=:\n\t"
        "v_mov_b32 v172, 0x3f803f80\n\t"
        "v_mov_b32 v173, 0x3f803f80\n\t"
        "v_mov_b32 v174, 0x3f803f80\n\t"
        "v_mov_b32 v175, 0x3f803f80\n\t"
        "ds_read_b64_tr_b16 v[144:145], v184 offset:0\n\t"
        "ds_read_b64_tr_b16 v[146:147], v184 offset:256\n\t"
        "ds_read_b64_tr_b16 v[148:149], v185 offset:0\n\t"
        "ds_read_b64_tr_b16 v[150:151], v185 offset:256\n\t"
        "ds_read_b64_tr_b16 v[152:153], v184 offset:2048\n\t"
        "ds_read_b64_tr_b16 v[154:155], v184 offset:2304\n\t"
        "ds_read_b64_tr_b16 v[156:157], v185 offset:2048\n\t"
        "ds_read_b64_tr_b16 v[158:159], v185 offset:2304\n\t"
        "ds_read_b64_tr_b16 v[160:161], v184 offset:4096\n\t"
        "ds_read_b64_tr_b16 v[162:163], v184 offset:4352\n\t"
        "ds_read_b64_tr_b16 v[164:165], v185 offset:4096\n\t"
        "ds_read_b64_tr_b16 v[166:167], v185 offset:4352\n\t"
        "s_waitcnt lgkmcnt(8)\n\t"
        "v_mfma_f32_16x16x32_bf16 v[112:115], v[144:147], v[0:3], 0\n\t"
        "ds_read_b64_tr_b16 v[168:169], v184 offset:6144\n\t"
        "ds_read_b64_tr_b16 v[170:171], v184 offset:6400\n\t"
        "v_mfma_f32_16x16x32_bf16 v[116:119], v[148:151], v[0:3], 0\n\t"
        "v_mfma_f32_16x16x32_bf16 v[176:179], v[172:175], v[0:3], 0\n\t"
        "ds_read_b64_tr_b16 v[144:145], v185 offset:6144\n\t"
        "ds_read_b64_tr_b16 v[146:147], v185 offset:6400\n\t"
        "s_waitcnt lgkmcnt(8)\n\t"
        "v_mfma_f32_16x16x32_bf16 v[120:123], v[152:155], v[0:3], 0\n\t"
        "ds_read_b64_tr_b16 v[148:149], v184 offset:8192\n\t"
        "ds_read_b64_tr_b16 v[150:151], v184 offset:8448\n\t"
        "v_mfma_f32_16x16x32_bf16 v[124:127], v[156:159], v[0:3], 0\n\t"
        "v_mfma_f32_16x16x32_bf16 v[180:183], v[0:3], v[0:3], 0\n\t"
        "ds_read_b64_tr_b16 v[152:153], v185 offset:8192\n\t"
        "ds_read_b64_tr_b16 v[154:155], v185 offset:8448\n\t"
        "s_waitcnt lgkmcnt(8)\n\t"
        "v_mfma_f32_16x16x32_bf16 v[128:131], v[0:3], v[160:163], 0\n\t"
        "ds_read_b64_tr_b16 v[156:157], v184 offset:10240\n\t"
        "ds_read_b64_tr_b16 v[158:159], v184 offset:10496\n\t"
        "v_mfma_f32_16x16x32_bf16 v[132:135], v[0:3], v[164:167], 0\n\t"
        "ds_read_b64_tr_b16 v[160:161], v185 offset:10240\n\t"
        "ds_read_b64_tr_b16 v[162:163], v185 offset:10496\n\t"
        "s_waitcnt lgkmcnt(8)\n\t"
        "v_mfma_f32_16x16x32_bf16 v[112:115], v[168:171], v[4:7], v[112:115]\n\t"
        "ds_read_b64_tr_b16 v[164:165], v184 offset:12288\n\t"
        "ds_read_b64_tr_b16 v[166:167], v184 offset:12544\n\t"
        "v_mfma_f32_16x16x32_bf16 v[116:119], v[144:147], v[4:7], v[116:119]\n\t"
        "v_mfma_f32_16x16x32_bf16 v[176:179], v[172:175], v[4:7], v[176:179]\n\t"
        "ds_read_b64_tr_b16 v[168:169], v185 offset:12288\n\t"
        "ds_read_b64_tr_b16 v[170:171], v185 offset:12544\n\t"
        "s_waitcnt lgkmcnt(8)\n\t"
        "v_mfma_f32_16x16x32_bf16 v[120:123], v[148:151], v[4:7], v[120:123]\n\t"
        "ds_read_b64_tr_b16 v[144:145], v184 offset:14336\n\t"
        "ds_read_b64_tr_b16 v[146:147], v184 offset:14592\n\t"
        "v_mfma_f32_16x16x32_bf16 v[124:127], v[152:155], v[4:7], v[124:127]\n\t"
        "v_mfma_f32_16x16x32_bf16 v[180:183], v[4:7], v[4:7], v[180:183]\n\t"
        "ds_read_b64_tr_b16 v[148:149], v185 offset:14336\n\t"
        "ds_read_b64_tr_b16 v[150:151], v185 offset:14592\n\t"
        "s_waitcnt lgkmcnt(8)\n\t"
        "v_mfma_f32_16x16x32_bf16 v[128:131], v[4:7], v[156:159], v[128:131]\n\t"
        "ds_read_b64_tr_b16 v[152:153], v184 offset:16384\n\t"
        "ds_read_b64_tr_b16 v[154:155], v184 offset:16640\n\t"
        "v_mfma_f32_16x16x32_bf16 v[132:135], v[4:7], v[160:163], v[132:135]\n\t"
        "ds_read_b64_tr_b16 v[156:157], v185 offset:16384\n\t"
        "ds_read_b64_tr_b16 v[158:159], v185 offset:16640\n\t"
        "s_waitcnt lgkmcnt(8)\n\t"
        "v_mfma_f32_16x16x32_bf16 v[112:115], v[164:167], v[8:11], v[112:115]\n\t"
        "ds_read_b64_tr_b16 v[160:161], v184 offset:18432\n\t"
        "ds_read_b64_tr_b16 v[162:163], v184 offset:18688\n\t"
        "v_mfma_f32_16x16x32_bf16 v[116:119], v[168:171], v[8:11], v[116:119]\n\t"
        "v_mfma_f32_16x16x32_bf16 v[176:179], v[172:175], v[8:11], v[176:179]\n\t"
        "ds_read_b64_tr_b16 v[164:165], v185 offset:18432\n\t"
        "ds_read_b64_tr_b16 v[166:167], v185 offset:18688\n\t"
        "s_waitcnt lgkmcnt(8)\n\t"
        "v_mfma_f32_16x16x32_bf16 v[120:123], v[144:147], v[8:11], v[120:123]\n\t"
        "ds_read_b64_tr_b16 v[168:169], v184 offset:20480\n\t"
        "ds_read_b64_tr_b16 v[170:171], v184 offset:20736\n\t"
        "v_mfma_f32_16x16x32_bf16 v[124:127], v[148:151], v[8:11], v[124:127]\n\t"
        "v_mfma_f32_16x16x32_bf16 v[180:183], v[8:11], v[8:11], v[180:183]\n\t"
        "ds_read_b64_tr_b16 v[144:145], v185 offset:20480\n\t"
        "ds_read_b64_tr_b16 v[146:147], v185 offset:20736\n\t"
        "s_waitcnt lgkmcnt(8)\n\t"
        "v_mfma_f32_16x16x32_bf16 v[128:131], v[8:11], v[152:155], v[128:131]\n\t"
        "ds_read_b64_tr_b16 v[148:149], v184 offset:22528\n\t"
        "ds_read_b64_tr_b16 v[150:151], v184 offset:22784\n\t"
        "v_mfma_f32_16x16x32_bf16 v[132:135], v[8:11], v[156:159], v[132:135]\n\t"
        "ds_read_b64_tr_b16 v[152:153], v185 offset:22528\n\t"
        "ds_read_b64_tr_b16 v[154:155], v185 offset:22784\n\t"
        "s_waitcnt lgkmcnt(8)\n\t"
        "v_mfma_f32_16x16x32_bf16 v[112:115], v[160:163], v[12:15], v[112:115]\n\t"
        "ds_read_b64_tr_b16 v[156:157], v184 offset:24576\n\t"
        "ds_read_b64_tr_b16 v[158:159], v184 offset:24832\n\t"
        "v_mfma_f32_16x16x32_bf16 v[116:119], v[164:167], v[12:15], v[116:119]\n\t"
        "v_mfma_f32_16x16x32_bf16 v[176:179], v[172:175], v[12:15], v[176:179]\n\t"
        "ds_read_b64_tr_b16 v[160:161], v185 offset:24576\n\t"
        "ds_read_b64_tr_b16 v[162:163], v185 offset:24832\n\t"
        "s_waitcnt lgkmcnt(8)\n\t"
        "v_mfma_f32_16x16x32_bf16 v[120:123], v[168:171], v[12:15], v[120:123]\n\t"
        "ds_read_b64_tr_b16 v[164:165], v184 offset:26624\n\t"
        "ds_read_b64_tr_b16 v[166:167], v184 offset:26880\n\t"
        "v_and_b32 v188, 0x7c, v239\n\t"
        "v_mul_u32_u24 v188, 224, v188\n\t"
        "v_lshrrev_b32 v189, 1, v244\n\t"
        "v_add_u32 v188, v188, v189\n\t"
        "v_add_u32 v188, s15, v188\n\t"
        "v_min_u32 v188, 1777640, v188\n\t"
        "v_add_u32 v189, 57344, v188\n\t"
        "v_mfma_f32_16x16x32_bf16 v[124:127], v[144:147], v[12:15], v[124:127]\n\t"
        "v_mfma_f32_16x16x32_bf16 v[180:183], v[12:15], v[12:15], v[180:183]\n\t"
        "ds_read_b64_tr_b16 v[168:169], v185 offset:26624\n\t"
        "ds_read_b64_tr_b16 v[170:171], v185 offset:26880\n\t"
        "s_waitcnt lgkmcnt(8)\n\t"
        "v_mfma_f32_16x16x32_bf16 v[128:131], v[12:15], v[148:151], v[128:131]\n\t"
        "ds_read_b64_tr_b16 v[144:145], v184 offset:28672\n\t"
        "ds_read_b64_tr_b16 v[146:147], v184 offset:28928\n\t"
        "v_mfma_f32_16x16x32_bf16 v[132:135], v[12:15], v[152:155], v[132:135]\n\t"
        "ds_read_b64_tr_b16 v[148:149], v185 offset:28672\n\t"
        "ds_read_b64_tr_b16 v[150:151], v185 offset:28928\n\t"
        "s_waitcnt lgkmcnt(8)\n\t"
        "v_mfma_f32_16x16x32_bf16 v[112:115], v[156:159], v[16:19], v[112:115]\n\t"
        "ds_read_b64_tr_b16 v[152:153], v184 offset:30720\n\t"
        "ds_read_b64_tr_b16 v[154:155], v184 offset:30976\n\t"
        "v_mfma_f32_16x16x32_bf16 v[116:119], v[160:163], v[16:19], v[116:119]\n\t"
        "v_mfma_f32_16x16x32_bf16 v[176:179], v[172:175], v[16:19], v[176:179]\n\t"
        "ds_read_b64_tr_b16 v[156:157], v185 offset:30720\n\t"
        "ds_read_b64_tr_b16 v[158:159], v185 offset:30976\n\t"
        "s_waitcnt lgkmcnt(8)\n\t"
        "v_mfma_f32_16x16x32_bf16 v[120:123], v[164:167], v[16:19], v[120:123]\n\t"
        "ds_read_b64_tr_b16 v[160:161], v184 offset:32768\n\t"
        "ds_read_b64_tr_b16 v[162:163], v184 offset:33024\n\t"
        "global_load_dwordx2 v[248:249], v188, s[8:9] offset:0\n\t"
        "v_mfma_f32_16x16x32_bf16 v[124:127], v[168:171], v[16:19], v[124:127]\n\t"
        "v_mfma_f32_16x16x32_bf16 v[180:183], v[16:19], v[16:19], v[180:183]\n\t"
        "ds_read_b64_tr_b16 v[164:165], v185 offset:32768\n\t"
        "ds_read_b64_tr_b16 v[166:167], v185 offset:33024\n\t"
        "s_waitcnt lgkmcnt(8)\n\t"
        "v_mfma_f32_16x16x32_bf16 v[128:131], v[16:19], v[144:147], v[128:131]\n\t"
        "ds_read_b64_tr_b16 v[168:169], v184 offset:34816\n\t"
        "ds_read_b64_tr_b16 v[170:171], v184 offset:35072\n\t"
        "v_mfma_f32_16x16x32_bf16 v[132:135], v[16:19], v[148:151], v[132:135]\n\t"
        "ds_read_b64_tr_b16 v[144:145], v185 offset:34816\n\t"
        "ds_read_b64_tr_b16 v[146:147], v185 offset:35072\n\t"
        "s_waitcnt lgkmcnt(8)\n\t"
        "v_mfma_f32_16x16x32_bf16 v[112:115], v[152:155], v[20:23], v[112:115]\n\t"
        "ds_read_b64_tr_b16 v[148:149], v184 offset:36864\n\t"
        "ds_read_b64_tr_b16 v[150:151], v184 offset:37120\n\t"
        "v_mfma_f32_16x16x32_bf16 v[116:119], v[156:159], v[20:23], v[116:119]\n\t"
        "v_mfma_f32_16x16x32_bf16 v[176:179], v[172:175], v[20:23], v[176:179]\n\t"
        "ds_read_b64_tr_b16 v[152:153], v185 offset:36864\n\t"
        "ds_read_b64_tr_b16 v[154:155], v185 offset:37120\n\t"
        "s_waitcnt lgkmcnt(8)\n\t"
        "v_mfma_f32_16x16x32_bf16 v[120:123], v[160:163], v[20:23], v[120:123]\n\t"
        "ds_read_b64_tr_b16 v[156:157], v184 offset:38912\n\t"
        "ds_read_b64_tr_b16 v[158:159], v184 offset:39168\n\t"
        "v_mfma_f32_16x16x32_bf16 v[124:127], v[164:167], v[20:23], v[124:127]\n\t"
        "v_mfma_f32_16x16x32_bf16 v[180:183], v[20:23], v[20:23], v[180:183]\n\t"
        "ds_read_b64_tr_b16 v[160:161], v185 offset:38912\n\t"
        "ds_read_b64_tr_b16 v[162:163], v185 offset:39168\n\t"
        "s_waitcnt lgkmcnt(8)\n\t"
        "v_mfma_f32_16x16x32_bf16 v[128:131], v[20:23], v[168:171], v[128:131]\n\t"
        "ds_read_b64_tr_b16 v[164:165], v184 offset:40960\n\t"
        "ds_read_b64_tr_b16 v[166:167], v184 offset:41216\n\t"
        "v_mfma_f32_16x16x32_bf16 v[132:135], v[20:23], v[144:147], v[132:135]\n\t"
        "ds_read_b64_tr_b16 v[168:169], v185 offset:40960\n\t"
        "ds_read_b64_tr_b16 v[170:171], v185 offset:41216\n\t"
        "s_waitcnt lgkmcnt(8)\n\t"
        "v_mfma_f32_16x16x32_bf16 v[112:115], v[148:151], v[24:27], v[112:115]\n\t"
        "ds_read_b64_tr_b16 v[144:145], v186 offset:0\n\t"
        "ds_read_b64_tr_b16 v[146:147], v186 offset:256\n\t"
        "v_mfma_f32_16x16x32_bf16 v[116:119], v[152:155], v[24:27], v[116:119]\n\t"
        "v_mfma_f32_16x16x32_bf16 v[176:179], v[172:175], v[24:27], v[176:179]\n\t"
        "ds_read_b64_tr_b16 v[148:149], v187 offset:0\n\t"
        "ds_read_b64_tr_b16 v[150:151], v187 offset:256\n\t"
        "s_waitcnt lgkmcnt(8)\n\t"
        "v_mfma_f32_16x16x32_bf16 v[120:123], v[156:159], v[24:27], v[120:123]\n\t"
        "ds_read_b64_tr_b16 v[152:153], v186 offset:2048\n\t"
        "ds_read_b64_tr_b16 v[154:155], v186 offset:2304\n\t"
        "global_load_dwordx2 v[250:251], v188, s[8:9] offset:16\n\t"
        "v_mfma_f32_16x16x32_bf16 v[124:127], v[160:163], v[24:27], v[124:127]\n\t"
        "v_mfma_f32_16x16x32_bf16 v[180:183], v[24:27], v[24:27], v[180:183]\n\t"
        "ds_read_b64_tr_b16 v[156:157], v187 offset:2048\n\t"
        "ds_read_b64_tr_b16 v[158:159], v187 offset:2304\n\t"
        "s_waitcnt lgkmcnt(8)\n\t"
        "v_mfma_f32_16x16x32_bf16 v[128:131], v[24:27], v[164:167], v[128:131]\n\t"
        "ds_read_b64_tr_b16 v[160:161], v186 offset:4096\n\t"
        "ds_read_b64_tr_b16 v[162:163], v186 offset:4352\n\t"
        "v_mfma_f32_16x16x32_bf16 v[132:135], v[24:27], v[168:171], v[132:135]\n\t"
        "ds_read_b64_tr_b16 v[164:165], v187 offset:4096\n\t"
        "ds_read_b64_tr_b16 v[166:167], v187 offset:4352\n\t"
        "s_waitcnt lgkmcnt(8)\n\t"
        "v_mfma_f32_16x16x32_bf16 v[112:115], v[144:147], v[28:31], v[112:115]\n\t"
        "ds_read_b64_tr_b16 v[168:169], v186 offset:6144\n\t"
        "ds_read_b64_tr_b16 v[170:171], v186 offset:6400\n\t"
        "v_mfma_f32_16x16x32_bf16 v[116:119], v[148:151], v[28:31], v[116:119]\n\t"
        "v_mfma_f32_16x16x32_bf16 v[176:179], v[172:175], v[28:31], v[176:179]\n\t"
        "ds_read_b64_tr_b16 v[144:145], v187 offset:6144\n\t"
        "ds_read_b64_tr_b16 v[146:147], v187 offset:6400\n\t"
        "s_waitcnt lgkmcnt(8)\n\t"
        "v_mfma_f32_16x16x32_bf16 v[120:123], v[152:155], v[28:31], v[120:123]\n\t"
        "ds_read_b64_tr_b16 v[148:149], v186 offset:8192\n\t"
        "ds_read_b64_tr_b16 v[150:151], v186 offset:8448\n\t"
        "v_mfma_f32_16x16x32_bf16 v[124:127], v[156:159], v[28:31], v[124:127]\n\t"
        "v_mfma_f32_16x16x32_bf16 v[180:183], v[28:31], v[28:31], v[180:183]\n\t"
        "ds_read_b64_tr_b16 v[152:153], v187 offset:8192\n\t"
        "ds_read_b64_tr_b16 v[154:155], v187 offset:8448\n\t"
        "s_waitcnt lgkmcnt(8)\n\t"
        "v_mfma_f32_16x16x32_bf16 v[128:131], v[28:31], v[160:163], v[128:131]\n\t"
        "ds_read_b64_tr_b16 v[156:157], v186 offset:10240\n\t"
        "ds_read_b64_tr_b16 v[158:159], v186 offset:10496\n\t"
        "v_mfma_f32_16x16x32_bf16 v[132:135], v[28:31], v[164:167], v[132:135]\n\t"
        "ds_read_b64_tr_b16 v[160:161], v187 offset:10240\n\t"
        "ds_read_b64_tr_b16 v[162:163], v187 offset:10496\n\t"
        "s_waitcnt lgkmcnt(8)\n\t"
        "v_mfma_f32_16x16x32_bf16 v[112:115], v[168:171], v[32:35], v[112:115]\n\t"
        "ds_read_b64_tr_b16 v[164:165], v186 offset:12288\n\t"
        "ds_read_b64_tr_b16 v[166:167], v186 offset:12544\n\t"
        "v_mfma_f32_16x16x32_bf16 v[116:119], v[144:147], v[32:35], v[116:119]\n\t"
        "v_mfma_f32_16x16x32_bf16 v[176:179], v[172:175], v[32:35], v[176:179]\n\t"
        "ds_read_b64_tr_b16 v[168:169], v187 offset:12288\n\t"
        "ds_read_b64_tr_b16 v[170:171], v187 offset:12544\n\t"
        "s_waitcnt lgkmcnt(8)\n\t"
        "v_mfma_f32_16x16x32_bf16 v[120:123], v[148:151], v[32:35], v[120:123]\n\t"
        "ds_read_b64_tr_b16 v[144:145], v186 offset:14336\n\t"
        "ds_read_b64_tr_b16 v[146:147], v186 offset:14592\n\t"
        "global_load_dwordx2 v[252:253], v189, s[8:9] offset:0\n\t"
        "v_mfma_f32_16x16x32_bf16 v[124:127], v[152:155], v[32:35], v[124:127]\n\t"
        "v_mfma_f32_16x16x32_bf16 v[180:183], v[32:35], v[32:35], v[180:183]\n\t"
        "ds_read_b64_tr_b16 v[148:149], v187 offset:14336\n\t"
        "ds_read_b64_tr_b16 v[150:151], v187 offset:14592\n\t"
        "s_waitcnt lgkmcnt(8)\n\t"
        "v_mfma_f32_16x16x32_bf16 v[128:131], v[32:35], v[156:159], v[128:131]\n\t"
        "ds_read_b64_tr_b16 v[152:153], v186 offset:16384\n\t"
        "ds_read_b64_tr_b16 v[154:155], v186 offset:16640\n\t"
        "v_mfma_f32_16x16x32_bf16 v[132:135], v[32:35], v[160:163], v[132:135]\n\t"
        "ds_read_b64_tr_b16 v[156:157], v187 offset:16384\n\t"
        "ds_read_b64_tr_b16 v[158:159], v187 offset:16640\n\t"
        "s_waitcnt lgkmcnt(8)\n\t"
        "v_mfma_f32_16x16x32_bf16 v[112:115], v[164:167], v[36:39], v[112:115]\n\t"
        "ds_read_b64_tr_b16 v[160:161], v186 offset:18432\n\t"
        "ds_read_b64_tr_b16 v[162:163], v186 offset:18688\n\t"
        "v_mfma_f32_16x16x32_bf16 v[116:119], v[168:171], v[36:39], v[116:119]\n\t"
        "v_mfma_f32_16x16x32_bf16 v[176:179], v[172:175], v[36:39], v[176:179]\n\t"
        "ds_read_b64_tr_b16 v[164:165], v187 offset:18432\n\t"
        "ds_read_b64_tr_b16 v[166:167], v187 offset:18688\n\t"
        "s_waitcnt lgkmcnt(8)\n\t"
        "v_mfma_f32_16x16x32_bf16 v[120:123], v[144:147], v[36:39], v[120:123]\n\t"
        "ds_read_b64_tr_b16 v[168:169], v186 offset:20480\n\t"
        "ds_read_b64_tr_b16 v[170:171], v186 offset:20736\n\t"
        "v_mfma_f32_16x16x32_bf16 v[124:127], v[148:151], v[36:39], v[124:127]\n\t"
        "v_mfma_f32_16x16x32_bf16 v[180:183], v[36:39], v[36:39], v[180:183]\n\t"
        "ds_read_b64_tr_b16 v[144:145], v187 offset:20480\n\t"
        "ds_read_b64_tr_b16 v[146:147], v187 offset:20736\n\t"
        "s_waitcnt lgkmcnt(8)\n\t"
        "v_mfma_f32_16x16x32_bf16 v[128:131], v[36:39], v[152:155], v[128:131]\n\t"
        "ds_read_b64_tr_b16 v[148:149], v186 offset:22528\n\t"
        "ds_read_b64_tr_b16 v[150:151], v186 offset:22784\n\t"
        "v_mfma_f32_16x16x32_bf16 v[132:135], v[36:39], v[156:159], v[132:135]\n\t"
        "ds_read_b64_tr_b16 v[152:153], v187 offset:22528\n\t"
        "ds_read_b64_tr_b16 v[154:155], v187 offset:22784\n\t"
        "s_waitcnt lgkmcnt(8)\n\t"
        "v_mfma_f32_16x16x32_bf16 v[112:115], v[160:163], v[40:43], v[112:115]\n\t"
        "ds_read_b64_tr_b16 v[156:157], v186 offset:24576\n\t"
        "ds_read_b64_tr_b16 v[158:159], v186 offset:24832\n\t"
        "v_mfma_f32_16x16x32_bf16 v[116:119], v[164:167], v[40:43], v[116:119]\n\t"
        "v_mfma_f32_16x16x32_bf16 v[176:179], v[172:175], v[40:43], v[176:179]\n\t"
        "ds_read_b64_tr_b16 v[160:161], v187 offset:24576\n\t"
        "ds_read_b64_tr_b16 v[162:163], v187 offset:24832\n\t"
        "s_waitcnt lgkmcnt(8)\n\t"
        "v_mfma_f32_16x16x32_bf16 v[120:123], v[168:171], v[40:43], v[120:123]\n\t"
        "ds_read_b64_tr_b16 v[164:165], v186 offset:26624\n\t"
        "ds_read_b64_tr_b16 v[166:167], v186 offset:26880\n\t"
        "global_load_dwordx2 v[254:255], v189, s[8:9] offset:16\n\t"
        "v_mfma_f32_16x16x32_bf16 v[124:127], v[144:147], v[40:43], v[124:127]\n\t"
        "v_mfma_f32_16x16x32_bf16 v[180:183], v[40:43], v[40:43], v[180:183]\n\t"
        "ds_read_b64_tr_b16 v[168:169], v187 offset:26624\n\t"
        "ds_read_b64_tr_b16 v[170:171], v187 offset:26880\n\t"
        "s_waitcnt lgkmcnt(8)\n\t"
        "v_mfma_f32_16x16x32_bf16 v[128:131], v[40:43], v[148:151], v[128:131]\n\t"
        "ds_read_b64_tr_b16 v[144:145], v186 offset:28672\n\t"
        "ds_read_b64_tr_b16 v[146:147], v186 offset:28928\n\t"
        "v_mfma_f32_16x16x32_bf16 v[132:135], v[40:43], v[152:155], v[132:135]\n\t"
        "ds_read_b64_tr_b16 v[148:149], v187 offset:28672\n\t"
        "ds_read_b64_tr_b16 v[150:151], v187 offset:28928\n\t"
        "s_waitcnt lgkmcnt(8)\n\t"
        "v_mfma_f32_16x16x32_bf16 v[112:115], v[156:159], v[44:47], v[112:115]\n\t"
        "ds_read_b64_tr_b16 v[152:153], v186 offset:30720\n\t"
        "ds_read_b64_tr_b16 v[154:155], v186 offset:30976\n\t"
        "v_mfma_f32_16x16x32_bf16 v[116:119], v[160:163], v[44:47], v[116:119]\n\t"
        "v_mfma_f32_16x16x32_bf16 v[176:179], v[172:175], v[44:47], v[176:179]\n\t"
        "ds_read_b64_tr_b16 v[156:157], v187 offset:30720\n\t"
        "ds_read_b64_tr_b16 v[158:159], v187 offset:30976\n\t"
        "s_waitcnt lgkmcnt(8)\n\t"
        "v_mfma_f32_16x16x32_bf16 v[120:123], v[164:167], v[44:47], v[120:123]\n\t"
        "ds_read_b64_tr_b16 v[160:161], v186 offset:32768\n\t"
        "ds_read_b64_tr_b16 v[162:163], v186 offset:33024\n\t"
        "v_mfma_f32_16x16x32_bf16 v[124:127], v[168:171], v[44:47], v[124:127]\n\t"
        "v_mfma_f32_16x16x32_bf16 v[180:183], v[44:47], v[44:47], v[180:183]\n\t"
        "ds_read_b64_tr_b16 v[164:165], v187 offset:32768\n\t"
        "ds_read_b64_tr_b16 v[166:167], v187 offset:33024\n\t"
        "s_waitcnt lgkmcnt(8)\n\t"
        "v_mfma_f32_16x16x32_bf16 v[128:131], v[44:47], v[144:147], v[128:131]\n\t"
        "ds_read_b64_tr_b16 v[168:169], v186 offset:34816\n\t"
        "ds_read_b64_tr_b16 v[170:171], v186 offset:35072\n\t"
        "v_mfma_f32_16x16x32_bf16 v[132:135], v[44:47], v[148:151], v[132:135]\n\t"
        "ds_read_b64_tr_b16 v[144:145], v187 offset:34816\n\t"
        "ds_read_b64_tr_b16 v[146:147], v187 offset:35072\n\t"
        "s_waitcnt lgkmcnt(8)\n\t"
        "v_mfma_f32_16x16x32_bf16 v[112:115], v[152:155], v[48:51], v[112:115]\n\t"
        "ds_read_b64_tr_b16 v[148:149], v186 offset:36864\n\t"
        "ds_read_b64_tr_b16 v[150:151], v186 offset:37120\n\t"
        "v_mfma_f32_16x16x32_bf16 v[116:119], v[156:159], v[48:51], v[116:119]\n\t"
        "v_mfma_f32_16x16x32_bf16 v[176:179], v[172:175], v[48:51], v[176:179]\n\t"
        "ds_read_b64_tr_b16 v[152:153], v187 offset:36864\n\t"
        "ds_read_b64_tr_b16 v[154:155], v187 offset:37120\n\t"
        "s_waitcnt lgkmcnt(8)\n\t"
        "v_mfma_f32_16x16x32_bf16 v[120:123], v[160:163], v[48:51], v[120:123]\n\t"
        "ds_read_b64_tr_b16 v[156:157], v186 offset:38912\n\t"
        "ds_read_b64_tr_b16 v[158:159], v186 offset:39168\n\t"
        "v_mfma_f32_16x16x32_bf16 v[124:127], v[164:167], v[48:51], v[124:127]\n\t"
        "v_mfma_f32_16x16x32_bf16 v[180:183], v[48:51], v[48:51], v[180:183]\n\t"
        "ds_read_b64_tr_b16 v[160:161], v187 offset:38912\n\t"
        "ds_read_b64_tr_b16 v[162:163], v187 offset:39168\n\t"
        "s_waitcnt lgkmcnt(8)\n\t"
        "v_mfma_f32_16x16x32_bf16 v[128:131], v[48:51], v[168:171], v[128:131]\n\t"
        "ds_read_b64_tr_b16 v[164:165], v186 offset:40960\n\t"
        "ds_read_b64_tr_b16 v[166:167], v186 offset:41216\n\t"
        "v_mfma_f32_16x16x32_bf16 v[132:135], v[48:51], v[144:147], v[132:135]\n\t"
        "ds_read_b64_tr_b16 v[168:169], v187 offset:40960\n\t"
        "ds_read_b64_tr_b16 v[170:171], v187 offset:41216\n\t"
        "s_waitcnt lgkmcnt(8)\n\t"
        "v_mfma_f32_16x16x32_bf16 v[112:115], v[148:151], v[52:55], v[112:115]\n\t"
        "v_mfma_f32_16x16x32_bf16 v[116:119], v[152:155], v[52:55], v[116:119]\n\t"
        "v_mfma_f32_16x16x32_bf16 v[176:179], v[172:175], v[52:55], v[176:179]\n\t"
        "s_waitcnt lgkmcnt(4)\n\t"
        "v_mfma_f32_16x16x32_bf16 v[120:123], v[156:159], v[52:55], v[120:123]\n\t"
        "v_mfma_f32_16x16x32_bf16 v[124:127], v[160:163], v[52:55], v[124:127]\n\t"
        "v_mfma_f32_16x16x32_bf16 v[180:183], v[52:55], v[52:55], v[180:183]\n\t"
        "s_waitcnt lgkmcnt(0)\n\t"
        "v_mfma_f32_16x16x32_bf16 v[128:131], v[52:55], v[164:167], v[128:131]\n\t"
        "v_mfma_f32_16x16x32_bf16 v[132:135], v[52:55], v[168:171], v[132:135]\n\t"
        "v_bfe_u32 v188, v239, 2, 2\n\t"
        "v_cmp_eq_u32 vcc, 1, v188\n\t"
        "s_lshl_b32 s38, s26, 7\n\t"
        "s_add_u32 s38, s38, 86016\n\t"
        "s_nop 0\n\t"
        "v_cndmask_b32 v180, v180, v181, vcc\n\t"
        "v_cmp_eq_u32 vcc, 2, v188\n\t"
        "v_add_u32 v226, s38, v213\n\t"
        "v_add_u32 v227, s38, v225\n\t"
        "v_cndmask_b32 v180, v180, v182, vcc\n\t"
        "v_cmp_eq_u32 vcc, 3, v188\n\t"
        "v_and_b32 v189, 0x30, v213\n\t"
        "v_mul_f32 v176, 0x3e800000, v176\n\t"
        "v_cndmask_b32 v180, v180, v183, vcc\n\t"
        "v_cmp_eq_u32 vcc, v189, v225\n\t"
        "s_nop 1\n\t"
        "v_cndmask_b32 v177, 0, v180, vcc\n\t"
        "v_mov_b32 v178, v176\n\t"
        "v_mov_b32 v179, v177\n\t"
        "s_nop 0\n\t"
        "v_permlane32_swap_b32 v176, v178\n\t"
        "v_permlane32_swap_b32 v177, v179\n\t"
        "v_add_f32 v176, v176, v178\n\t"
        "v_add_f32 v177, v177, v179\n\t"
        "v_mov_b32 v178, v176\n\t"
        "v_mov_b32 v179, v177\n\t"
        "s_nop 0\n\t"
        "v_permlane16_swap_b32 v176, v178\n\t"
        "v_permlane16_swap_b32 v177, v179\n\t"
        "v_add_f32 v176, v176, v178\n\t"
        "v_add_f32 v177, v177, v179\n\t"
        "v_mul_f32 v176, 0x3b124925, v176\n\t"
        "v_mul_f32 v177, 0x3b124925, v177\n\t"
        "v_fma_f32 v177, -v176, v176, v177\n\t"
        "v_max_f32 v177, 0, v177\n\t"
        "v_add_f32 v177, 0x3727c5ac, v177\n\t"
        "v_rsq_f32 v214, v177\n\t"
        "s_nop 0\n\t"
        "v_mul_f32 v215, -v176, v214\n\t"
        "ds_write2_b32 v226, v214, v215 offset0:0 offset1:16\n\t"
        "ds_read_b128 v[0:3], v227\n\t"
        "ds_read_b128 v[4:7], v227 offset:64\n\t"
        "s_mul_i32 s9, s27, 147456\n\t"
        "s_add_u32 s9, s9, s12\n\t"
        "s_add_u32 s9, s9, 8192\n\t"
        "buffer_load_dwordx2 v[232:233], v238, s[16:19], s9 offen sc1\n\t"
        "s_add_u32 s47, s31, s28\n\t"
        "s_add_u32 s47, s47, 4399104\n\t"
        "s_add_u32 s33, s37, s28\n\t"
        "s_add_u32 s33, s33, 2297856\n\t"
        "v_cmp_eq_u32 vcc, 48, v225\n\t"
        "v_mov_b32 v219, 0\n\t"
        "v_mul_f32 v216, 0x3e8b97f6, v214\n\t"
        "v_cndmask_b32 v218, 0, 1.0, vcc\n\t"
        "v_cmp_eq_u32 vcc, 48, v213\n\t"
        "v_mul_f32 v217, 0x3e8b97f6, v215\n\t"
        "s_lshl_b32 s38, s54, 10\n\t"
        "v_cndmask_b32 v220, 0, 1.0, vcc\n\t"
        "s_add_u32 s38, s38, s47\n\t"
        "v_pk_mul_f32 v[8:9], v[200:201], v[214:215] op_sel:[0,1] op_sel_hi:[1,1]\n\t"
        "v_pk_mul_f32 v[10:11], v[202:203], v[214:215] op_sel:[0,1] op_sel_hi:[1,1]\n\t"
        "v_pk_fma_f32 v[12:13], v[204:205], v[214:215], v[218:219] op_sel:[0,1,0] op_sel_hi:[1,1,0]\n\t"
        "v_pk_fma_f32 v[14:15], v[206:207], v[214:215], v[218:219] op_sel:[0,1,0] op_sel_hi:[1,1,1]\n\t"
        "v_pk_fma_f32 v[120:121], v[120:121], v[214:215], v[8:9] op_sel_hi:[1,0,1]\n\t"
        "v_pk_fma_f32 v[122:123], v[122:123], v[214:215], v[10:11] op_sel_hi:[1,0,1]\n\t"
        "v_pk_fma_f32 v[124:125], v[124:125], v[214:215], v[12:13] op_sel_hi:[1,0,1]\n\t"
        "v_pk_fma_f32 v[126:127], v[126:127], v[214:215], v[14:15] op_sel_hi:[1,0,1]\n\t"
        "v_cvt_pk_bf16_f32 v136, v120, v121\n\t"
        "v_cvt_pk_bf16_f32 v137, v122, v123\n\t"
        "v_cvt_pk_bf16_f32 v138, v124, v125\n\t"
        "v_cvt_pk_bf16_f32 v139, v126, v127\n\t"
        "v_cmp_eq_u32 s[48:49], 48, v225\n\t"
        "v_mov_b32 v188, 0x3f803f80\n\t"
        "v_mov_b32 v189, 0x3f80\n\t"
        "v_mov_b32 v183, 0\n\t"
        "v_cndmask_b32 v138, v138, v188, s[48:49]\n\t"
        "v_cndmask_b32 v139, v139, v189, s[48:49]\n\t"
        "buffer_store_dwordx4 v[136:139], v231, s[16:19], s47 offen sc1\n\t"
        "s_waitcnt lgkmcnt(0)\n\t"
        "v_pk_mul_f32 v[16:17], v[4:5], v[208:209] op_sel_hi:[1,0]\n\t"
        "v_pk_fma_f32 v[20:21], v[4:5], v[210:211], v[220:221] op_sel_hi:[1,0,0]\n\t"
        "v_pk_mul_f32 v[18:19], v[6:7], v[208:209] op_sel_hi:[1,0]\n\t"
        "v_pk_fma_f32 v[22:23], v[6:7], v[210:211], v[220:221] op_sel_hi:[1,0,0]\n\t"
        "v_pk_fma_f32 v[128:129], v[128:129], v[0:1], v[16:17]\n\t"
        "v_pk_fma_f32 v[130:131], v[130:131], v[2:3], v[18:19]\n\t"
        "v_pk_fma_f32 v[132:133], v[132:133], v[0:1], v[20:21]\n\t"
        "v_pk_fma_f32 v[134:135], v[134:135], v[2:3], v[22:23]\n\t"
        "v_cvt_pk_bf16_f32 v140, v128, v129\n\t"
        "v_cvt_pk_bf16_f32 v141, v130, v131\n\t"
        "v_cvt_pk_bf16_f32 v142, v132, v133\n\t"
        "v_cvt_pk_bf16_f32 v143, v134, v135\n\t"
        "v_pk_mul_f32 v[8:9], v[192:193], v[216:217] op_sel:[0,1] op_sel_hi:[1,1]\n\t"
        "v_pk_mul_f32 v[10:11], v[194:195], v[216:217] op_sel:[0,1] op_sel_hi:[1,1]\n\t"
        "v_pk_mul_f32 v[12:13], v[196:197], v[216:217] op_sel:[0,1] op_sel_hi:[1,1]\n\t"
        "v_pk_mul_f32 v[14:15], v[198:199], v[216:217] op_sel:[0,1] op_sel_hi:[1,1]\n\t"
        "v_cndmask_b32 v189, v183, v188, vcc\n\t"
        "v_cmp_le_u32 vcc, 48, v213\n\t"
        "s_nop 1\n\t"
        "v_cndmask_b32 v142, v142, v189, vcc\n\t"
        "v_cndmask_b32 v143, v143, v189, vcc\n\t"
        "s_nop 0\n\t"
        "v_permlane32_swap_b32 v140, v142\n\t"
        "v_permlane32_swap_b32 v141, v143\n\t"
        "v_pk_fma_f32 v[112:113], v[112:113], v[216:217], v[8:9] op_sel_hi:[1,0,1]\n\t"
        "v_pk_fma_f32 v[114:115], v[114:115], v[216:217], v[10:11] op_sel_hi:[1,0,1]\n\t"
        "v_pk_fma_f32 v[116:117], v[116:117], v[216:217], v[12:13] op_sel_hi:[1,0,1]\n\t"
        "v_pk_fma_f32 v[118:119], v[118:119], v[216:217], v[14:15] op_sel_hi:[1,0,1]\n\t"
        "buffer_store_dwordx4 v[140:143], v224, s[16:19], s38 offen offset:2048 sc1\n\t"
        "v_cvt_pk_bf16_f32 v144, v112, v113\n\t"
        "v_cvt_pk_bf16_f32 v145, v114, v115\n\t"
        "v_cvt_pk_bf16_f32 v146, v116, v117\n\t"
        "v_cvt_pk_bf16_f32 v147, v118, v119\n\t"
        "v_cndmask_b32 v146, v146, v183, s[48:49]\n\t"
        "v_cndmask_b32 v147, v147, v183, s[48:49]\n\t"
        "buffer_store_dwordx4 v[144:147], v231, s[16:19], s33 offen sc1\n\t"
        "s_waitcnt vmcnt(3)\n\t"
        "v_cmp_ne_u64 vcc, s[10:11], v[232:233]\n\t"
        "s_nop 0\n\t"
        "s_cbranch_vccnz sp_16_%=\n\t"
        "bk_17_%=:\n\t"
        "s_add_u32 s63, s44, s28\n\t"
        "s_add_u32 s63, s63, 4390912\n\t"
        "buffer_load_dwordx4 v[96:99], v247, s[16:19], s63 offen offset:0 sc1\n\t"
        "buffer_load_dwordx4 v[100:103], v247, s[16:19], s63 offen offset:1024 sc1\n\t"
        "s_add_u32 s34, s45, s28\n\t"
        "s_add_u32 s34, s34, 2293760\n\t"
        "buffer_load_dwordx4 v[64:67], v247, s[16:19], s34 offen offset:0 sc1\n\t"
        "buffer_load_dwordx4 v[68:71], v247, s[16:19], s34 offen offset:1024 sc1\n\t"
        "s_add_u32 s34, s45, s28\n\t"
        "s_add_u32 s34, s34, 2293760\n\t"
        "s_xor_b32 s34, s34, 2048\n\t"
        "buffer_load_dwordx4 v[72:75], v247, s[16:19], s34 offen offset:0 sc1\n\t"
        "buffer_load_dwordx4 v[76:79], v247, s[16:19], s34 offen offset:1024 sc1\n\t"
        "buffer_load_dwordx4 v[104:107], v246, s[16:19], s63 offen offset:2048 sc1\n\t"
        "buffer_load_dwordx4 v[108:111], v246, s[16:19], s63 offen offset:3072 sc1\n\t"
        "s_mov_b32 s47, s63\n\t"
        "s_add_u32 s47, s47, 4096\n\t"
        "buffer_load_dwordx4 v[112:115], v247, s[16:19], s47 offen offset:0 sc1\n\t"
        "buffer_load_dwordx4 v[116:119], v247, s[16:19], s47 offen offset:1024 sc1\n\t"
        "buffer_load_dwordx4 v[120:123], v246, s[16:19], s47 offen offset:2048 sc1\n\t"
        "buffer_load_dwordx4 v[124:127], v246, s[16:19], s47 offen offset:3072 sc1\n\t"
        "v_lshrrev_b32 v223, 2, v239\n\t"
        "v_lshrrev_b32 v222, 4, v244\n\t"
        "v_and_b32 v228, 31, v223\n\t"
        "v_and_b32 v227, 15, v223\n\t"
        "v_mul_lo_u32 v227, v227, s78\n\t"
        "v_lshrrev_b32 v226, 4, v223\n\t"
        "v_mul_lo_u32 v226, v226, s79\n\t"
        "v_add_u32 v227, v227, v226\n\t"
        "v_and_b32 v0, 15, v223\n\t"
        "v_lshrrev_b32 v1, 2, v0\n\t"
        "v_lshl_add_u32 v1, v222, 2, v1\n\t"
        "v_lshlrev_b32 v1, 6, v1\n\t"
        "v_and_b32 v0, 3, v0\n\t"
        "v_lshl_add_u32 v1, v0, 3, v1\n\t"
        "v_bfe_u32 v0, v223, 4, 1\n\t"
        "v_lshl_add_u32 v1, v0, 5, v1\n\t"
        "s_mul_i32 s38, s26, 7168\n\t"
        "s_and_b32 s39, s74, 8\n\t"
        "s_add_u32 s38, s38, s39\n\t"
        "s_add_u32 s38, s38, 114688\n\t"
        "v_add_u32 v1, s38, v1\n\t"
        "s_and_b32 s74, s74, 7\n\t"
        "v_mov_b32 v2, 0\n\t"
        "v_mov_b32 v3, 0\n\t"
        "s_waitcnt vmcnt(15)\n\t"
        "s_cmp_eq_u32 s74, 4\n\t"
        "s_cbranch_scc1 rn_22_%=\n\t"
        "s_cmp_eq_u32 s74, 2\n\t"
        "s_cbranch_scc1 ro_19_%=\n\t"
        "s_cmp_eq_u32 s72, 0\n\t"
        "s_cbranch_scc1 r2_23_%=\n\t"
        "ds_read_b64_tr_b16 v[8:9], v1 offset:0\n\t"
        "ds_read_b64_tr_b16 v[10:11], v1 offset:512\n\t"
        "ds_read_b64_tr_b16 v[12:13], v1 offset:1024\n\t"
        "ds_read_b64_tr_b16 v[14:15], v1 offset:1536\n\t"
        "ds_read_b64_tr_b16 v[16:17], v1 offset:2048\n\t"
        "ds_read_b64_tr_b16 v[18:19], v1 offset:2560\n\t"
        "ds_read_b64_tr_b16 v[20:21], v1 offset:3072\n\t"
        "ds_read_b64_tr_b16 v[22:23], v1 offset:3584\n\t"
        "ds_read_b64_tr_b16 v[24:25], v1 offset:4096\n\t"
        "ds_read_b64_tr_b16 v[26:27], v1 offset:4608\n\t"
        "ds_read_b64_tr_b16 v[28:29], v1 offset:5120\n\t"
        "ds_read_b64_tr_b16 v[30:31], v1 offset:5632\n\t"
        "ds_read_b64_tr_b16 v[32:33], v1 offset:6144\n\t"
        "ds_read_b64_tr_b16 v[34:35], v1 offset:6656\n\t"
        "s_branch rd_24_%=\n\t"
        "r2_23_%=:\n\t"
        "s_barrier\n\t"
        "s_lshl_b32 s38, s54, 9\n\t"
        "v_subrev_u32 v1, s38, v1\n\t"
        "s_lshl_b32 s38, s54, 10\n\t"
        "v_add_u32 v7, s38, v1\n\t"
        "ds_read_b64_tr_b16 v[8:9], v1 offset:0\n\t"
        "ds_read_b64_tr_b16 v[10:11], v1 offset:1024\n\t"
        "ds_read_b64_tr_b16 v[12:13], v7 offset:512\n\t"
        "ds_read_b64_tr_b16 v[14:15], v7 offset:1536\n\t"
        "ds_read_b64_tr_b16 v[16:17], v1 offset:2048\n\t"
        "ds_read_b64_tr_b16 v[18:19], v1 offset:3072\n\t"
        "ds_read_b64_tr_b16 v[20:21], v7 offset:2560\n\t"
        "ds_read_b64_tr_b16 v[22:23], v7 offset:3584\n\t"
        "ds_read_b64_tr_b16 v[24:25], v1 offset:4096\n\t"
        "ds_read_b64_tr_b16 v[26:27], v1 offset:5120\n\t"
        "ds_read_b64_tr_b16 v[28:29], v7 offset:4608\n\t"
        "ds_read_b64_tr_b16 v[30:31], v7 offset:5632\n\t"
        "ds_read_b64_tr_b16 v[32:33], v1 offset:6144\n\t"
        "ds_read_b64_tr_b16 v[34:35], v1 offset:7168\n\t"
        "rd_24_%=:\n\t"
        "s_mul_i32 s38, s26, 7\n\t"
        "s_mul_i32 s38, s38, s71\n\t"
        "s_add_u32 s70, s70, s38\n\t"
        "v_cmp_le_u32 vcc, 28, v228\n\t"
        "s_cmp_eq_u32 s74, 1\n\t"
        "s_cselect_b64 vcc, vcc, 0\n\t"
        "s_waitcnt lgkmcnt(13)\n\t"
        "v_cndmask_b32 v8, v8, v2, vcc\n\t"
        "v_cndmask_b32 v9, v9, v2, vcc\n\t"
        "s_waitcnt lgkmcnt(12)\n\t"
        "v_cndmask_b32 v10, v10, v2, vcc\n\t"
        "v_cndmask_b32 v11, v11, v2, vcc\n\t"
        "buffer_store_dwordx4 v[8:11], v247, s[16:19], s70 offen sc1\n\t"
        "s_add_u32 s70, s70, s71\n\t"
        "s_waitcnt lgkmcnt(11)\n\t"
        "v_cndmask_b32 v12, v12, v2, vcc\n\t"
        "v_cndmask_b32 v13, v13, v2, vcc\n\t"
        "s_waitcnt lgkmcnt(10)\n\t"
        "v_cndmask_b32 v14, v14, v2, vcc\n\t"
        "v_cndmask_b32 v15, v15, v2, vcc\n\t"
        "buffer_store_dwordx4 v[12:15], v247, s[16:19], s70 offen sc1\n\t"
        "s_add_u32 s70, s70, s71\n\t"
        "s_waitcnt lgkmcnt(9)\n\t"
        "v_cndmask_b32 v16, v16, v2, vcc\n\t"
        "v_cndmask_b32 v17, v17, v2, vcc\n\t"
        "s_waitcnt lgkmcnt(8)\n\t"
        "v_cndmask_b32 v18, v18, v2, vcc\n\t"
        "v_cndmask_b32 v19, v19, v2, vcc\n\t"
        "buffer_store_dwordx4 v[16:19], v247, s[16:19], s70 offen sc1\n\t"
        "s_add_u32 s70, s70, s71\n\t"
        "s_waitcnt lgkmcnt(7)\n\t"
        "v_cndmask_b32 v20, v20, v2, vcc\n\t"
        "v_cndmask_b32 v21, v21, v2, vcc\n\t"
        "s_waitcnt lgkmcnt(6)\n\t"
        "v_cndmask_b32 v22, v22, v2, vcc\n\t"
        "v_cndmask_b32 v23, v23, v2, vcc\n\t"
        "buffer_store_dwordx4 v[20:23], v247, s[16:19], s70 offen sc1\n\t"
        "s_add_u32 s70, s70, s71\n\t"
        "s_waitcnt lgkmcnt(5)\n\t"
        "v_cndmask_b32 v24, v24, v2, vcc\n\t"
        "v_cndmask_b32 v25, v25, v2, vcc\n\t"
        "s_waitcnt lgkmcnt(4)\n\t"
        "v_cndmask_b32 v26, v26, v2, vcc\n\t"
        "v_cndmask_b32 v27, v27, v2, vcc\n\t"
        "buffer_store_dwordx4 v[24:27], v247, s[16:19], s70 offen sc1\n\t"
        "s_add_u32 s70, s70, s71\n\t"
        "s_waitcnt lgkmcnt(3)\n\t"
        "v_cndmask_b32 v28, v28, v2, vcc\n\t"
        "v_cndmask_b32 v29, v29, v2, vcc\n\t"
        "s_waitcnt lgkmcnt(2)\n\t"
        "v_cndmask_b32 v30, v30, v2, vcc\n\t"
        "v_cndmask_b32 v31, v31, v2, vcc\n\t"
        "buffer_store_dwordx4 v[28:31], v247, s[16:19], s70 offen sc1\n\t"
        "s_add_u32 s70, s70, s71\n\t"
        "s_waitcnt lgkmcnt(1)\n\t"
        "v_cndmask_b32 v32, v32, v2, vcc\n\t"
        "v_cndmask_b32 v33, v33, v2, vcc\n\t"
        "s_waitcnt lgkmcnt(0)\n\t"
        "v_cndmask_b32 v34, v34, v2, vcc\n\t"
        "v_cndmask_b32 v35, v35, v2, vcc\n\t"
        "buffer_store_dwordx4 v[32:35], v247, s[16:19], s70 offen sc1\n\t"
        "s_cmp_eq_u32 s72, 0\n\t"
        "s_cbranch_scc1 rp_21_%=\n\t"
        "v_lshlrev_b32 v4, 16, v8\n\t"
        "v_and_b32 v5, 0xffff0000, v8\n\t"
        "v_pk_add_f32 v[2:3], v[2:3], v[4:5]\n\t"
        "v_lshlrev_b32 v4, 16, v9\n\t"
        "v_and_b32 v5, 0xffff0000, v9\n\t"
        "v_pk_add_f32 v[2:3], v[2:3], v[4:5]\n\t"
        "v_lshlrev_b32 v4, 16, v10\n\t"
        "v_and_b32 v5, 0xffff0000, v10\n\t"
        "v_pk_add_f32 v[2:3], v[2:3], v[4:5]\n\t"
        "v_lshlrev_b32 v4, 16, v11\n\t"
        "v_and_b32 v5, 0xffff0000, v11\n\t"
        "v_pk_add_f32 v[2:3], v[2:3], v[4:5]\n\t"
        "v_lshlrev_b32 v4, 16, v12\n\t"
        "v_and_b32 v5, 0xffff0000, v12\n\t"
        "v_pk_add_f32 v[2:3], v[2:3], v[4:5]\n\t"
        "v_lshlrev_b32 v4, 16, v13\n\t"
        "v_and_b32 v5, 0xffff0000, v13\n\t"
        "v_pk_add_f32 v[2:3], v[2:3], v[4:5]\n\t"
        "v_lshlrev_b32 v4, 16, v14\n\t"
        "v_and_b32 v5, 0xffff0000, v14\n\t"
        "v_pk_add_f32 v[2:3], v[2:3], v[4:5]\n\t"
        "v_lshlrev_b32 v4, 16, v15\n\t"
        "v_and_b32 v5, 0xffff0000, v15\n\t"
        "v_pk_add_f32 v[2:3], v[2:3], v[4:5]\n\t"
        "v_lshlrev_b32 v4, 16, v16\n\t"
        "v_and_b32 v5, 0xffff0000, v16\n\t"
        "v_pk_add_f32 v[2:3], v[2:3], v[4:5]\n\t"
        "v_lshlrev_b32 v4, 16, v17\n\t"
        "v_and_b32 v5, 0xffff0000, v17\n\t"
        "v_pk_add_f32 v[2:3], v[2:3], v[4:5]\n\t"
        "v_lshlrev_b32 v4, 16, v18\n\t"
        "v_and_b32 v5, 0xffff0000, v18\n\t"
        "v_pk_add_f32 v[2:3], v[2:3], v[4:5]\n\t"
        "v_lshlrev_b32 v4, 16, v19\n\t"
        "v_and_b32 v5, 0xffff0000, v19\n\t"
        "v_pk_add_f32 v[2:3], v[2:3], v[4:5]\n\t"
        "v_lshlrev_b32 v4, 16, v20\n\t"
        "v_and_b32 v5, 0xffff0000, v20\n\t"
        "v_pk_add_f32 v[2:3], v[2:3], v[4:5]\n\t"
        "v_lshlrev_b32 v4, 16, v21\n\t"
        "v_and_b32 v5, 0xffff0000, v21\n\t"
        "v_pk_add_f32 v[2:3], v[2:3], v[4:5]\n\t"
        "v_lshlrev_b32 v4, 16, v22\n\t"
        "v_and_b32 v5, 0xffff0000, v22\n\t"
        "v_pk_add_f32 v[2:3], v[2:3], v[4:5]\n\t"
        "v_lshlrev_b32 v4, 16, v23\n\t"
        "v_and_b32 v5, 0xffff0000, v23\n\t"
        "v_pk_add_f32 v[2:3], v[2:3], v[4:5]\n\t"
        "v_lshlrev_b32 v4, 16, v24\n\t"
        "v_and_b32 v5, 0xffff0000, v24\n\t"
        "v_pk_add_f32 v[2:3], v[2:3], v[4:5]\n\t"
        "v_lshlrev_b32 v4, 16, v25\n\t"
        "v_and_b32 v5, 0xffff0000, v25\n\t"
        "v_pk_add_f32 v[2:3], v[2:3], v[4:5]\n\t"
        "v_lshlrev_b32 v4, 16, v26\n\t"
        "v_and_b32 v5, 0xffff0000, v26\n\t"
        "v_pk_add_f32 v[2:3], v[2:3], v[4:5]\n\t"
        "v_lshlrev_b32 v4, 16, v27\n\t"
        "v_and_b32 v5, 0xffff0000, v27\n\t"
        "v_pk_add_f32 v[2:3], v[2:3], v[4:5]\n\t"
        "v_lshlrev_b32 v4, 16, v28\n\t"
        "v_and_b32 v5, 0xffff0000, v28\n\t"
        "v_pk_add_f32 v[2:3], v[2:3], v[4:5]\n\t"
        "v_lshlrev_b32 v4, 16, v29\n\t"
        "v_and_b32 v5, 0xffff0000, v29\n\t"
        "v_pk_add_f32 v[2:3], v[2:3], v[4:5]\n\t"
        "v_lshlrev_b32 v4, 16, v30\n\t"
        "v_and_b32 v5, 0xffff0000, v30\n\t"
        "v_pk_add_f32 v[2:3], v[2:3], v[4:5]\n\t"
        "v_lshlrev_b32 v4, 16, v31\n\t"
        "v_and_b32 v5, 0xffff0000, v31\n\t"
        "v_pk_add_f32 v[2:3], v[2:3], v[4:5]\n\t"
        "v_lshlrev_b32 v4, 16, v32\n\t"
        "v_and_b32 v5, 0xffff0000, v32\n\t"
        "v_pk_add_f32 v[2:3], v[2:3], v[4:5]\n\t"
        "v_lshlrev_b32 v4, 16, v33\n\t"
        "v_and_b32 v5, 0xffff0000, v33\n\t"
        "v_pk_add_f32 v[2:3], v[2:3], v[4:5]\n\t"
        "v_lshlrev_b32 v4, 16, v34\n\t"
        "v_and_b32 v5, 0xffff0000, v34\n\t"
        "v_pk_add_f32 v[2:3], v[2:3], v[4:5]\n\t"
        "v_lshlrev_b32 v4, 16, v35\n\t"
        "v_and_b32 v5, 0xffff0000, v35\n\t"
        "v_pk_add_f32 v[2:3], v[2:3], v[4:5]\n\t"
        "v_add_f32 v2, v2, v3\n\t"
        "v_mov_b32 v3, v2\n\t"
        "s_lshl_b32 s38, s26, 7\n\t"
        "s_add_u32 s38, s38, 147456\n\t"
        "v_permlane32_swap_b32 v2, v3\n\t"
        "v_lshlrev_b32 v4, 2, v228\n\t"
        "v_add_u32 v5, s38, v4\n\t"
        "v_add_f32 v2, v2, v3\n\t"
        "ds_write_b32 v5, v2\n\t"
        "v_add_u32 v5, 147456, v4\n\t"
        "s_waitcnt lgkmcnt(0)\n\t"
        "s_barrier\n\t"
        "s_cmp_lg_u32 s26, 0\n\t"
        "s_cbranch_scc1 rp_21_%=\n\t"
        "ds_read_b32 v40, v5 offset:0\n\t"
        "ds_read_b32 v41, v5 offset:128\n\t"
        "ds_read_b32 v42, v5 offset:256\n\t"
        "ds_read_b32 v43, v5 offset:384\n\t"
        "v_lshlrev_b32 v230, 16, v230\n\t"
        "v_mul_f32 v230, 0x3fb8aa3b, v230\n\t"
        "s_waitcnt lgkmcnt(2)\n\t"
        "v_add_f32 v40, v40, v41\n\t"
        "s_waitcnt lgkmcnt(0)\n\t"
        "v_add_f32 v42, v42, v43\n\t"
        "v_add_f32 v40, v40, v42\n\t"
        "v_cmp_lt_u32 vcc, 15, v244\n\t"
        "s_cmp_eq_u32 s74, 1\n\t"
        "s_cselect_b64 exec, 0xffffffff, -1\n\t"
        "v_cndmask_b32 v40, v40, v230, vcc\n\t"
        "buffer_store_dword v40, v239, s[16:19], s72 offen sc1\n\t"
        "s_mov_b64 exec, -1\n\t"
        "s_branch rp_21_%=\n\t"
        "ro_19_%=:\n\t"
        "ds_read_b64_tr_b16 v[8:9], v1 offset:0\n\t"
        "ds_read_b64_tr_b16 v[10:11], v1 offset:512\n\t"
        "ds_read_b64_tr_b16 v[12:13], v1 offset:1024\n\t"
        "ds_read_b64_tr_b16 v[14:15], v1 offset:1536\n\t"
        "ds_read_b64_tr_b16 v[16:17], v1 offset:1792\n\t"
        "ds_read_b64_tr_b16 v[18:19], v1 offset:2304\n\t"
        "ds_read_b64_tr_b16 v[20:21], v1 offset:2816\n\t"
        "ds_read_b64_tr_b16 v[22:23], v1 offset:3328\n\t"
        "ds_read_b64_tr_b16 v[24:25], v1 offset:3584\n\t"
        "ds_read_b64_tr_b16 v[26:27], v1 offset:4096\n\t"
        "ds_read_b64_tr_b16 v[28:29], v1 offset:4608\n\t"
        "ds_read_b64_tr_b16 v[30:31], v1 offset:5120\n\t"
        "ds_read_b64_tr_b16 v[32:33], v1 offset:5376\n\t"
        "ds_read_b64_tr_b16 v[34:35], v1 offset:5888\n\t"
        "ds_read_b64_tr_b16 v[36:37], v1 offset:6400\n\t"
        "s_waitcnt lgkmcnt(14)\n\t"
        "ds_read_b64_tr_b16 v[38:39], v1 offset:6912\n\t"
        "s_lshl_b32 s38, s26, 13\n\t"
        "s_add_u32 s70, s70, s38\n\t"
        "v_cmp_lt_u32 vcc, 15, v244\n\t"
        "s_waitcnt lgkmcnt(14)\n\t"
        "buffer_store_dwordx4 v[8:11], v247, s[16:19], s70 offen offset:0 sc1\n\t"
        "s_waitcnt lgkmcnt(12)\n\t"
        "v_cndmask_b32 v14, v14, v2, vcc\n\t"
        "v_cndmask_b32 v15, v15, v2, vcc\n\t"
        "buffer_store_dwordx4 v[12:15], v247, s[16:19], s70 offen offset:1024 sc1\n\t"
        "s_waitcnt lgkmcnt(10)\n\t"
        "buffer_store_dwordx4 v[16:19], v247, s[16:19], s70 offen offset:2048 sc1\n\t"
        "s_waitcnt lgkmcnt(8)\n\t"
        "v_cndmask_b32 v22, v22, v2, vcc\n\t"
        "v_cndmask_b32 v23, v23, v2, vcc\n\t"
        "buffer_store_dwordx4 v[20:23], v247, s[16:19], s70 offen offset:3072 sc1\n\t"
        "s_add_u32 s70, s70, 4096\n\t"
        "s_waitcnt lgkmcnt(6)\n\t"
        "buffer_store_dwordx4 v[24:27], v247, s[16:19], s70 offen offset:0 sc1\n\t"
        "s_waitcnt lgkmcnt(4)\n\t"
        "v_cndmask_b32 v30, v30, v2, vcc\n\t"
        "v_cndmask_b32 v31, v31, v2, vcc\n\t"
        "buffer_store_dwordx4 v[28:31], v247, s[16:19], s70 offen offset:1024 sc1\n\t"
        "s_waitcnt lgkmcnt(2)\n\t"
        "buffer_store_dwordx4 v[32:35], v247, s[16:19], s70 offen offset:2048 sc1\n\t"
        "s_waitcnt lgkmcnt(0)\n\t"
        "v_cndmask_b32 v38, v38, v2, vcc\n\t"
        "v_cndmask_b32 v39, v39, v2, vcc\n\t"
        "buffer_store_dwordx4 v[36:39], v247, s[16:19], s70 offen offset:3072 sc1\n\t"
        "s_branch rp_21_%=\n\t"
        "rn_22_%=:\n\t"
        "s_waitcnt vmcnt(4)\n\t"
        "rp_21_%=:\n\t"
        "s_mul_i32 s38, s27, 147456\n\t"
        "s_lshl_b32 s39, s25, 11\n\t"
        "s_lshl_b32 s40, s24, 3\n\t"
        "v_lshlrev_b32 v236, 4, v238\n\t"
        "s_add_u32 s38, s38, s39\n\t"
        "s_add_u32 s38, s38, s40\n\t"
        "s_add_u32 s38, s38, 40960\n\t"
        "s_waitcnt vmcnt(12) lgkmcnt(0)\n\t"
        "s_barrier\n\t"
        "s_cmp_lg_u32 s26, 0\n\t"
        "s_cbranch_scc1 sig_25_%=\n\t"
        "s_mov_b64 exec, 0xffff\n\t"
        "buffer_atomic_swap_x2 v[234:235], v236, s[16:19], s38 offen\n\t"
        "s_mov_b64 exec, -1\n\t"
        "sig_25_%=:\n\t"
        "s_branch att_26_%=\n\t"
        "layer_1_%=:\n\t"
        "s_lshl_b32 s38, s54, 8\n\t"
        "v_subrev_u32 v225, s38, v231\n\t"
        "v_lshlrev_b32 v222, 7, v244\n\t"
        "v_add_u32 v222, v222, v225\n\t"
        "v_add_u32 v223, 43008, v222\n\t"
        "v_lshrrev_b32 v225, 2, v239\n\t"
        "v_and_b32 v225, 0x30, v225\n\t"
        "v_and_b32 v213, 0x3c, v239\n\t"
        "v_bfe_u32 v224, v225, 4, 1\n\t"
        "v_lshlrev_b32 v224, 9, v224\n\t"
        "v_lshl_add_u32 v224, v244, 4, v224\n\t"
        "v_lshl_add_u32 v224, v213, 2, v224\n\t"
        "s_mul_i32 s34, s25, 384\n\t"
        "s_add_u32 s34, s34, 5414912\n\t"
        "ds_read_b128 v[144:147], v222 offset:0\n\t"
        "ds_read_b128 v[148:151], v222 offset:256\n\t"
        "ds_read_b128 v[152:155], v222 offset:1024\n\t"
        "ds_read_b128 v[156:159], v222 offset:1280\n\t"
        "ds_read_b128 v[160:163], v222 offset:2048\n\t"
        "ds_read_b128 v[164:167], v222 offset:2304\n\t"
        "ds_read_b128 v[168:171], v222 offset:6144\n\t"
        "ds_read_b128 v[172:175], v222 offset:6400\n\t"
        "ds_read_b128 v[176:179], v222 offset:7168\n\t"
        "ds_read_b128 v[180:183], v222 offset:7424\n\t"
        "ds_read_b128 v[184:187], v222 offset:8192\n\t"
        "s_waitcnt lgkmcnt(7)\n\t"
        "s_waitcnt vmcnt(7)\n\t"
        "v_mfma_f32_16x16x32_bf16 v[112:115], v[144:147], v[56:59], 0\n\t"
        "ds_read_b128 v[188:191], v222 offset:8448\n\t"
        "s_add_u32 s62, s62, 4096\n\t"
        "buffer_load_dwordx4 v[80:83], v231, s[16:19], s62 offen offset:0 sc1\n\t"
        "v_mfma_f32_16x16x32_bf16 v[116:119], v[148:151], v[56:59], 0\n\t"
        "ds_read_b128 v[144:147], v222 offset:12288\n\t"
        "v_mfma_f32_16x16x32_bf16 v[120:123], v[152:155], v[56:59], 0\n\t"
        "ds_read_b128 v[148:151], v222 offset:12544\n\t"
        "v_mfma_f32_16x16x32_bf16 v[124:127], v[156:159], v[56:59], 0\n\t"
        "ds_read_b128 v[152:155], v222 offset:13312\n\t"
        "buffer_load_dwordx4 v[84:87], v231, s[16:19], s62 offen offset:2048 sc1\n\t"
        "s_waitcnt lgkmcnt(7)\n\t"
        "v_mfma_f32_16x16x32_bf16 v[128:131], v[56:59], v[160:163], 0\n\t"
        "ds_read_b128 v[156:159], v222 offset:13568\n\t"
        "v_mfma_f32_16x16x32_bf16 v[132:135], v[56:59], v[164:167], 0\n\t"
        "ds_read_b128 v[160:163], v222 offset:14336\n\t"
        "s_waitcnt vmcnt(8)\n\t"
        "v_mfma_f32_16x16x32_bf16 v[112:115], v[168:171], v[60:63], v[112:115]\n\t"
        "ds_read_b128 v[164:167], v222 offset:14592\n\t"
        "s_add_u32 s62, s62, 4096\n\t"
        "buffer_load_dwordx4 v[88:91], v231, s[16:19], s62 offen offset:0 sc1\n\t"
        "v_mfma_f32_16x16x32_bf16 v[116:119], v[172:175], v[60:63], v[116:119]\n\t"
        "ds_read_b128 v[168:171], v222 offset:18432\n\t"
        "s_waitcnt lgkmcnt(7)\n\t"
        "v_mfma_f32_16x16x32_bf16 v[120:123], v[176:179], v[60:63], v[120:123]\n\t"
        "ds_read_b128 v[172:175], v222 offset:18688\n\t"
        "v_mfma_f32_16x16x32_bf16 v[124:127], v[180:183], v[60:63], v[124:127]\n\t"
        "ds_read_b128 v[176:179], v222 offset:19456\n\t"
        "buffer_load_dwordx4 v[92:95], v231, s[16:19], s62 offen offset:2048 sc1\n\t"
        "v_mfma_f32_16x16x32_bf16 v[128:131], v[60:63], v[184:187], v[128:131]\n\t"
        "ds_read_b128 v[180:183], v222 offset:19712\n\t"
        "v_mfma_f32_16x16x32_bf16 v[132:135], v[60:63], v[188:191], v[132:135]\n\t"
        "ds_read_b128 v[184:187], v222 offset:20480\n\t"
        "s_waitcnt lgkmcnt(7)\n\t"
        "s_waitcnt vmcnt(9)\n\t"
        "v_mfma_f32_16x16x32_bf16 v[112:115], v[144:147], v[64:67], v[112:115]\n\t"
        "ds_read_b128 v[188:191], v222 offset:20736\n\t"
        "s_add_u32 s62, s62, 4096\n\t"
        "buffer_load_dwordx4 v[96:99], v231, s[16:19], s62 offen offset:0 sc1\n\t"
        "v_mfma_f32_16x16x32_bf16 v[116:119], v[148:151], v[64:67], v[116:119]\n\t"
        "ds_read_b128 v[144:147], v222 offset:24576\n\t"
        "v_mfma_f32_16x16x32_bf16 v[120:123], v[152:155], v[64:67], v[120:123]\n\t"
        "ds_read_b128 v[148:151], v222 offset:24832\n\t"
        "v_mfma_f32_16x16x32_bf16 v[124:127], v[156:159], v[64:67], v[124:127]\n\t"
        "ds_read_b128 v[152:155], v222 offset:25600\n\t"
        "buffer_load_dwordx4 v[100:103], v231, s[16:19], s62 offen offset:2048 sc1\n\t"
        "s_waitcnt lgkmcnt(7)\n\t"
        "v_mfma_f32_16x16x32_bf16 v[128:131], v[64:67], v[160:163], v[128:131]\n\t"
        "ds_read_b128 v[156:159], v222 offset:25856\n\t"
        "v_mfma_f32_16x16x32_bf16 v[132:135], v[64:67], v[164:167], v[132:135]\n\t"
        "ds_read_b128 v[160:163], v222 offset:26624\n\t"
        "s_waitcnt vmcnt(10)\n\t"
        "v_mfma_f32_16x16x32_bf16 v[112:115], v[168:171], v[68:71], v[112:115]\n\t"
        "ds_read_b128 v[164:167], v222 offset:26880\n\t"
        "s_add_u32 s62, s62, 4096\n\t"
        "buffer_load_dwordx4 v[104:107], v231, s[16:19], s62 offen offset:0 sc1\n\t"
        "v_mfma_f32_16x16x32_bf16 v[116:119], v[172:175], v[68:71], v[116:119]\n\t"
        "ds_read_b128 v[168:171], v222 offset:30720\n\t"
        "s_waitcnt lgkmcnt(7)\n\t"
        "v_mfma_f32_16x16x32_bf16 v[120:123], v[176:179], v[68:71], v[120:123]\n\t"
        "ds_read_b128 v[172:175], v222 offset:30976\n\t"
        "v_mfma_f32_16x16x32_bf16 v[124:127], v[180:183], v[68:71], v[124:127]\n\t"
        "ds_read_b128 v[176:179], v222 offset:31744\n\t"
        "buffer_load_dwordx4 v[108:111], v231, s[16:19], s62 offen offset:2048 sc1\n\t"
        "v_mfma_f32_16x16x32_bf16 v[128:131], v[68:71], v[184:187], v[128:131]\n\t"
        "ds_read_b128 v[180:183], v222 offset:32000\n\t"
        "v_mfma_f32_16x16x32_bf16 v[132:135], v[68:71], v[188:191], v[132:135]\n\t"
        "ds_read_b128 v[184:187], v222 offset:32768\n\t"
        "s_waitcnt lgkmcnt(7)\n\t"
        "s_waitcnt vmcnt(11)\n\t"
        "v_mfma_f32_16x16x32_bf16 v[112:115], v[144:147], v[72:75], v[112:115]\n\t"
        "ds_read_b128 v[188:191], v222 offset:33024\n\t"
        "s_add_u32 s33, s30, s28\n\t"
        "s_add_u32 s33, s33, 1835008\n\t"
        "buffer_load_dwordx2 v[0:1], v245, s[16:19], s33 offen offset:0 sc1\n\t"
        "v_mfma_f32_16x16x32_bf16 v[116:119], v[148:151], v[72:75], v[116:119]\n\t"
        "ds_read_b128 v[144:147], v222 offset:36864\n\t"
        "v_mfma_f32_16x16x32_bf16 v[120:123], v[152:155], v[72:75], v[120:123]\n\t"
        "ds_read_b128 v[148:151], v222 offset:37120\n\t"
        "v_mfma_f32_16x16x32_bf16 v[124:127], v[156:159], v[72:75], v[124:127]\n\t"
        "ds_read_b128 v[152:155], v222 offset:37888\n\t"
        "buffer_load_dwordx2 v[2:3], v245, s[16:19], s33 offen offset:256 sc1\n\t"
        "s_waitcnt lgkmcnt(7)\n\t"
        "v_mfma_f32_16x16x32_bf16 v[128:131], v[72:75], v[160:163], v[128:131]\n\t"
        "ds_read_b128 v[156:159], v222 offset:38144\n\t"
        "v_mfma_f32_16x16x32_bf16 v[132:135], v[72:75], v[164:167], v[132:135]\n\t"
        "ds_read_b128 v[160:163], v222 offset:38912\n\t"
        "s_mul_i32 s38, s27, 147456\n\t"
        "s_lshl_b32 s39, s24, 7\n\t"
        "s_lshl_b32 s40, s25, 3\n\t"
        "s_add_u32 s38, s38, s39\n\t"
        "s_add_u32 s38, s38, s40\n\t"
        "s_add_u32 s38, s38, 2048\n\t"
        "s_waitcnt vmcnt(10)\n\t"
        "s_barrier\n\t"
        "s_cmp_lg_u32 s26, 0\n\t"
        "s_cbranch_scc1 sig_27_%=\n\t"
        "s_mov_b64 exec, 1\n\t"
        "buffer_atomic_swap_x2 v[234:235], off, s[16:19], s38\n\t"
        "s_mov_b64 exec, -1\n\t"
        "sig_27_%=:\n\t"
        "v_mfma_f32_16x16x32_bf16 v[112:115], v[168:171], v[76:79], v[112:115]\n\t"
        "ds_read_b128 v[164:167], v222 offset:39168\n\t"
        "buffer_load_dwordx2 v[4:5], v245, s[16:19], s33 offen offset:512 sc1\n\t"
        "v_mfma_f32_16x16x32_bf16 v[116:119], v[172:175], v[76:79], v[116:119]\n\t"
        "ds_read_b128 v[168:171], v223 offset:0\n\t"
        "s_waitcnt lgkmcnt(7)\n\t"
        "v_mfma_f32_16x16x32_bf16 v[120:123], v[176:179], v[76:79], v[120:123]\n\t"
        "ds_read_b128 v[172:175], v223 offset:256\n\t"
        "v_mfma_f32_16x16x32_bf16 v[124:127], v[180:183], v[76:79], v[124:127]\n\t"
        "ds_read_b128 v[176:179], v223 offset:1024\n\t"
        "buffer_load_dwordx2 v[6:7], v245, s[16:19], s33 offen offset:768 sc1\n\t"
        "v_mfma_f32_16x16x32_bf16 v[128:131], v[76:79], v[184:187], v[128:131]\n\t"
        "ds_read_b128 v[180:183], v223 offset:1280\n\t"
        "v_mfma_f32_16x16x32_bf16 v[132:135], v[76:79], v[188:191], v[132:135]\n\t"
        "ds_read_b128 v[184:187], v223 offset:2048\n\t"
        "s_waitcnt lgkmcnt(7)\n\t"
        "s_waitcnt vmcnt(11)\n\t"
        "v_mfma_f32_16x16x32_bf16 v[112:115], v[144:147], v[80:83], v[112:115]\n\t"
        "ds_read_b128 v[188:191], v223 offset:2304\n\t"
        "buffer_load_dwordx2 v[8:9], v245, s[16:19], s33 offen offset:1024 sc1\n\t"
        "v_mfma_f32_16x16x32_bf16 v[116:119], v[148:151], v[80:83], v[116:119]\n\t"
        "ds_read_b128 v[144:147], v223 offset:6144\n\t"
        "v_mfma_f32_16x16x32_bf16 v[120:123], v[152:155], v[80:83], v[120:123]\n\t"
        "ds_read_b128 v[148:151], v223 offset:6400\n\t"
        "v_mfma_f32_16x16x32_bf16 v[124:127], v[156:159], v[80:83], v[124:127]\n\t"
        "ds_read_b128 v[152:155], v223 offset:7168\n\t"
        "buffer_load_dwordx2 v[10:11], v245, s[16:19], s33 offen offset:1280 sc1\n\t"
        "s_waitcnt lgkmcnt(7)\n\t"
        "v_mfma_f32_16x16x32_bf16 v[128:131], v[80:83], v[160:163], v[128:131]\n\t"
        "ds_read_b128 v[156:159], v223 offset:7424\n\t"
        "v_mfma_f32_16x16x32_bf16 v[132:135], v[80:83], v[164:167], v[132:135]\n\t"
        "ds_read_b128 v[160:163], v223 offset:8192\n\t"
        "s_waitcnt vmcnt(12)\n\t"
        "v_mfma_f32_16x16x32_bf16 v[112:115], v[168:171], v[84:87], v[112:115]\n\t"
        "ds_read_b128 v[164:167], v223 offset:8448\n\t"
        "buffer_load_dwordx2 v[12:13], v245, s[16:19], s33 offen offset:1536 sc1\n\t"
        "v_mfma_f32_16x16x32_bf16 v[116:119], v[172:175], v[84:87], v[116:119]\n\t"
        "ds_read_b128 v[168:171], v223 offset:12288\n\t"
        "s_waitcnt lgkmcnt(7)\n\t"
        "v_mfma_f32_16x16x32_bf16 v[120:123], v[176:179], v[84:87], v[120:123]\n\t"
        "ds_read_b128 v[172:175], v223 offset:12544\n\t"
        "v_mfma_f32_16x16x32_bf16 v[124:127], v[180:183], v[84:87], v[124:127]\n\t"
        "ds_read_b128 v[176:179], v223 offset:13312\n\t"
        "buffer_load_dwordx4 v[192:195], v225, s[16:19], s34 offen offset:0 sc1\n\t"
        "v_mfma_f32_16x16x32_bf16 v[128:131], v[84:87], v[184:187], v[128:131]\n\t"
        "ds_read_b128 v[180:183], v223 offset:13568\n\t"
        "v_mfma_f32_16x16x32_bf16 v[132:135], v[84:87], v[188:191], v[132:135]\n\t"
        "ds_read_b128 v[184:187], v223 offset:14336\n\t"
        "s_waitcnt lgkmcnt(7)\n\t"
        "s_waitcnt vmcnt(13)\n\t"
        "v_mfma_f32_16x16x32_bf16 v[112:115], v[144:147], v[88:91], v[112:115]\n\t"
        "ds_read_b128 v[188:191], v223 offset:14592\n\t"
        "buffer_load_dwordx4 v[196:199], v225, s[16:19], s34 offen offset:64 sc1\n\t"
        "v_mfma_f32_16x16x32_bf16 v[116:119], v[148:151], v[88:91], v[116:119]\n\t"
        "ds_read_b128 v[144:147], v223 offset:18432\n\t"
        "v_mfma_f32_16x16x32_bf16 v[120:123], v[152:155], v[88:91], v[120:123]\n\t"
        "ds_read_b128 v[148:151], v223 offset:18688\n\t"
        "v_mfma_f32_16x16x32_bf16 v[124:127], v[156:159], v[88:91], v[124:127]\n\t"
        "ds_read_b128 v[152:155], v223 offset:19456\n\t"
        "buffer_load_dwordx4 v[200:203], v225, s[16:19], s34 offen offset:128 sc1\n\t"
        "s_waitcnt lgkmcnt(7)\n\t"
        "v_mfma_f32_16x16x32_bf16 v[128:131], v[88:91], v[160:163], v[128:131]\n\t"
        "ds_read_b128 v[156:159], v223 offset:19712\n\t"
        "v_mfma_f32_16x16x32_bf16 v[132:135], v[88:91], v[164:167], v[132:135]\n\t"
        "ds_read_b128 v[160:163], v223 offset:20480\n\t"
        "s_waitcnt vmcnt(14)\n\t"
        "v_mfma_f32_16x16x32_bf16 v[112:115], v[168:171], v[92:95], v[112:115]\n\t"
        "ds_read_b128 v[164:167], v223 offset:20736\n\t"
        "buffer_load_dwordx4 v[204:207], v225, s[16:19], s34 offen offset:192 sc1\n\t"
        "v_mfma_f32_16x16x32_bf16 v[116:119], v[172:175], v[92:95], v[116:119]\n\t"
        "ds_read_b128 v[168:171], v223 offset:24576\n\t"
        "s_waitcnt lgkmcnt(7)\n\t"
        "v_mfma_f32_16x16x32_bf16 v[120:123], v[176:179], v[92:95], v[120:123]\n\t"
        "ds_read_b128 v[172:175], v223 offset:24832\n\t"
        "v_mfma_f32_16x16x32_bf16 v[124:127], v[180:183], v[92:95], v[124:127]\n\t"
        "ds_read_b128 v[176:179], v223 offset:25600\n\t"
        "buffer_load_dword v208, v213, s[16:19], s34 offen offset:256 sc1\n\t"
        "v_mfma_f32_16x16x32_bf16 v[128:131], v[92:95], v[184:187], v[128:131]\n\t"
        "ds_read_b128 v[180:183], v223 offset:25856\n\t"
        "v_mfma_f32_16x16x32_bf16 v[132:135], v[92:95], v[188:191], v[132:135]\n\t"
        "ds_read_b128 v[184:187], v223 offset:26624\n\t"
        "s_waitcnt lgkmcnt(7)\n\t"
        "s_waitcnt vmcnt(15)\n\t"
        "v_mfma_f32_16x16x32_bf16 v[112:115], v[144:147], v[96:99], v[112:115]\n\t"
        "ds_read_b128 v[188:191], v223 offset:26880\n\t"
        "buffer_load_dword v210, v213, s[16:19], s34 offen offset:320 sc1\n\t"
        "s_waitcnt vmcnt(11)\n\t"
        "v_add_f32 v0, v0, v2\n\t"
        "v_mfma_f32_16x16x32_bf16 v[116:119], v[148:151], v[96:99], v[116:119]\n\t"
        "ds_read_b128 v[144:147], v223 offset:30720\n\t"
        "v_add_f32 v1, v1, v3\n\t"
        "s_waitcnt vmcnt(9)\n\t"
        "v_add_f32 v4, v4, v6\n\t"
        "v_mfma_f32_16x16x32_bf16 v[120:123], v[152:155], v[96:99], v[120:123]\n\t"
        "ds_read_b128 v[148:151], v223 offset:30976\n\t"
        "v_add_f32 v5, v5, v7\n\t"
        "v_mfma_f32_16x16x32_bf16 v[124:127], v[156:159], v[96:99], v[124:127]\n\t"
        "ds_read_b128 v[152:155], v223 offset:31744\n\t"
        "s_waitcnt vmcnt(7)\n\t"
        "v_add_f32 v8, v8, v10\n\t"
        "v_add_f32 v9, v9, v11\n\t"
        "s_waitcnt lgkmcnt(7)\n\t"
        "v_mfma_f32_16x16x32_bf16 v[128:131], v[96:99], v[160:163], v[128:131]\n\t"
        "ds_read_b128 v[156:159], v223 offset:32000\n\t"
        "s_waitcnt vmcnt(6)\n\t"
        "v_add_f32 v0, v0, v12\n\t"
        "v_mfma_f32_16x16x32_bf16 v[132:135], v[96:99], v[164:167], v[132:135]\n\t"
        "ds_read_b128 v[160:163], v223 offset:32768\n\t"
        "v_add_f32 v1, v1, v13\n\t"
        "v_add_f32 v4, v4, v8\n\t"
        "v_mfma_f32_16x16x32_bf16 v[112:115], v[168:171], v[100:103], v[112:115]\n\t"
        "ds_read_b128 v[164:167], v223 offset:33024\n\t"
        "v_add_f32 v5, v5, v9\n\t"
        "v_mfma_f32_16x16x32_bf16 v[116:119], v[172:175], v[100:103], v[116:119]\n\t"
        "ds_read_b128 v[168:171], v223 offset:36864\n\t"
        "v_add_f32 v0, v0, v4\n\t"
        "v_add_f32 v1, v1, v5\n\t"
        "s_waitcnt lgkmcnt(7)\n\t"
        "v_mfma_f32_16x16x32_bf16 v[120:123], v[176:179], v[100:103], v[120:123]\n\t"
        "ds_read_b128 v[172:175], v223 offset:37120\n\t"
        "v_mov_b32 v2, v0\n\t"
        "v_mfma_f32_16x16x32_bf16 v[124:127], v[180:183], v[100:103], v[124:127]\n\t"
        "ds_read_b128 v[176:179], v223 offset:37888\n\t"
        "v_mov_b32 v3, v1\n\t"
        "v_permlane32_swap_b32 v0, v2\n\t"
        "v_mfma_f32_16x16x32_bf16 v[128:131], v[100:103], v[184:187], v[128:131]\n\t"
        "ds_read_b128 v[180:183], v223 offset:38144\n\t"
        "v_permlane32_swap_b32 v1, v3\n\t"
        "v_mfma_f32_16x16x32_bf16 v[132:135], v[100:103], v[188:191], v[132:135]\n\t"
        "ds_read_b128 v[184:187], v223 offset:38912\n\t"
        "v_add_f32 v0, v0, v2\n\t"
        "v_add_f32 v1, v1, v3\n\t"
        "s_waitcnt lgkmcnt(7)\n\t"
        "v_mfma_f32_16x16x32_bf16 v[112:115], v[144:147], v[104:107], v[112:115]\n\t"
        "ds_read_b128 v[188:191], v223 offset:39168\n\t"
        "v_mov_b32 v2, v0\n\t"
        "v_mfma_f32_16x16x32_bf16 v[116:119], v[148:151], v[104:107], v[116:119]\n\t"
        "v_mov_b32 v3, v1\n\t"
        "v_permlane16_swap_b32 v0, v2\n\t"
        "v_mfma_f32_16x16x32_bf16 v[120:123], v[152:155], v[104:107], v[120:123]\n\t"
        "v_permlane16_swap_b32 v1, v3\n\t"
        "v_mfma_f32_16x16x32_bf16 v[124:127], v[156:159], v[104:107], v[124:127]\n\t"
        "v_add_f32 v0, v0, v2\n\t"
        "v_add_f32 v1, v1, v3\n\t"
        "s_waitcnt lgkmcnt(4)\n\t"
        "v_mfma_f32_16x16x32_bf16 v[128:131], v[104:107], v[160:163], v[128:131]\n\t"
        "v_mul_f32 v0, 0x3b124925, v0\n\t"
        "v_mfma_f32_16x16x32_bf16 v[132:135], v[104:107], v[164:167], v[132:135]\n\t"
        "v_mul_f32 v1, 0x3b124925, v1\n\t"
        "v_fma_f32 v1, -v0, v0, v1\n\t"
        "v_mfma_f32_16x16x32_bf16 v[112:115], v[168:171], v[108:111], v[112:115]\n\t"
        "v_max_f32 v1, 0, v1\n\t"
        "v_mfma_f32_16x16x32_bf16 v[116:119], v[172:175], v[108:111], v[116:119]\n\t"
        "v_add_f32 v1, 0x3727c5ac, v1\n\t"
        "v_rsq_f32 v214, v1\n\t"
        "s_waitcnt lgkmcnt(0)\n\t"
        "v_mfma_f32_16x16x32_bf16 v[120:123], v[176:179], v[108:111], v[120:123]\n\t"
        "v_mul_f32 v215, -v0, v214\n\t"
        "v_mfma_f32_16x16x32_bf16 v[124:127], v[180:183], v[108:111], v[124:127]\n\t"
        "s_lshl_b32 s38, s26, 7\n\t"
        "s_add_u32 s38, s38, 86016\n\t"
        "v_mfma_f32_16x16x32_bf16 v[128:131], v[108:111], v[184:187], v[128:131]\n\t"
        "v_add_u32 v226, s38, v213\n\t"
        "v_mfma_f32_16x16x32_bf16 v[132:135], v[108:111], v[188:191], v[132:135]\n\t"
        "v_add_u32 v227, s38, v225\n\t"
        "ds_write2_b32 v226, v214, v215 offset0:0 offset1:16\n\t"
        "ds_read_b128 v[56:59], v227\n\t"
        "ds_read_b128 v[60:63], v227 offset:64\n\t"
        "s_mul_i32 s9, s27, 147456\n\t"
        "s_add_u32 s9, s9, s14\n\t"
        "s_add_u32 s9, s9, 2048\n\t"
        "buffer_load_dwordx2 v[232:233], v237, s[16:19], s9 offen sc1\n\t"
        "s_add_u32 s47, s31, s28\n\t"
        "s_add_u32 s47, s47, 4390912\n\t"
        "s_add_u32 s33, s37, s28\n\t"
        "s_add_u32 s33, s33, 2293760\n\t"
        "v_cmp_eq_u32 vcc, 48, v225\n\t"
        "v_mov_b32 v219, 0\n\t"
        "v_mul_f32 v216, 0x3e8b97f6, v214\n\t"
        "v_cndmask_b32 v218, 0, 1.0, vcc\n\t"
        "v_cmp_eq_u32 vcc, 48, v213\n\t"
        "v_mul_f32 v217, 0x3e8b97f6, v215\n\t"
        "s_lshl_b32 s38, s54, 10\n\t"
        "v_cndmask_b32 v220, 0, 1.0, vcc\n\t"
        "s_add_u32 s38, s38, s47\n\t"
        "s_waitcnt vmcnt(4)\n\t"
        "v_pk_mul_f32 v[64:65], v[200:201], v[214:215] op_sel:[0,1] op_sel_hi:[1,1]\n\t"
        "v_pk_mul_f32 v[66:67], v[202:203], v[214:215] op_sel:[0,1] op_sel_hi:[1,1]\n\t"
        "s_waitcnt vmcnt(3)\n\t"
        "v_pk_fma_f32 v[68:69], v[204:205], v[214:215], v[218:219] op_sel:[0,1,0] op_sel_hi:[1,1,0]\n\t"
        "v_pk_fma_f32 v[70:71], v[206:207], v[214:215], v[218:219] op_sel:[0,1,0] op_sel_hi:[1,1,1]\n\t"
        "v_pk_fma_f32 v[120:121], v[120:121], v[214:215], v[64:65] op_sel_hi:[1,0,1]\n\t"
        "v_pk_fma_f32 v[122:123], v[122:123], v[214:215], v[66:67] op_sel_hi:[1,0,1]\n\t"
        "v_pk_fma_f32 v[124:125], v[124:125], v[214:215], v[68:69] op_sel_hi:[1,0,1]\n\t"
        "v_pk_fma_f32 v[126:127], v[126:127], v[214:215], v[70:71] op_sel_hi:[1,0,1]\n\t"
        "v_cvt_pk_bf16_f32 v136, v120, v121\n\t"
        "v_cvt_pk_bf16_f32 v137, v122, v123\n\t"
        "v_cvt_pk_bf16_f32 v138, v124, v125\n\t"
        "v_cvt_pk_bf16_f32 v139, v126, v127\n\t"
        "buffer_store_dwordx4 v[136:139], v231, s[16:19], s47 offen sc1\n\t"
        "s_waitcnt vmcnt(3) lgkmcnt(0)\n\t"
        "v_pk_mul_f32 v[72:73], v[60:61], v[208:209] op_sel_hi:[1,0]\n\t"
        "s_waitcnt vmcnt(2)\n\t"
        "v_pk_fma_f32 v[76:77], v[60:61], v[210:211], v[220:221] op_sel_hi:[1,0,0]\n\t"
        "v_pk_mul_f32 v[74:75], v[62:63], v[208:209] op_sel_hi:[1,0]\n\t"
        "v_pk_fma_f32 v[78:79], v[62:63], v[210:211], v[220:221] op_sel_hi:[1,0,0]\n\t"
        "v_pk_fma_f32 v[128:129], v[128:129], v[56:57], v[72:73]\n\t"
        "v_pk_fma_f32 v[130:131], v[130:131], v[58:59], v[74:75]\n\t"
        "v_pk_fma_f32 v[132:133], v[132:133], v[56:57], v[76:77]\n\t"
        "v_pk_fma_f32 v[134:135], v[134:135], v[58:59], v[78:79]\n\t"
        "v_cvt_pk_bf16_f32 v140, v128, v129\n\t"
        "v_cvt_pk_bf16_f32 v141, v130, v131\n\t"
        "v_cvt_pk_bf16_f32 v142, v132, v133\n\t"
        "v_cvt_pk_bf16_f32 v143, v134, v135\n\t"
        "v_pk_mul_f32 v[64:65], v[192:193], v[216:217] op_sel:[0,1] op_sel_hi:[1,1]\n\t"
        "v_pk_mul_f32 v[66:67], v[194:195], v[216:217] op_sel:[0,1] op_sel_hi:[1,1]\n\t"
        "v_pk_mul_f32 v[68:69], v[196:197], v[216:217] op_sel:[0,1] op_sel_hi:[1,1]\n\t"
        "v_pk_mul_f32 v[70:71], v[198:199], v[216:217] op_sel:[0,1] op_sel_hi:[1,1]\n\t"
        "v_permlane32_swap_b32 v140, v142\n\t"
        "v_permlane32_swap_b32 v141, v143\n\t"
        "v_pk_fma_f32 v[112:113], v[112:113], v[216:217], v[64:65] op_sel_hi:[1,0,1]\n\t"
        "v_pk_fma_f32 v[114:115], v[114:115], v[216:217], v[66:67] op_sel_hi:[1,0,1]\n\t"
        "v_pk_fma_f32 v[116:117], v[116:117], v[216:217], v[68:69] op_sel_hi:[1,0,1]\n\t"
        "v_pk_fma_f32 v[118:119], v[118:119], v[216:217], v[70:71] op_sel_hi:[1,0,1]\n\t"
        "buffer_store_dwordx4 v[140:143], v224, s[16:19], s38 offen offset:2048 sc1\n\t"
        "v_cvt_pk_bf16_f32 v144, v112, v113\n\t"
        "v_cvt_pk_bf16_f32 v145, v114, v115\n\t"
        "v_cvt_pk_bf16_f32 v146, v116, v117\n\t"
        "v_cvt_pk_bf16_f32 v147, v118, v119\n\t"
        "buffer_store_dwordx4 v[144:147], v231, s[16:19], s33 offen sc1\n\t"
        "s_waitcnt vmcnt(3)\n\t"
        "v_cmp_ne_u64 vcc, s[10:11], v[232:233]\n\t"
        "s_nop 0\n\t"
        "s_cbranch_vccnz sp_28_%=\n\t"
        "bk_29_%=:\n\t"
        "s_add_u32 s62, s29, s28\n\t"
        "s_add_u32 s62, s62, 57344\n\t"
        "buffer_load_dwordx4 v[0:3], v231, s[16:19], s62 offen offset:0 sc1\n\t"
        "buffer_load_dwordx4 v[4:7], v231, s[16:19], s62 offen offset:2048 sc1\n\t"
        "s_add_u32 s62, s62, 4096\n\t"
        "buffer_load_dwordx4 v[8:11], v231, s[16:19], s62 offen offset:0 sc1\n\t"
        "buffer_load_dwordx4 v[12:15], v231, s[16:19], s62 offen offset:2048 sc1\n\t"
        "s_add_u32 s62, s62, 4096\n\t"
        "buffer_load_dwordx4 v[16:19], v231, s[16:19], s62 offen offset:0 sc1\n\t"
        "buffer_load_dwordx4 v[20:23], v231, s[16:19], s62 offen offset:2048 sc1\n\t"
        "s_mul_i32 s38, s27, 147456\n\t"
        "s_lshl_b32 s39, s25, 11\n\t"
        "s_lshl_b32 s40, s24, 3\n\t"
        "v_lshlrev_b32 v236, 4, v238\n\t"
        "s_add_u32 s38, s38, s39\n\t"
        "s_add_u32 s38, s38, s40\n\t"
        "s_add_u32 s38, s38, 8192\n\t"
        "s_waitcnt vmcnt(6)\n\t"
        "s_barrier\n\t"
        "s_cmp_lg_u32 s26, 0\n\t"
        "s_cbranch_scc1 sig_30_%=\n\t"
        "s_mov_b64 exec, 0xffff\n\t"
        "buffer_atomic_swap_x2 v[234:235], v236, s[16:19], s38 offen\n\t"
        "s_mov_b64 exec, -1\n\t"
        "sig_30_%=:\n\t"
        "ds_read_b128 v[144:147], v222 offset:0\n\t"
        "ds_read_b128 v[148:151], v222 offset:256\n\t"
        "ds_read_b128 v[152:155], v222 offset:1024\n\t"
        "ds_read_b128 v[156:159], v222 offset:1280\n\t"
        "ds_read_b128 v[160:163], v222 offset:2048\n\t"
        "ds_read_b128 v[164:167], v222 offset:2304\n\t"
        "ds_read_b128 v[168:171], v222 offset:6144\n\t"
        "ds_read_b128 v[172:175], v222 offset:6400\n\t"
        "ds_read_b128 v[176:179], v222 offset:7168\n\t"
        "ds_read_b128 v[180:183], v222 offset:7424\n\t"
        "ds_read_b128 v[184:187], v222 offset:8192\n\t"
        "s_waitcnt lgkmcnt(7)\n\t"
        "s_waitcnt vmcnt(5)\n\t"
        "v_mfma_f32_16x16x32_bf16 v[112:115], v[144:147], v[0:3], 0\n\t"
        "ds_read_b128 v[188:191], v222 offset:8448\n\t"
        "s_add_u32 s62, s62, 4096\n\t"
        "buffer_load_dwordx4 v[24:27], v231, s[16:19], s62 offen offset:0 sc1\n\t"
        "v_mfma_f32_16x16x32_bf16 v[116:119], v[148:151], v[0:3], 0\n\t"
        "ds_read_b128 v[144:147], v222 offset:12288\n\t"
        "v_mfma_f32_16x16x32_bf16 v[120:123], v[152:155], v[0:3], 0\n\t"
        "ds_read_b128 v[148:151], v222 offset:12544\n\t"
        "v_mfma_f32_16x16x32_bf16 v[124:127], v[156:159], v[0:3], 0\n\t"
        "ds_read_b128 v[152:155], v222 offset:13312\n\t"
        "buffer_load_dwordx4 v[28:31], v231, s[16:19], s62 offen offset:2048 sc1\n\t"
        "s_waitcnt lgkmcnt(7)\n\t"
        "v_mfma_f32_16x16x32_bf16 v[128:131], v[0:3], v[160:163], 0\n\t"
        "ds_read_b128 v[156:159], v222 offset:13568\n\t"
        "v_mfma_f32_16x16x32_bf16 v[132:135], v[0:3], v[164:167], 0\n\t"
        "ds_read_b128 v[160:163], v222 offset:14336\n\t"
        "s_waitcnt vmcnt(6)\n\t"
        "v_mfma_f32_16x16x32_bf16 v[112:115], v[168:171], v[4:7], v[112:115]\n\t"
        "ds_read_b128 v[164:167], v222 offset:14592\n\t"
        "s_add_u32 s62, s62, 4096\n\t"
        "buffer_load_dwordx4 v[32:35], v231, s[16:19], s62 offen offset:0 sc1\n\t"
        "v_mfma_f32_16x16x32_bf16 v[116:119], v[172:175], v[4:7], v[116:119]\n\t"
        "ds_read_b128 v[168:171], v222 offset:18432\n\t"
        "s_waitcnt lgkmcnt(7)\n\t"
        "v_mfma_f32_16x16x32_bf16 v[120:123], v[176:179], v[4:7], v[120:123]\n\t"
        "ds_read_b128 v[172:175], v222 offset:18688\n\t"
        "v_mfma_f32_16x16x32_bf16 v[124:127], v[180:183], v[4:7], v[124:127]\n\t"
        "ds_read_b128 v[176:179], v222 offset:19456\n\t"
        "buffer_load_dwordx4 v[36:39], v231, s[16:19], s62 offen offset:2048 sc1\n\t"
        "v_mfma_f32_16x16x32_bf16 v[128:131], v[4:7], v[184:187], v[128:131]\n\t"
        "ds_read_b128 v[180:183], v222 offset:19712\n\t"
        "v_mfma_f32_16x16x32_bf16 v[132:135], v[4:7], v[188:191], v[132:135]\n\t"
        "ds_read_b128 v[184:187], v222 offset:20480\n\t"
        "s_waitcnt lgkmcnt(7)\n\t"
        "s_waitcnt vmcnt(7)\n\t"
        "v_mfma_f32_16x16x32_bf16 v[112:115], v[144:147], v[8:11], v[112:115]\n\t"
        "ds_read_b128 v[188:191], v222 offset:20736\n\t"
        "s_add_u32 s62, s62, 4096\n\t"
        "buffer_load_dwordx4 v[40:43], v231, s[16:19], s62 offen offset:0 sc1\n\t"
        "v_mfma_f32_16x16x32_bf16 v[116:119], v[148:151], v[8:11], v[116:119]\n\t"
        "ds_read_b128 v[144:147], v222 offset:24576\n\t"
        "v_mfma_f32_16x16x32_bf16 v[120:123], v[152:155], v[8:11], v[120:123]\n\t"
        "ds_read_b128 v[148:151], v222 offset:24832\n\t"
        "v_mfma_f32_16x16x32_bf16 v[124:127], v[156:159], v[8:11], v[124:127]\n\t"
        "ds_read_b128 v[152:155], v222 offset:25600\n\t"
        "buffer_load_dwordx4 v[44:47], v231, s[16:19], s62 offen offset:2048 sc1\n\t"
        "s_waitcnt lgkmcnt(7)\n\t"
        "v_mfma_f32_16x16x32_bf16 v[128:131], v[8:11], v[160:163], v[128:131]\n\t"
        "ds_read_b128 v[156:159], v222 offset:25856\n\t"
        "v_mfma_f32_16x16x32_bf16 v[132:135], v[8:11], v[164:167], v[132:135]\n\t"
        "ds_read_b128 v[160:163], v222 offset:26624\n\t"
        "s_waitcnt vmcnt(8)\n\t"
        "v_mfma_f32_16x16x32_bf16 v[112:115], v[168:171], v[12:15], v[112:115]\n\t"
        "ds_read_b128 v[164:167], v222 offset:26880\n\t"
        "s_add_u32 s62, s62, 4096\n\t"
        "buffer_load_dwordx4 v[48:51], v231, s[16:19], s62 offen offset:0 sc1\n\t"
        "v_mfma_f32_16x16x32_bf16 v[116:119], v[172:175], v[12:15], v[116:119]\n\t"
        "ds_read_b128 v[168:171], v222 offset:30720\n\t"
        "s_waitcnt lgkmcnt(7)\n\t"
        "v_mfma_f32_16x16x32_bf16 v[120:123], v[176:179], v[12:15], v[120:123]\n\t"
        "ds_read_b128 v[172:175], v222 offset:30976\n\t"
        "v_mfma_f32_16x16x32_bf16 v[124:127], v[180:183], v[12:15], v[124:127]\n\t"
        "ds_read_b128 v[176:179], v222 offset:31744\n\t"
        "buffer_load_dwordx4 v[52:55], v231, s[16:19], s62 offen offset:2048 sc1\n\t"
        "v_mfma_f32_16x16x32_bf16 v[128:131], v[12:15], v[184:187], v[128:131]\n\t"
        "ds_read_b128 v[180:183], v222 offset:32000\n\t"
        "v_mfma_f32_16x16x32_bf16 v[132:135], v[12:15], v[188:191], v[132:135]\n\t"
        "ds_read_b128 v[184:187], v222 offset:32768\n\t"
        "s_waitcnt lgkmcnt(7)\n\t"
        "s_waitcnt vmcnt(9)\n\t"
        "v_mfma_f32_16x16x32_bf16 v[112:115], v[144:147], v[16:19], v[112:115]\n\t"
        "ds_read_b128 v[188:191], v222 offset:33024\n\t"
        "s_add_u32 s33, s30, s28\n\t"
        "s_add_u32 s33, s33, 1849344\n\t"
        "buffer_load_dwordx2 v[56:57], v245, s[16:19], s33 offen offset:0 sc1\n\t"
        "v_mfma_f32_16x16x32_bf16 v[116:119], v[148:151], v[16:19], v[116:119]\n\t"
        "ds_read_b128 v[144:147], v222 offset:36864\n\t"
        "v_mfma_f32_16x16x32_bf16 v[120:123], v[152:155], v[16:19], v[120:123]\n\t"
        "ds_read_b128 v[148:151], v222 offset:37120\n\t"
        "v_mfma_f32_16x16x32_bf16 v[124:127], v[156:159], v[16:19], v[124:127]\n\t"
        "ds_read_b128 v[152:155], v222 offset:37888\n\t"
        "buffer_load_dwordx2 v[58:59], v245, s[16:19], s33 offen offset:256 sc1\n\t"
        "s_waitcnt lgkmcnt(7)\n\t"
        "v_mfma_f32_16x16x32_bf16 v[128:131], v[16:19], v[160:163], v[128:131]\n\t"
        "ds_read_b128 v[156:159], v222 offset:38144\n\t"
        "v_mfma_f32_16x16x32_bf16 v[132:135], v[16:19], v[164:167], v[132:135]\n\t"
        "ds_read_b128 v[160:163], v222 offset:38912\n\t"
        "s_waitcnt vmcnt(10)\n\t"
        "v_mfma_f32_16x16x32_bf16 v[112:115], v[168:171], v[20:23], v[112:115]\n\t"
        "ds_read_b128 v[164:167], v222 offset:39168\n\t"
        "buffer_load_dwordx2 v[60:61], v245, s[16:19], s33 offen offset:512 sc1\n\t"
        "v_mfma_f32_16x16x32_bf16 v[116:119], v[172:175], v[20:23], v[116:119]\n\t"
        "ds_read_b128 v[168:171], v223 offset:0\n\t"
        "s_waitcnt lgkmcnt(7)\n\t"
        "v_mfma_f32_16x16x32_bf16 v[120:123], v[176:179], v[20:23], v[120:123]\n\t"
        "ds_read_b128 v[172:175], v223 offset:256\n\t"
        "v_mfma_f32_16x16x32_bf16 v[124:127], v[180:183], v[20:23], v[124:127]\n\t"
        "ds_read_b128 v[176:179], v223 offset:1024\n\t"
        "buffer_load_dwordx2 v[62:63], v245, s[16:19], s33 offen offset:768 sc1\n\t"
        "v_mfma_f32_16x16x32_bf16 v[128:131], v[20:23], v[184:187], v[128:131]\n\t"
        "ds_read_b128 v[180:183], v223 offset:1280\n\t"
        "v_mfma_f32_16x16x32_bf16 v[132:135], v[20:23], v[188:191], v[132:135]\n\t"
        "ds_read_b128 v[184:187], v223 offset:2048\n\t"
        "s_waitcnt lgkmcnt(7)\n\t"
        "s_waitcnt vmcnt(11)\n\t"
        "v_mfma_f32_16x16x32_bf16 v[112:115], v[144:147], v[24:27], v[112:115]\n\t"
        "ds_read_b128 v[188:191], v223 offset:2304\n\t"
        "buffer_load_dwordx2 v[64:65], v245, s[16:19], s33 offen offset:1024 sc1\n\t"
        "v_mfma_f32_16x16x32_bf16 v[116:119], v[148:151], v[24:27], v[116:119]\n\t"
        "ds_read_b128 v[144:147], v223 offset:6144\n\t"
        "v_mfma_f32_16x16x32_bf16 v[120:123], v[152:155], v[24:27], v[120:123]\n\t"
        "ds_read_b128 v[148:151], v223 offset:6400\n\t"
        "v_mfma_f32_16x16x32_bf16 v[124:127], v[156:159], v[24:27], v[124:127]\n\t"
        "ds_read_b128 v[152:155], v223 offset:7168\n\t"
        "buffer_load_dwordx2 v[66:67], v245, s[16:19], s33 offen offset:1280 sc1\n\t"
        "s_waitcnt lgkmcnt(7)\n\t"
        "v_mfma_f32_16x16x32_bf16 v[128:131], v[24:27], v[160:163], v[128:131]\n\t"
        "ds_read_b128 v[156:159], v223 offset:7424\n\t"
        "v_mfma_f32_16x16x32_bf16 v[132:135], v[24:27], v[164:167], v[132:135]\n\t"
        "ds_read_b128 v[160:163], v223 offset:8192\n\t"
        "s_waitcnt vmcnt(12)\n\t"
        "v_mfma_f32_16x16x32_bf16 v[112:115], v[168:171], v[28:31], v[112:115]\n\t"
        "ds_read_b128 v[164:167], v223 offset:8448\n\t"
        "buffer_load_dwordx2 v[68:69], v245, s[16:19], s33 offen offset:1536 sc1\n\t"
        "v_mfma_f32_16x16x32_bf16 v[116:119], v[172:175], v[28:31], v[116:119]\n\t"
        "ds_read_b128 v[168:171], v223 offset:12288\n\t"
        "s_waitcnt lgkmcnt(7)\n\t"
        "v_mfma_f32_16x16x32_bf16 v[120:123], v[176:179], v[28:31], v[120:123]\n\t"
        "ds_read_b128 v[172:175], v223 offset:12544\n\t"
        "v_mfma_f32_16x16x32_bf16 v[124:127], v[180:183], v[28:31], v[124:127]\n\t"
        "ds_read_b128 v[176:179], v223 offset:13312\n\t"
        "v_mfma_f32_16x16x32_bf16 v[128:131], v[28:31], v[184:187], v[128:131]\n\t"
        "ds_read_b128 v[180:183], v223 offset:13568\n\t"
        "v_mfma_f32_16x16x32_bf16 v[132:135], v[28:31], v[188:191], v[132:135]\n\t"
        "ds_read_b128 v[184:187], v223 offset:14336\n\t"
        "s_waitcnt lgkmcnt(7)\n\t"
        "s_waitcnt vmcnt(12)\n\t"
        "v_mfma_f32_16x16x32_bf16 v[112:115], v[144:147], v[32:35], v[112:115]\n\t"
        "ds_read_b128 v[188:191], v223 offset:14592\n\t"
        "v_mfma_f32_16x16x32_bf16 v[116:119], v[148:151], v[32:35], v[116:119]\n\t"
        "ds_read_b128 v[144:147], v223 offset:18432\n\t"
        "v_mfma_f32_16x16x32_bf16 v[120:123], v[152:155], v[32:35], v[120:123]\n\t"
        "ds_read_b128 v[148:151], v223 offset:18688\n\t"
        "v_mfma_f32_16x16x32_bf16 v[124:127], v[156:159], v[32:35], v[124:127]\n\t"
        "ds_read_b128 v[152:155], v223 offset:19456\n\t"
        "s_waitcnt lgkmcnt(7)\n\t"
        "v_mfma_f32_16x16x32_bf16 v[128:131], v[32:35], v[160:163], v[128:131]\n\t"
        "ds_read_b128 v[156:159], v223 offset:19712\n\t"
        "v_mfma_f32_16x16x32_bf16 v[132:135], v[32:35], v[164:167], v[132:135]\n\t"
        "ds_read_b128 v[160:163], v223 offset:20480\n\t"
        "s_waitcnt vmcnt(11)\n\t"
        "v_mfma_f32_16x16x32_bf16 v[112:115], v[168:171], v[36:39], v[112:115]\n\t"
        "ds_read_b128 v[164:167], v223 offset:20736\n\t"
        "v_mfma_f32_16x16x32_bf16 v[116:119], v[172:175], v[36:39], v[116:119]\n\t"
        "ds_read_b128 v[168:171], v223 offset:24576\n\t"
        "s_waitcnt lgkmcnt(7)\n\t"
        "v_mfma_f32_16x16x32_bf16 v[120:123], v[176:179], v[36:39], v[120:123]\n\t"
        "ds_read_b128 v[172:175], v223 offset:24832\n\t"
        "v_mfma_f32_16x16x32_bf16 v[124:127], v[180:183], v[36:39], v[124:127]\n\t"
        "ds_read_b128 v[176:179], v223 offset:25600\n\t"
        "v_mfma_f32_16x16x32_bf16 v[128:131], v[36:39], v[184:187], v[128:131]\n\t"
        "ds_read_b128 v[180:183], v223 offset:25856\n\t"
        "v_mfma_f32_16x16x32_bf16 v[132:135], v[36:39], v[188:191], v[132:135]\n\t"
        "ds_read_b128 v[184:187], v223 offset:26624\n\t"
        "s_waitcnt lgkmcnt(7)\n\t"
        "s_waitcnt vmcnt(10)\n\t"
        "v_mfma_f32_16x16x32_bf16 v[112:115], v[144:147], v[40:43], v[112:115]\n\t"
        "ds_read_b128 v[188:191], v223 offset:26880\n\t"
        "s_waitcnt vmcnt(5)\n\t"
        "v_add_f32 v56, v56, v58\n\t"
        "v_mfma_f32_16x16x32_bf16 v[116:119], v[148:151], v[40:43], v[116:119]\n\t"
        "ds_read_b128 v[144:147], v223 offset:30720\n\t"
        "v_add_f32 v57, v57, v59\n\t"
        "s_waitcnt vmcnt(3)\n\t"
        "v_add_f32 v60, v60, v62\n\t"
        "v_mfma_f32_16x16x32_bf16 v[120:123], v[152:155], v[40:43], v[120:123]\n\t"
        "ds_read_b128 v[148:151], v223 offset:30976\n\t"
        "v_add_f32 v61, v61, v63\n\t"
        "v_mfma_f32_16x16x32_bf16 v[124:127], v[156:159], v[40:43], v[124:127]\n\t"
        "ds_read_b128 v[152:155], v223 offset:31744\n\t"
        "s_waitcnt vmcnt(1)\n\t"
        "v_add_f32 v64, v64, v66\n\t"
        "v_add_f32 v65, v65, v67\n\t"
        "s_waitcnt lgkmcnt(7)\n\t"
        "v_mfma_f32_16x16x32_bf16 v[128:131], v[40:43], v[160:163], v[128:131]\n\t"
        "ds_read_b128 v[156:159], v223 offset:32000\n\t"
        "s_mul_i32 s9, s27, 147456\n\t"
        "s_add_u32 s9, s9, s12\n\t"
        "s_add_u32 s9, s9, 8192\n\t"
        "buffer_load_dwordx2 v[232:233], v238, s[16:19], s9 offen sc1\n\t"
        "s_waitcnt vmcnt(1)\n\t"
        "v_add_f32 v56, v56, v68\n\t"
        "v_mfma_f32_16x16x32_bf16 v[132:135], v[40:43], v[164:167], v[132:135]\n\t"
        "ds_read_b128 v[160:163], v223 offset:32768\n\t"
        "v_add_f32 v57, v57, v69\n\t"
        "v_add_f32 v60, v60, v64\n\t"
        "v_mfma_f32_16x16x32_bf16 v[112:115], v[168:171], v[44:47], v[112:115]\n\t"
        "ds_read_b128 v[164:167], v223 offset:33024\n\t"
        "v_add_f32 v61, v61, v65\n\t"
        "v_mfma_f32_16x16x32_bf16 v[116:119], v[172:175], v[44:47], v[116:119]\n\t"
        "ds_read_b128 v[168:171], v223 offset:36864\n\t"
        "v_add_f32 v56, v56, v60\n\t"
        "v_add_f32 v57, v57, v61\n\t"
        "s_waitcnt lgkmcnt(7)\n\t"
        "v_mfma_f32_16x16x32_bf16 v[120:123], v[176:179], v[44:47], v[120:123]\n\t"
        "ds_read_b128 v[172:175], v223 offset:37120\n\t"
        "v_mov_b32 v58, v56\n\t"
        "v_mfma_f32_16x16x32_bf16 v[124:127], v[180:183], v[44:47], v[124:127]\n\t"
        "ds_read_b128 v[176:179], v223 offset:37888\n\t"
        "v_mov_b32 v59, v57\n\t"
        "v_permlane32_swap_b32 v56, v58\n\t"
        "v_mfma_f32_16x16x32_bf16 v[128:131], v[44:47], v[184:187], v[128:131]\n\t"
        "ds_read_b128 v[180:183], v223 offset:38144\n\t"
        "v_permlane32_swap_b32 v57, v59\n\t"
        "v_mfma_f32_16x16x32_bf16 v[132:135], v[44:47], v[188:191], v[132:135]\n\t"
        "ds_read_b128 v[184:187], v223 offset:38912\n\t"
        "v_add_f32 v56, v56, v58\n\t"
        "v_add_f32 v57, v57, v59\n\t"
        "s_waitcnt lgkmcnt(7)\n\t"
        "v_mfma_f32_16x16x32_bf16 v[112:115], v[144:147], v[48:51], v[112:115]\n\t"
        "ds_read_b128 v[188:191], v223 offset:39168\n\t"
        "v_mov_b32 v58, v56\n\t"
        "v_mfma_f32_16x16x32_bf16 v[116:119], v[148:151], v[48:51], v[116:119]\n\t"
        "v_mov_b32 v59, v57\n\t"
        "v_permlane16_swap_b32 v56, v58\n\t"
        "v_mfma_f32_16x16x32_bf16 v[120:123], v[152:155], v[48:51], v[120:123]\n\t"
        "v_permlane16_swap_b32 v57, v59\n\t"
        "v_mfma_f32_16x16x32_bf16 v[124:127], v[156:159], v[48:51], v[124:127]\n\t"
        "v_add_f32 v56, v56, v58\n\t"
        "v_add_f32 v57, v57, v59\n\t"
        "s_waitcnt lgkmcnt(4)\n\t"
        "v_mfma_f32_16x16x32_bf16 v[128:131], v[48:51], v[160:163], v[128:131]\n\t"
        "v_mul_f32 v56, 0x3b124925, v56\n\t"
        "v_mfma_f32_16x16x32_bf16 v[132:135], v[48:51], v[164:167], v[132:135]\n\t"
        "v_mul_f32 v57, 0x3b124925, v57\n\t"
        "v_fma_f32 v57, -v56, v56, v57\n\t"
        "v_mfma_f32_16x16x32_bf16 v[112:115], v[168:171], v[52:55], v[112:115]\n\t"
        "v_max_f32 v57, 0, v57\n\t"
        "v_mfma_f32_16x16x32_bf16 v[116:119], v[172:175], v[52:55], v[116:119]\n\t"
        "v_add_f32 v57, 0x3727c5ac, v57\n\t"
        "v_rsq_f32 v214, v57\n\t"
        "s_waitcnt lgkmcnt(0)\n\t"
        "v_mfma_f32_16x16x32_bf16 v[120:123], v[176:179], v[52:55], v[120:123]\n\t"
        "v_mul_f32 v215, -v56, v214\n\t"
        "v_mfma_f32_16x16x32_bf16 v[124:127], v[180:183], v[52:55], v[124:127]\n\t"
        "s_lshl_b32 s38, s26, 7\n\t"
        "s_add_u32 s38, s38, 86016\n\t"
        "v_mfma_f32_16x16x32_bf16 v[128:131], v[52:55], v[184:187], v[128:131]\n\t"
        "v_add_u32 v226, s38, v213\n\t"
        "v_mfma_f32_16x16x32_bf16 v[132:135], v[52:55], v[188:191], v[132:135]\n\t"
        "v_add_u32 v227, s38, v225\n\t"
        "ds_write2_b32 v226, v214, v215 offset0:0 offset1:16\n\t"
        "s_waitcnt vmcnt(0)\n\t"
        "v_cmp_ne_u64 vcc, s[10:11], v[232:233]\n\t"
        "s_nop 0\n\t"
        "s_cbranch_vccnz sp_31_%=\n\t"
        "bk_32_%=:\n\t"
        "s_add_u32 s63, s44, s28\n\t"
        "s_add_u32 s63, s63, 4390912\n\t"
        "buffer_load_dwordx4 v[96:99], v247, s[16:19], s63 offen offset:0 sc1\n\t"
        "buffer_load_dwordx4 v[100:103], v247, s[16:19], s63 offen offset:1024 sc1\n\t"
        "s_add_u32 s34, s45, s28\n\t"
        "s_add_u32 s34, s34, 2293760\n\t"
        "buffer_load_dwordx4 v[64:67], v247, s[16:19], s34 offen offset:0 sc1\n\t"
        "buffer_load_dwordx4 v[68:71], v247, s[16:19], s34 offen offset:1024 sc1\n\t"
        "s_add_u32 s34, s45, s28\n\t"
        "s_add_u32 s34, s34, 2293760\n\t"
        "s_xor_b32 s34, s34, 2048\n\t"
        "buffer_load_dwordx4 v[72:75], v247, s[16:19], s34 offen offset:0 sc1\n\t"
        "buffer_load_dwordx4 v[76:79], v247, s[16:19], s34 offen offset:1024 sc1\n\t"
        "buffer_load_dwordx4 v[104:107], v246, s[16:19], s63 offen offset:2048 sc1\n\t"
        "buffer_load_dwordx4 v[108:111], v246, s[16:19], s63 offen offset:3072 sc1\n\t"
        "ds_read_b128 v[0:3], v227\n\t"
        "ds_read_b128 v[4:7], v227 offset:64\n\t"
        "s_add_u32 s47, s31, s28\n\t"
        "s_add_u32 s47, s47, 4399104\n\t"
        "s_add_u32 s33, s37, s28\n\t"
        "s_add_u32 s33, s33, 2297856\n\t"
        "v_cmp_eq_u32 vcc, 48, v225\n\t"
        "v_mov_b32 v219, 0\n\t"
        "v_mul_f32 v216, 0x3e8b97f6, v214\n\t"
        "v_cndmask_b32 v218, 0, 1.0, vcc\n\t"
        "v_cmp_eq_u32 vcc, 48, v213\n\t"
        "v_mul_f32 v217, 0x3e8b97f6, v215\n\t"
        "s_lshl_b32 s38, s54, 10\n\t"
        "v_cndmask_b32 v220, 0, 1.0, vcc\n\t"
        "s_add_u32 s38, s38, s47\n\t"
        "v_pk_mul_f32 v[8:9], v[200:201], v[214:215] op_sel:[0,1] op_sel_hi:[1,1]\n\t"
        "v_pk_mul_f32 v[10:11], v[202:203], v[214:215] op_sel:[0,1] op_sel_hi:[1,1]\n\t"
        "v_pk_fma_f32 v[12:13], v[204:205], v[214:215], v[218:219] op_sel:[0,1,0] op_sel_hi:[1,1,0]\n\t"
        "v_pk_fma_f32 v[14:15], v[206:207], v[214:215], v[218:219] op_sel:[0,1,0] op_sel_hi:[1,1,1]\n\t"
        "v_pk_fma_f32 v[120:121], v[120:121], v[214:215], v[8:9] op_sel_hi:[1,0,1]\n\t"
        "v_pk_fma_f32 v[122:123], v[122:123], v[214:215], v[10:11] op_sel_hi:[1,0,1]\n\t"
        "v_pk_fma_f32 v[124:125], v[124:125], v[214:215], v[12:13] op_sel_hi:[1,0,1]\n\t"
        "v_pk_fma_f32 v[126:127], v[126:127], v[214:215], v[14:15] op_sel_hi:[1,0,1]\n\t"
        "v_cvt_pk_bf16_f32 v136, v120, v121\n\t"
        "v_cvt_pk_bf16_f32 v137, v122, v123\n\t"
        "v_cvt_pk_bf16_f32 v138, v124, v125\n\t"
        "v_cvt_pk_bf16_f32 v139, v126, v127\n\t"
        "buffer_store_dwordx4 v[136:139], v231, s[16:19], s47 offen sc1\n\t"
        "s_waitcnt lgkmcnt(0)\n\t"
        "v_pk_mul_f32 v[16:17], v[4:5], v[208:209] op_sel_hi:[1,0]\n\t"
        "v_pk_fma_f32 v[20:21], v[4:5], v[210:211], v[220:221] op_sel_hi:[1,0,0]\n\t"
        "v_pk_mul_f32 v[18:19], v[6:7], v[208:209] op_sel_hi:[1,0]\n\t"
        "v_pk_fma_f32 v[22:23], v[6:7], v[210:211], v[220:221] op_sel_hi:[1,0,0]\n\t"
        "v_pk_fma_f32 v[128:129], v[128:129], v[0:1], v[16:17]\n\t"
        "v_pk_fma_f32 v[130:131], v[130:131], v[2:3], v[18:19]\n\t"
        "v_pk_fma_f32 v[132:133], v[132:133], v[0:1], v[20:21]\n\t"
        "v_pk_fma_f32 v[134:135], v[134:135], v[2:3], v[22:23]\n\t"
        "v_cvt_pk_bf16_f32 v140, v128, v129\n\t"
        "v_cvt_pk_bf16_f32 v141, v130, v131\n\t"
        "v_cvt_pk_bf16_f32 v142, v132, v133\n\t"
        "v_cvt_pk_bf16_f32 v143, v134, v135\n\t"
        "v_pk_mul_f32 v[8:9], v[192:193], v[216:217] op_sel:[0,1] op_sel_hi:[1,1]\n\t"
        "v_pk_mul_f32 v[10:11], v[194:195], v[216:217] op_sel:[0,1] op_sel_hi:[1,1]\n\t"
        "v_pk_mul_f32 v[12:13], v[196:197], v[216:217] op_sel:[0,1] op_sel_hi:[1,1]\n\t"
        "v_pk_mul_f32 v[14:15], v[198:199], v[216:217] op_sel:[0,1] op_sel_hi:[1,1]\n\t"
        "v_permlane32_swap_b32 v140, v142\n\t"
        "v_permlane32_swap_b32 v141, v143\n\t"
        "v_pk_fma_f32 v[112:113], v[112:113], v[216:217], v[8:9] op_sel_hi:[1,0,1]\n\t"
        "v_pk_fma_f32 v[114:115], v[114:115], v[216:217], v[10:11] op_sel_hi:[1,0,1]\n\t"
        "v_pk_fma_f32 v[116:117], v[116:117], v[216:217], v[12:13] op_sel_hi:[1,0,1]\n\t"
        "v_pk_fma_f32 v[118:119], v[118:119], v[216:217], v[14:15] op_sel_hi:[1,0,1]\n\t"
        "buffer_store_dwordx4 v[140:143], v224, s[16:19], s38 offen offset:2048 sc1\n\t"
        "v_cvt_pk_bf16_f32 v144, v112, v113\n\t"
        "v_cvt_pk_bf16_f32 v145, v114, v115\n\t"
        "v_cvt_pk_bf16_f32 v146, v116, v117\n\t"
        "v_cvt_pk_bf16_f32 v147, v118, v119\n\t"
        "buffer_store_dwordx4 v[144:147], v231, s[16:19], s33 offen sc1\n\t"
        "s_mov_b32 s47, s63\n\t"
        "s_add_u32 s47, s47, 4096\n\t"
        "buffer_load_dwordx4 v[112:115], v247, s[16:19], s47 offen offset:0 sc1\n\t"
        "buffer_load_dwordx4 v[116:119], v247, s[16:19], s47 offen offset:1024 sc1\n\t"
        "buffer_load_dwordx4 v[120:123], v246, s[16:19], s47 offen offset:2048 sc1\n\t"
        "buffer_load_dwordx4 v[124:127], v246, s[16:19], s47 offen offset:3072 sc1\n\t"
        "att_26_%=:\n\t"
        "s_waitcnt vmcnt(12)\n\t"
        "v_mfma_f32_32x32x16_bf16 v[160:175], v[96:99], v[64:67], 0\n\t"
        "s_waitcnt vmcnt(11)\n\t"
        "v_mfma_f32_32x32x16_bf16 v[160:175], v[100:103], v[68:71], v[160:175]\n\t"
        "s_waitcnt vmcnt(10)\n\t"
        "v_mfma_f32_32x32x16_bf16 v[176:191], v[96:99], v[72:75], 0\n\t"
        "s_nop 9\n\t"
        "v_exp_f32 v160, v160\n\t"
        "v_exp_f32 v161, v161\n\t"
        "v_exp_f32 v162, v162\n\t"
        "v_exp_f32 v163, v163\n\t"
        "s_add_u32 s47, s47, 12288\n\t"
        "buffer_load_dwordx4 v[128:131], v247, s[16:19], s47 offen offset:0 sc1\n\t"
        "buffer_load_dwordx4 v[132:135], v247, s[16:19], s47 offen offset:1024 sc1\n\t"
        "buffer_load_dwordx4 v[136:139], v246, s[16:19], s47 offen offset:2048 sc1\n\t"
        "buffer_load_dwordx4 v[140:143], v246, s[16:19], s47 offen offset:3072 sc1\n\t"
        "s_add_u32 s47, s47, 4096\n\t"
        "buffer_load_dwordx4 v[144:147], v247, s[16:19], s47 offen offset:0 sc1\n\t"
        "v_exp_f32 v164, v164\n\t"
        "v_exp_f32 v165, v165\n\t"
        "v_cvt_pk_bf16_f32 v192, v160, v161\n\t"
        "v_exp_f32 v166, v166\n\t"
        "v_exp_f32 v167, v167\n\t"
        "v_cvt_pk_bf16_f32 v193, v162, v163\n\t"
        "s_waitcnt vmcnt(14)\n\t"
        "v_mfma_f32_32x32x16_bf16 v[176:191], v[100:103], v[76:79], v[176:191]\n\t"
        "v_exp_f32 v168, v168\n\t"
        "v_exp_f32 v169, v169\n\t"
        "v_cvt_pk_bf16_f32 v194, v164, v165\n\t"
        "v_exp_f32 v170, v170\n\t"
        "v_cvt_pk_bf16_f32 v195, v166, v167\n\t"
        "v_exp_f32 v171, v171\n\t"
        "v_exp_f32 v172, v172\n\t"
        "s_waitcnt vmcnt(13)\n\t"
        "v_mfma_f32_32x32x16_bf16 v[0:15], v[104:107], v[192:195], 0\n\t"
        "buffer_load_dwordx4 v[148:151], v247, s[16:19], s47 offen offset:1024 sc1\n\t"
        "s_waitcnt vmcnt(6)\n\t"
        "v_exp_f32 v173, v173\n\t"
        "v_cvt_pk_bf16_f32 v196, v168, v169\n\t"
        "v_exp_f32 v174, v174\n\t"
        "v_cvt_pk_bf16_f32 v197, v170, v171\n\t"
        "v_exp_f32 v175, v175\n\t"
        "v_cvt_pk_bf16_f32 v198, v172, v173\n\t"
        "v_cvt_pk_bf16_f32 v199, v174, v175\n\t"
        "v_mfma_f32_32x32x16_bf16 v[160:175], v[112:115], v[64:67], 0\n\t"
        "v_exp_f32 v176, v176\n\t"
        "v_exp_f32 v177, v177\n\t"
        "v_exp_f32 v178, v178\n\t"
        "v_exp_f32 v179, v179\n\t"
        "v_mfma_f32_32x32x16_bf16 v[0:15], v[108:111], v[196:199], v[0:15]\n\t"
        "buffer_load_dwordx4 v[152:155], v246, s[16:19], s47 offen offset:2048 sc1\n\t"
        "s_mul_i32 s38, s27, 147456\n\t"
        "s_lshl_b32 s39, s25, 11\n\t"
        "s_lshl_b32 s40, s24, 3\n\t"
        "v_lshlrev_b32 v236, 4, v238\n\t"
        "s_add_u32 s38, s38, s39\n\t"
        "s_add_u32 s38, s38, s40\n\t"
        "s_add_u32 s38, s38, 40960\n\t"
        "s_barrier\n\t"
        "s_cmp_lg_u32 s26, 0\n\t"
        "s_cbranch_scc1 sig_33_%=\n\t"
        "s_mov_b64 exec, 0xffff\n\t"
        "buffer_atomic_swap_x2 v[234:235], v236, s[16:19], s38 offen\n\t"
        "s_mov_b64 exec, -1\n\t"
        "sig_33_%=:\n\t"
        "v_exp_f32 v180, v180\n\t"
        "v_exp_f32 v181, v181\n\t"
        "v_cvt_pk_bf16_f32 v192, v176, v177\n\t"
        "v_exp_f32 v182, v182\n\t"
        "v_exp_f32 v183, v183\n\t"
        "v_cvt_pk_bf16_f32 v193, v178, v179\n\t"
        "v_mfma_f32_32x32x16_bf16 v[160:175], v[116:119], v[68:71], v[160:175]\n\t"
        "v_exp_f32 v184, v184\n\t"
        "v_exp_f32 v185, v185\n\t"
        "v_cvt_pk_bf16_f32 v194, v180, v181\n\t"
        "v_exp_f32 v186, v186\n\t"
        "v_cvt_pk_bf16_f32 v195, v182, v183\n\t"
        "v_exp_f32 v187, v187\n\t"
        "v_exp_f32 v188, v188\n\t"
        "v_mfma_f32_32x32x16_bf16 v[16:31], v[104:107], v[192:195], 0\n\t"
        "buffer_load_dwordx4 v[156:159], v246, s[16:19], s47 offen offset:3072 sc1\n\t"
        "v_exp_f32 v189, v189\n\t"
        "v_cvt_pk_bf16_f32 v196, v184, v185\n\t"
        "v_exp_f32 v190, v190\n\t"
        "v_cvt_pk_bf16_f32 v197, v186, v187\n\t"
        "v_exp_f32 v191, v191\n\t"
        "v_cvt_pk_bf16_f32 v198, v188, v189\n\t"
        "v_cvt_pk_bf16_f32 v199, v190, v191\n\t"
        "v_mfma_f32_32x32x16_bf16 v[176:191], v[112:115], v[72:75], 0\n\t"
        "v_exp_f32 v160, v160\n\t"
        "v_exp_f32 v161, v161\n\t"
        "v_exp_f32 v162, v162\n\t"
        "v_exp_f32 v163, v163\n\t"
        "v_mfma_f32_32x32x16_bf16 v[16:31], v[108:111], v[196:199], v[16:31]\n\t"
        "s_add_u32 s47, s47, 12288\n\t"
        "buffer_load_dwordx4 v[96:99], v247, s[16:19], s47 offen offset:0 sc1\n\t"
        "v_exp_f32 v164, v164\n\t"
        "v_exp_f32 v165, v165\n\t"
        "v_cvt_pk_bf16_f32 v192, v160, v161\n\t"
        "v_exp_f32 v166, v166\n\t"
        "v_exp_f32 v167, v167\n\t"
        "v_cvt_pk_bf16_f32 v193, v162, v163\n\t"
        "v_mfma_f32_32x32x16_bf16 v[176:191], v[116:119], v[76:79], v[176:191]\n\t"
        "v_exp_f32 v168, v168\n\t"
        "v_exp_f32 v169, v169\n\t"
        "v_cvt_pk_bf16_f32 v194, v164, v165\n\t"
        "v_exp_f32 v170, v170\n\t"
        "v_cvt_pk_bf16_f32 v195, v166, v167\n\t"
        "v_exp_f32 v171, v171\n\t"
        "v_exp_f32 v172, v172\n\t"
        "v_mfma_f32_32x32x16_bf16 v[0:15], v[120:123], v[192:195], v[0:15]\n\t"
        "buffer_load_dwordx4 v[100:103], v247, s[16:19], s47 offen offset:1024 sc1\n\t"
        "s_waitcnt vmcnt(6)\n\t"
        "v_exp_f32 v173, v173\n\t"
        "v_cvt_pk_bf16_f32 v196, v168, v169\n\t"
        "v_exp_f32 v174, v174\n\t"
        "v_cvt_pk_bf16_f32 v197, v170, v171\n\t"
        "v_exp_f32 v175, v175\n\t"
        "v_cvt_pk_bf16_f32 v198, v172, v173\n\t"
        "v_cvt_pk_bf16_f32 v199, v174, v175\n\t"
        "v_mfma_f32_32x32x16_bf16 v[160:175], v[128:131], v[64:67], 0\n\t"
        "v_exp_f32 v176, v176\n\t"
        "v_exp_f32 v177, v177\n\t"
        "v_exp_f32 v178, v178\n\t"
        "v_exp_f32 v179, v179\n\t"
        "v_mfma_f32_32x32x16_bf16 v[0:15], v[124:127], v[196:199], v[0:15]\n\t"
        "buffer_load_dwordx4 v[104:107], v246, s[16:19], s47 offen offset:2048 sc1\n\t"
        "v_exp_f32 v180, v180\n\t"
        "v_exp_f32 v181, v181\n\t"
        "v_cvt_pk_bf16_f32 v192, v176, v177\n\t"
        "v_exp_f32 v182, v182\n\t"
        "v_exp_f32 v183, v183\n\t"
        "v_cvt_pk_bf16_f32 v193, v178, v179\n\t"
        "v_mfma_f32_32x32x16_bf16 v[160:175], v[132:135], v[68:71], v[160:175]\n\t"
        "v_exp_f32 v184, v184\n\t"
        "v_exp_f32 v185, v185\n\t"
        "v_cvt_pk_bf16_f32 v194, v180, v181\n\t"
        "v_exp_f32 v186, v186\n\t"
        "v_cvt_pk_bf16_f32 v195, v182, v183\n\t"
        "v_exp_f32 v187, v187\n\t"
        "v_exp_f32 v188, v188\n\t"
        "v_mfma_f32_32x32x16_bf16 v[16:31], v[120:123], v[192:195], v[16:31]\n\t"
        "buffer_load_dwordx4 v[108:111], v246, s[16:19], s47 offen offset:3072 sc1\n\t"
        "v_exp_f32 v189, v189\n\t"
        "v_cvt_pk_bf16_f32 v196, v184, v185\n\t"
        "v_exp_f32 v190, v190\n\t"
        "v_cvt_pk_bf16_f32 v197, v186, v187\n\t"
        "v_exp_f32 v191, v191\n\t"
        "v_cvt_pk_bf16_f32 v198, v188, v189\n\t"
        "v_cvt_pk_bf16_f32 v199, v190, v191\n\t"
        "v_mfma_f32_32x32x16_bf16 v[176:191], v[128:131], v[72:75], 0\n\t"
        "v_exp_f32 v160, v160\n\t"
        "v_exp_f32 v161, v161\n\t"
        "v_exp_f32 v162, v162\n\t"
        "v_exp_f32 v163, v163\n\t"
        "v_mfma_f32_32x32x16_bf16 v[16:31], v[124:127], v[196:199], v[16:31]\n\t"
        "s_add_u32 s47, s47, 4096\n\t"
        "buffer_load_dwordx4 v[112:115], v247, s[16:19], s47 offen offset:0 sc1\n\t"
        "v_exp_f32 v164, v164\n\t"
        "v_exp_f32 v165, v165\n\t"
        "v_cvt_pk_bf16_f32 v192, v160, v161\n\t"
        "v_exp_f32 v166, v166\n\t"
        "v_exp_f32 v167, v167\n\t"
        "v_cvt_pk_bf16_f32 v193, v162, v163\n\t"
        "v_mfma_f32_32x32x16_bf16 v[176:191], v[132:135], v[76:79], v[176:191]\n\t"
        "v_exp_f32 v168, v168\n\t"
        "v_exp_f32 v169, v169\n\t"
        "v_cvt_pk_bf16_f32 v194, v164, v165\n\t"
        "v_exp_f32 v170, v170\n\t"
        "v_cvt_pk_bf16_f32 v195, v166, v167\n\t"
        "v_exp_f32 v171, v171\n\t"
        "v_exp_f32 v172, v172\n\t"
        "v_mfma_f32_32x32x16_bf16 v[0:15], v[136:139], v[192:195], v[0:15]\n\t"
        "buffer_load_dwordx4 v[116:119], v247, s[16:19], s47 offen offset:1024 sc1\n\t"
        "s_waitcnt vmcnt(6)\n\t"
        "v_exp_f32 v173, v173\n\t"
        "v_cvt_pk_bf16_f32 v196, v168, v169\n\t"
        "v_exp_f32 v174, v174\n\t"
        "v_cvt_pk_bf16_f32 v197, v170, v171\n\t"
        "v_exp_f32 v175, v175\n\t"
        "v_cvt_pk_bf16_f32 v198, v172, v173\n\t"
        "v_cvt_pk_bf16_f32 v199, v174, v175\n\t"
        "v_mfma_f32_32x32x16_bf16 v[160:175], v[144:147], v[64:67], 0\n\t"
        "v_exp_f32 v176, v176\n\t"
        "v_exp_f32 v177, v177\n\t"
        "v_exp_f32 v178, v178\n\t"
        "v_exp_f32 v179, v179\n\t"
        "v_mfma_f32_32x32x16_bf16 v[0:15], v[140:143], v[196:199], v[0:15]\n\t"
        "buffer_load_dwordx4 v[120:123], v246, s[16:19], s47 offen offset:2048 sc1\n\t"
        "s_cmp_lg_u32 s27, 0\n\t"
        "s_cbranch_scc1 l0_34_%=\n\t"
        "s_mov_b32 s38, s73\n\t"
        "s_barrier\n\t"
        "s_cmp_lg_u32 s26, 0\n\t"
        "s_cbranch_scc1 sig_35_%=\n\t"
        "s_mov_b64 exec, s[76:77]\n\t"
        "buffer_atomic_swap_x2 v[234:235], v227, s[16:19], s38 offen\n\t"
        "s_mov_b64 exec, -1\n\t"
        "sig_35_%=:\n\t"
        "l0_34_%=:\n\t"
        "v_exp_f32 v180, v180\n\t"
        "v_exp_f32 v181, v181\n\t"
        "v_cvt_pk_bf16_f32 v192, v176, v177\n\t"
        "v_exp_f32 v182, v182\n\t"
        "v_exp_f32 v183, v183\n\t"
        "v_cvt_pk_bf16_f32 v193, v178, v179\n\t"
        "v_mfma_f32_32x32x16_bf16 v[160:175], v[148:151], v[68:71], v[160:175]\n\t"
        "v_exp_f32 v184, v184\n\t"
        "v_exp_f32 v185, v185\n\t"
        "v_cvt_pk_bf16_f32 v194, v180, v181\n\t"
        "v_exp_f32 v186, v186\n\t"
        "v_cvt_pk_bf16_f32 v195, v182, v183\n\t"
        "v_exp_f32 v187, v187\n\t"
        "v_exp_f32 v188, v188\n\t"
        "v_mfma_f32_32x32x16_bf16 v[16:31], v[136:139], v[192:195], v[16:31]\n\t"
        "buffer_load_dwordx4 v[124:127], v246, s[16:19], s47 offen offset:3072 sc1\n\t"
        "v_exp_f32 v189, v189\n\t"
        "v_cvt_pk_bf16_f32 v196, v184, v185\n\t"
        "v_exp_f32 v190, v190\n\t"
        "v_cvt_pk_bf16_f32 v197, v186, v187\n\t"
        "v_exp_f32 v191, v191\n\t"
        "v_cvt_pk_bf16_f32 v198, v188, v189\n\t"
        "v_cvt_pk_bf16_f32 v199, v190, v191\n\t"
        "v_mfma_f32_32x32x16_bf16 v[176:191], v[144:147], v[72:75], 0\n\t"
        "v_exp_f32 v160, v160\n\t"
        "v_exp_f32 v161, v161\n\t"
        "v_exp_f32 v162, v162\n\t"
        "v_exp_f32 v163, v163\n\t"
        "v_mfma_f32_32x32x16_bf16 v[16:31], v[140:143], v[196:199], v[16:31]\n\t"
        "s_add_u32 s47, s47, 12288\n\t"
        "buffer_load_dwordx4 v[128:131], v247, s[16:19], s47 offen offset:0 sc1\n\t"
        "v_exp_f32 v164, v164\n\t"
        "v_exp_f32 v165, v165\n\t"
        "v_cvt_pk_bf16_f32 v192, v160, v161\n\t"
        "v_exp_f32 v166, v166\n\t"
        "v_exp_f32 v167, v167\n\t"
        "v_cvt_pk_bf16_f32 v193, v162, v163\n\t"
        "v_mfma_f32_32x32x16_bf16 v[176:191], v[148:151], v[76:79], v[176:191]\n\t"
        "v_exp_f32 v168, v168\n\t"
        "v_exp_f32 v169, v169\n\t"
        "v_cvt_pk_bf16_f32 v194, v164, v165\n\t"
        "v_exp_f32 v170, v170\n\t"
        "v_cvt_pk_bf16_f32 v195, v166, v167\n\t"
        "v_exp_f32 v171, v171\n\t"
        "v_exp_f32 v172, v172\n\t"
        "v_mfma_f32_32x32x16_bf16 v[0:15], v[152:155], v[192:195], v[0:15]\n\t"
        "buffer_load_dwordx4 v[132:135], v247, s[16:19], s47 offen offset:1024 sc1\n\t"
        "s_waitcnt vmcnt(6)\n\t"
        "v_exp_f32 v173, v173\n\t"
        "v_cvt_pk_bf16_f32 v196, v168, v169\n\t"
        "v_exp_f32 v174, v174\n\t"
        "v_cvt_pk_bf16_f32 v197, v170, v171\n\t"
        "v_exp_f32 v175, v175\n\t"
        "v_cvt_pk_bf16_f32 v198, v172, v173\n\t"
        "v_cvt_pk_bf16_f32 v199, v174, v175\n\t"
        "v_mfma_f32_32x32x16_bf16 v[160:175], v[96:99], v[64:67], 0\n\t"
        "v_exp_f32 v176, v176\n\t"
        "v_exp_f32 v177, v177\n\t"
        "v_exp_f32 v178, v178\n\t"
        "v_exp_f32 v179, v179\n\t"
        "v_mfma_f32_32x32x16_bf16 v[0:15], v[156:159], v[196:199], v[0:15]\n\t"
        "buffer_load_dwordx4 v[136:139], v246, s[16:19], s47 offen offset:2048 sc1\n\t"
        "v_exp_f32 v180, v180\n\t"
        "v_exp_f32 v181, v181\n\t"
        "v_cvt_pk_bf16_f32 v192, v176, v177\n\t"
        "v_exp_f32 v182, v182\n\t"
        "v_exp_f32 v183, v183\n\t"
        "v_cvt_pk_bf16_f32 v193, v178, v179\n\t"
        "v_mfma_f32_32x32x16_bf16 v[160:175], v[100:103], v[68:71], v[160:175]\n\t"
        "v_exp_f32 v184, v184\n\t"
        "v_exp_f32 v185, v185\n\t"
        "v_cvt_pk_bf16_f32 v194, v180, v181\n\t"
        "v_exp_f32 v186, v186\n\t"
        "v_cvt_pk_bf16_f32 v195, v182, v183\n\t"
        "v_exp_f32 v187, v187\n\t"
        "v_exp_f32 v188, v188\n\t"
        "v_mfma_f32_32x32x16_bf16 v[16:31], v[152:155], v[192:195], v[16:31]\n\t"
        "buffer_load_dwordx4 v[140:143], v246, s[16:19], s47 offen offset:3072 sc1\n\t"
        "v_exp_f32 v189, v189\n\t"
        "v_cvt_pk_bf16_f32 v196, v184, v185\n\t"
        "v_exp_f32 v190, v190\n\t"
        "v_cvt_pk_bf16_f32 v197, v186, v187\n\t"
        "v_exp_f32 v191, v191\n\t"
        "v_cvt_pk_bf16_f32 v198, v188, v189\n\t"
        "v_cvt_pk_bf16_f32 v199, v190, v191\n\t"
        "v_mfma_f32_32x32x16_bf16 v[176:191], v[96:99], v[72:75], 0\n\t"
        "v_exp_f32 v160, v160\n\t"
        "v_exp_f32 v161, v161\n\t"
        "v_exp_f32 v162, v162\n\t"
        "v_exp_f32 v163, v163\n\t"
        "v_mfma_f32_32x32x16_bf16 v[16:31], v[156:159], v[196:199], v[16:31]\n\t"
        "s_mul_i32 s9, s27, 147456\n\t"
        "s_add_u32 s9, s9, s12\n\t"
        "s_add_u32 s9, s9, 40960\n\t"
        "buffer_load_dwordx2 v[232:233], v238, s[16:19], s9 offen sc1\n\t"
        "s_add_u32 s47, s47, 4096\n\t"
        "buffer_load_dwordx4 v[144:147], v247, s[16:19], s47 offen offset:0 sc1\n\t"
        "v_exp_f32 v164, v164\n\t"
        "v_exp_f32 v165, v165\n\t"
        "v_cvt_pk_bf16_f32 v192, v160, v161\n\t"
        "v_exp_f32 v166, v166\n\t"
        "v_exp_f32 v167, v167\n\t"
        "v_cvt_pk_bf16_f32 v193, v162, v163\n\t"
        "v_mfma_f32_32x32x16_bf16 v[176:191], v[100:103], v[76:79], v[176:191]\n\t"
        "v_exp_f32 v168, v168\n\t"
        "v_exp_f32 v169, v169\n\t"
        "v_cvt_pk_bf16_f32 v194, v164, v165\n\t"
        "v_exp_f32 v170, v170\n\t"
        "v_cvt_pk_bf16_f32 v195, v166, v167\n\t"
        "v_exp_f32 v171, v171\n\t"
        "v_exp_f32 v172, v172\n\t"
        "v_mfma_f32_32x32x16_bf16 v[0:15], v[104:107], v[192:195], v[0:15]\n\t"
        "buffer_load_dwordx4 v[148:151], v247, s[16:19], s47 offen offset:1024 sc1\n\t"
        "s_waitcnt vmcnt(7)\n\t"
        "v_exp_f32 v173, v173\n\t"
        "v_cvt_pk_bf16_f32 v196, v168, v169\n\t"
        "v_exp_f32 v174, v174\n\t"
        "v_cvt_pk_bf16_f32 v197, v170, v171\n\t"
        "v_exp_f32 v175, v175\n\t"
        "v_cvt_pk_bf16_f32 v198, v172, v173\n\t"
        "v_cvt_pk_bf16_f32 v199, v174, v175\n\t"
        "v_mfma_f32_32x32x16_bf16 v[160:175], v[112:115], v[64:67], 0\n\t"
        "v_exp_f32 v176, v176\n\t"
        "v_exp_f32 v177, v177\n\t"
        "v_exp_f32 v178, v178\n\t"
        "v_exp_f32 v179, v179\n\t"
        "v_mfma_f32_32x32x16_bf16 v[0:15], v[108:111], v[196:199], v[0:15]\n\t"
        "buffer_load_dwordx4 v[152:155], v246, s[16:19], s47 offen offset:2048 sc1\n\t"
        "v_exp_f32 v180, v180\n\t"
        "v_exp_f32 v181, v181\n\t"
        "v_cvt_pk_bf16_f32 v192, v176, v177\n\t"
        "v_exp_f32 v182, v182\n\t"
        "v_exp_f32 v183, v183\n\t"
        "v_cvt_pk_bf16_f32 v193, v178, v179\n\t"
        "v_mfma_f32_32x32x16_bf16 v[160:175], v[116:119], v[68:71], v[160:175]\n\t"
        "v_exp_f32 v184, v184\n\t"
        "v_exp_f32 v185, v185\n\t"
        "v_cvt_pk_bf16_f32 v194, v180, v181\n\t"
        "v_exp_f32 v186, v186\n\t"
        "v_cvt_pk_bf16_f32 v195, v182, v183\n\t"
        "v_exp_f32 v187, v187\n\t"
        "v_exp_f32 v188, v188\n\t"
        "v_mfma_f32_32x32x16_bf16 v[16:31], v[104:107], v[192:195], v[16:31]\n\t"
        "buffer_load_dwordx4 v[156:159], v246, s[16:19], s47 offen offset:3072 sc1\n\t"
        "v_exp_f32 v189, v189\n\t"
        "v_cvt_pk_bf16_f32 v196, v184, v185\n\t"
        "v_exp_f32 v190, v190\n\t"
        "v_cvt_pk_bf16_f32 v197, v186, v187\n\t"
        "v_exp_f32 v191, v191\n\t"
        "v_cvt_pk_bf16_f32 v198, v188, v189\n\t"
        "v_cvt_pk_bf16_f32 v199, v190, v191\n\t"
        "v_mfma_f32_32x32x16_bf16 v[176:191], v[112:115], v[72:75], 0\n\t"
        "v_exp_f32 v160, v160\n\t"
        "v_exp_f32 v161, v161\n\t"
        "v_exp_f32 v162, v162\n\t"
        "v_exp_f32 v163, v163\n\t"
        "v_mfma_f32_32x32x16_bf16 v[16:31], v[108:111], v[196:199], v[16:31]\n\t"
        "s_waitcnt vmcnt(4)\n\t"
        "v_cmp_ne_u64 vcc, s[10:11], v[232:233]\n\t"
        "s_nop 0\n\t"
        "s_cbranch_vccnz sp_36_%=\n\t"
        "bk_37_%=:\n\t"
        "s_add_u32 s34, s45, s28\n\t"
        "s_add_u32 s34, s34, 2297856\n\t"
        "buffer_load_dwordx4 v[80:83], v247, s[16:19], s34 offen offset:0 sc1\n\t"
        "buffer_load_dwordx4 v[84:87], v247, s[16:19], s34 offen offset:1024 sc1\n\t"
        "s_add_u32 s34, s45, s28\n\t"
        "s_add_u32 s34, s34, 2297856\n\t"
        "s_xor_b32 s34, s34, 2048\n\t"
        "buffer_load_dwordx4 v[88:91], v247, s[16:19], s34 offen offset:0 sc1\n\t"
        "buffer_load_dwordx4 v[92:95], v247, s[16:19], s34 offen offset:1024 sc1\n\t"
        "s_sub_u32 s47, s47, 45056\n\t"
        "buffer_load_dwordx4 v[96:99], v247, s[16:19], s47 offen offset:0 sc1\n\t"
        "v_exp_f32 v164, v164\n\t"
        "v_exp_f32 v165, v165\n\t"
        "v_cvt_pk_bf16_f32 v192, v160, v161\n\t"
        "v_exp_f32 v166, v166\n\t"
        "v_exp_f32 v167, v167\n\t"
        "v_cvt_pk_bf16_f32 v193, v162, v163\n\t"
        "v_mfma_f32_32x32x16_bf16 v[176:191], v[116:119], v[76:79], v[176:191]\n\t"
        "v_exp_f32 v168, v168\n\t"
        "v_exp_f32 v169, v169\n\t"
        "v_cvt_pk_bf16_f32 v194, v164, v165\n\t"
        "v_exp_f32 v170, v170\n\t"
        "v_cvt_pk_bf16_f32 v195, v166, v167\n\t"
        "v_exp_f32 v171, v171\n\t"
        "v_exp_f32 v172, v172\n\t"
        "v_mfma_f32_32x32x16_bf16 v[0:15], v[120:123], v[192:195], v[0:15]\n\t"
        "buffer_load_dwordx4 v[100:103], v247, s[16:19], s47 offen offset:1024 sc1\n\t"
        "v_exp_f32 v173, v173\n\t"
        "v_cvt_pk_bf16_f32 v196, v168, v169\n\t"
        "v_exp_f32 v174, v174\n\t"
        "v_cvt_pk_bf16_f32 v197, v170, v171\n\t"
        "v_exp_f32 v175, v175\n\t"
        "v_cvt_pk_bf16_f32 v198, v172, v173\n\t"
        "v_cvt_pk_bf16_f32 v199, v174, v175\n\t"
        "v_mfma_f32_32x32x16_bf16 v[160:175], v[128:131], v[64:67], 0\n\t"
        "v_exp_f32 v176, v176\n\t"
        "v_exp_f32 v177, v177\n\t"
        "v_exp_f32 v178, v178\n\t"
        "v_exp_f32 v179, v179\n\t"
        "v_mfma_f32_32x32x16_bf16 v[0:15], v[124:127], v[196:199], v[0:15]\n\t"
        "buffer_load_dwordx4 v[104:107], v246, s[16:19], s47 offen offset:2048 sc1\n\t"
        "v_exp_f32 v180, v180\n\t"
        "v_exp_f32 v181, v181\n\t"
        "v_cvt_pk_bf16_f32 v192, v176, v177\n\t"
        "v_exp_f32 v182, v182\n\t"
        "v_exp_f32 v183, v183\n\t"
        "v_cvt_pk_bf16_f32 v193, v178, v179\n\t"
        "v_mfma_f32_32x32x16_bf16 v[160:175], v[132:135], v[68:71], v[160:175]\n\t"
        "v_exp_f32 v184, v184\n\t"
        "v_exp_f32 v185, v185\n\t"
        "v_cvt_pk_bf16_f32 v194, v180, v181\n\t"
        "v_exp_f32 v186, v186\n\t"
        "v_cvt_pk_bf16_f32 v195, v182, v183\n\t"
        "v_exp_f32 v187, v187\n\t"
        "v_exp_f32 v188, v188\n\t"
        "v_mfma_f32_32x32x16_bf16 v[16:31], v[120:123], v[192:195], v[16:31]\n\t"
        "buffer_load_dwordx4 v[108:111], v246, s[16:19], s47 offen offset:3072 sc1\n\t"
        "v_exp_f32 v189, v189\n\t"
        "v_cvt_pk_bf16_f32 v196, v184, v185\n\t"
        "v_exp_f32 v190, v190\n\t"
        "v_cvt_pk_bf16_f32 v197, v186, v187\n\t"
        "v_exp_f32 v191, v191\n\t"
        "v_cvt_pk_bf16_f32 v198, v188, v189\n\t"
        "v_cvt_pk_bf16_f32 v199, v190, v191\n\t"
        "v_mfma_f32_32x32x16_bf16 v[176:191], v[128:131], v[72:75], 0\n\t"
        "v_exp_f32 v160, v160\n\t"
        "v_exp_f32 v161, v161\n\t"
        "v_exp_f32 v162, v162\n\t"
        "v_exp_f32 v163, v163\n\t"
        "v_mfma_f32_32x32x16_bf16 v[16:31], v[124:127], v[196:199], v[16:31]\n\t"
        "s_cmp_lg_u32 s27, 0\n\t"
        "s_cbranch_scc1 l0_38_%=\n\t"
        "s_cmp_ge_u32 s25, 14\n\t"
        "s_mov_b32 s39, 335872\n\t"
        "s_cselect_b32 s39, 8192, s39\n\t"
        "s_add_u32 s9, s12, s39\n\t"
        "buffer_load_dwordx2 v[232:233], v237, s[16:19], s9 offen sc1\n\t"
        "l0_38_%=:\n\t"
        "s_add_u32 s47, s47, 4096\n\t"
        "buffer_load_dwordx4 v[112:115], v247, s[16:19], s47 offen offset:0 sc1\n\t"
        "v_exp_f32 v164, v164\n\t"
        "v_exp_f32 v165, v165\n\t"
        "v_cvt_pk_bf16_f32 v192, v160, v161\n\t"
        "v_exp_f32 v166, v166\n\t"
        "v_exp_f32 v167, v167\n\t"
        "v_cvt_pk_bf16_f32 v193, v162, v163\n\t"
        "v_mfma_f32_32x32x16_bf16 v[176:191], v[132:135], v[76:79], v[176:191]\n\t"
        "v_exp_f32 v168, v168\n\t"
        "v_exp_f32 v169, v169\n\t"
        "v_cvt_pk_bf16_f32 v194, v164, v165\n\t"
        "v_exp_f32 v170, v170\n\t"
        "v_cvt_pk_bf16_f32 v195, v166, v167\n\t"
        "v_exp_f32 v171, v171\n\t"
        "v_exp_f32 v172, v172\n\t"
        "v_mfma_f32_32x32x16_bf16 v[0:15], v[136:139], v[192:195], v[0:15]\n\t"
        "buffer_load_dwordx4 v[116:119], v247, s[16:19], s47 offen offset:1024 sc1\n\t"
        "s_waitcnt vmcnt(10)\n\t"
        "v_exp_f32 v173, v173\n\t"
        "v_cvt_pk_bf16_f32 v196, v168, v169\n\t"
        "v_exp_f32 v174, v174\n\t"
        "v_cvt_pk_bf16_f32 v197, v170, v171\n\t"
        "v_exp_f32 v175, v175\n\t"
        "v_cvt_pk_bf16_f32 v198, v172, v173\n\t"
        "v_cvt_pk_bf16_f32 v199, v174, v175\n\t"
        "v_mfma_f32_32x32x16_bf16 v[160:175], v[144:147], v[64:67], 0\n\t"
        "v_exp_f32 v176, v176\n\t"
        "v_exp_f32 v177, v177\n\t"
        "v_exp_f32 v178, v178\n\t"
        "v_exp_f32 v179, v179\n\t"
        "v_mfma_f32_32x32x16_bf16 v[0:15], v[140:143], v[196:199], v[0:15]\n\t"
        "buffer_load_dwordx4 v[120:123], v246, s[16:19], s47 offen offset:2048 sc1\n\t"
        "v_exp_f32 v180, v180\n\t"
        "v_exp_f32 v181, v181\n\t"
        "v_cvt_pk_bf16_f32 v192, v176, v177\n\t"
        "v_exp_f32 v182, v182\n\t"
        "v_exp_f32 v183, v183\n\t"
        "v_cvt_pk_bf16_f32 v193, v178, v179\n\t"
        "v_mfma_f32_32x32x16_bf16 v[160:175], v[148:151], v[68:71], v[160:175]\n\t"
        "v_exp_f32 v184, v184\n\t"
        "v_exp_f32 v185, v185\n\t"
        "v_cvt_pk_bf16_f32 v194, v180, v181\n\t"
        "v_exp_f32 v186, v186\n\t"
        "v_cvt_pk_bf16_f32 v195, v182, v183\n\t"
        "v_exp_f32 v187, v187\n\t"
        "v_exp_f32 v188, v188\n\t"
        "v_mfma_f32_32x32x16_bf16 v[16:31], v[136:139], v[192:195], v[16:31]\n\t"
        "buffer_load_dwordx4 v[124:127], v246, s[16:19], s47 offen offset:3072 sc1\n\t"
        "v_exp_f32 v189, v189\n\t"
        "v_cvt_pk_bf16_f32 v196, v184, v185\n\t"
        "v_exp_f32 v190, v190\n\t"
        "v_cvt_pk_bf16_f32 v197, v186, v187\n\t"
        "v_exp_f32 v191, v191\n\t"
        "v_cvt_pk_bf16_f32 v198, v188, v189\n\t"
        "v_cvt_pk_bf16_f32 v199, v190, v191\n\t"
        "v_mfma_f32_32x32x16_bf16 v[176:191], v[144:147], v[72:75], 0\n\t"
        "v_exp_f32 v160, v160\n\t"
        "v_exp_f32 v161, v161\n\t"
        "v_exp_f32 v162, v162\n\t"
        "v_exp_f32 v163, v163\n\t"
        "v_mfma_f32_32x32x16_bf16 v[16:31], v[140:143], v[196:199], v[16:31]\n\t"
        "s_add_u32 s47, s47, 12288\n\t"
        "buffer_load_dwordx4 v[128:131], v247, s[16:19], s47 offen offset:0 sc1\n\t"
        "v_exp_f32 v164, v164\n\t"
        "v_exp_f32 v165, v165\n\t"
        "v_cvt_pk_bf16_f32 v192, v160, v161\n\t"
        "v_exp_f32 v166, v166\n\t"
        "v_exp_f32 v167, v167\n\t"
        "v_cvt_pk_bf16_f32 v193, v162, v163\n\t"
        "v_mfma_f32_32x32x16_bf16 v[176:191], v[148:151], v[76:79], v[176:191]\n\t"
        "v_exp_f32 v168, v168\n\t"
        "v_exp_f32 v169, v169\n\t"
        "v_cvt_pk_bf16_f32 v194, v164, v165\n\t"
        "v_exp_f32 v170, v170\n\t"
        "v_cvt_pk_bf16_f32 v195, v166, v167\n\t"
        "v_exp_f32 v171, v171\n\t"
        "v_exp_f32 v172, v172\n\t"
        "v_mfma_f32_32x32x16_bf16 v[0:15], v[152:155], v[192:195], v[0:15]\n\t"
        "buffer_load_dwordx4 v[132:135], v247, s[16:19], s47 offen offset:1024 sc1\n\t"
        "v_exp_f32 v173, v173\n\t"
        "v_cvt_pk_bf16_f32 v196, v168, v169\n\t"
        "v_exp_f32 v174, v174\n\t"
        "v_cvt_pk_bf16_f32 v197, v170, v171\n\t"
        "v_exp_f32 v175, v175\n\t"
        "v_cvt_pk_bf16_f32 v198, v172, v173\n\t"
        "v_cvt_pk_bf16_f32 v199, v174, v175\n\t"
        "s_waitcnt vmcnt(13)\n\t"
        "v_mfma_f32_32x32x16_bf16 v[160:175], v[144:147], v[80:83], 0\n\t"
        "v_exp_f32 v176, v176\n\t"
        "v_exp_f32 v177, v177\n\t"
        "v_exp_f32 v178, v178\n\t"
        "v_exp_f32 v179, v179\n\t"
        "v_mfma_f32_32x32x16_bf16 v[0:15], v[156:159], v[196:199], v[0:15]\n\t"
        "buffer_load_dwordx4 v[136:139], v246, s[16:19], s47 offen offset:2048 sc1\n\t"
        "v_exp_f32 v180, v180\n\t"
        "v_exp_f32 v181, v181\n\t"
        "v_cvt_pk_bf16_f32 v192, v176, v177\n\t"
        "v_exp_f32 v182, v182\n\t"
        "v_exp_f32 v183, v183\n\t"
        "v_cvt_pk_bf16_f32 v193, v178, v179\n\t"
        "s_waitcnt vmcnt(13)\n\t"
        "v_mfma_f32_32x32x16_bf16 v[160:175], v[148:151], v[84:87], v[160:175]\n\t"
        "v_exp_f32 v184, v184\n\t"
        "v_exp_f32 v185, v185\n\t"
        "v_cvt_pk_bf16_f32 v194, v180, v181\n\t"
        "v_exp_f32 v186, v186\n\t"
        "v_cvt_pk_bf16_f32 v195, v182, v183\n\t"
        "v_exp_f32 v187, v187\n\t"
        "v_exp_f32 v188, v188\n\t"
        "v_mfma_f32_32x32x16_bf16 v[16:31], v[152:155], v[192:195], v[16:31]\n\t"
        "buffer_load_dwordx4 v[140:143], v246, s[16:19], s47 offen offset:3072 sc1\n\t"
        "v_exp_f32 v189, v189\n\t"
        "v_cvt_pk_bf16_f32 v196, v184, v185\n\t"
        "v_exp_f32 v190, v190\n\t"
        "v_cvt_pk_bf16_f32 v197, v186, v187\n\t"
        "v_exp_f32 v191, v191\n\t"
        "v_cvt_pk_bf16_f32 v198, v188, v189\n\t"
        "v_cvt_pk_bf16_f32 v199, v190, v191\n\t"
        "s_waitcnt vmcnt(13)\n\t"
        "v_mfma_f32_32x32x16_bf16 v[176:191], v[144:147], v[88:91], 0\n\t"
        "v_exp_f32 v160, v160\n\t"
        "v_exp_f32 v161, v161\n\t"
        "v_exp_f32 v162, v162\n\t"
        "v_exp_f32 v163, v163\n\t"
        "v_mfma_f32_32x32x16_bf16 v[16:31], v[156:159], v[196:199], v[16:31]\n\t"
        "v_exp_f32 v164, v164\n\t"
        "v_exp_f32 v165, v165\n\t"
        "v_cvt_pk_bf16_f32 v192, v160, v161\n\t"
        "v_exp_f32 v166, v166\n\t"
        "v_exp_f32 v167, v167\n\t"
        "v_cvt_pk_bf16_f32 v193, v162, v163\n\t"
        "s_waitcnt vmcnt(12)\n\t"
        "v_mfma_f32_32x32x16_bf16 v[176:191], v[148:151], v[92:95], v[176:191]\n\t"
        "v_exp_f32 v168, v168\n\t"
        "v_exp_f32 v169, v169\n\t"
        "v_cvt_pk_bf16_f32 v194, v164, v165\n\t"
        "v_exp_f32 v170, v170\n\t"
        "v_cvt_pk_bf16_f32 v195, v166, v167\n\t"
        "v_exp_f32 v171, v171\n\t"
        "v_exp_f32 v172, v172\n\t"
        "v_mfma_f32_32x32x16_bf16 v[32:47], v[152:155], v[192:195], 0\n\t"
        "s_waitcnt vmcnt(8)\n\t"
        "v_exp_f32 v173, v173\n\t"
        "v_cvt_pk_bf16_f32 v196, v168, v169\n\t"
        "v_exp_f32 v174, v174\n\t"
        "v_cvt_pk_bf16_f32 v197, v170, v171\n\t"
        "v_exp_f32 v175, v175\n\t"
        "v_cvt_pk_bf16_f32 v198, v172, v173\n\t"
        "v_cvt_pk_bf16_f32 v199, v174, v175\n\t"
        "v_mfma_f32_32x32x16_bf16 v[160:175], v[96:99], v[64:67], 0\n\t"
        "v_exp_f32 v176, v176\n\t"
        "v_exp_f32 v177, v177\n\t"
        "v_exp_f32 v178, v178\n\t"
        "v_exp_f32 v179, v179\n\t"
        "v_mfma_f32_32x32x16_bf16 v[32:47], v[156:159], v[196:199], v[32:47]\n\t"
        "v_exp_f32 v180, v180\n\t"
        "v_exp_f32 v181, v181\n\t"
        "v_cvt_pk_bf16_f32 v192, v176, v177\n\t"
        "v_exp_f32 v182, v182\n\t"
        "v_exp_f32 v183, v183\n\t"
        "v_cvt_pk_bf16_f32 v193, v178, v179\n\t"
        "v_mfma_f32_32x32x16_bf16 v[160:175], v[100:103], v[68:71], v[160:175]\n\t"
        "v_exp_f32 v184, v184\n\t"
        "v_exp_f32 v185, v185\n\t"
        "v_cvt_pk_bf16_f32 v194, v180, v181\n\t"
        "v_exp_f32 v186, v186\n\t"
        "v_cvt_pk_bf16_f32 v195, v182, v183\n\t"
        "v_exp_f32 v187, v187\n\t"
        "v_exp_f32 v188, v188\n\t"
        "v_mfma_f32_32x32x16_bf16 v[48:63], v[152:155], v[192:195], 0\n\t"
        "v_exp_f32 v189, v189\n\t"
        "v_cvt_pk_bf16_f32 v196, v184, v185\n\t"
        "v_exp_f32 v190, v190\n\t"
        "v_cvt_pk_bf16_f32 v197, v186, v187\n\t"
        "v_exp_f32 v191, v191\n\t"
        "v_cvt_pk_bf16_f32 v198, v188, v189\n\t"
        "v_cvt_pk_bf16_f32 v199, v190, v191\n\t"
        "v_mfma_f32_32x32x16_bf16 v[176:191], v[96:99], v[72:75], 0\n\t"
        "v_exp_f32 v160, v160\n\t"
        "v_exp_f32 v161, v161\n\t"
        "v_exp_f32 v162, v162\n\t"
        "v_exp_f32 v163, v163\n\t"
        "v_mfma_f32_32x32x16_bf16 v[48:63], v[156:159], v[196:199], v[48:63]\n\t"
        "s_add_u32 s47, s47, 4096\n\t"
        "buffer_load_dwordx4 v[144:147], v247, s[16:19], s47 offen offset:0 sc1\n\t"
        "v_exp_f32 v164, v164\n\t"
        "v_exp_f32 v165, v165\n\t"
        "v_cvt_pk_bf16_f32 v192, v160, v161\n\t"
        "v_exp_f32 v166, v166\n\t"
        "v_exp_f32 v167, v167\n\t"
        "v_cvt_pk_bf16_f32 v193, v162, v163\n\t"
        "v_mfma_f32_32x32x16_bf16 v[176:191], v[100:103], v[76:79], v[176:191]\n\t"
        "v_exp_f32 v168, v168\n\t"
        "v_exp_f32 v169, v169\n\t"
        "v_cvt_pk_bf16_f32 v194, v164, v165\n\t"
        "v_exp_f32 v170, v170\n\t"
        "v_cvt_pk_bf16_f32 v195, v166, v167\n\t"
        "v_exp_f32 v171, v171\n\t"
        "v_exp_f32 v172, v172\n\t"
        "v_mfma_f32_32x32x16_bf16 v[0:15], v[104:107], v[192:195], v[0:15]\n\t"
        "buffer_load_dwordx4 v[148:151], v247, s[16:19], s47 offen offset:1024 sc1\n\t"
        "v_exp_f32 v173, v173\n\t"
        "v_cvt_pk_bf16_f32 v196, v168, v169\n\t"
        "v_exp_f32 v174, v174\n\t"
        "v_cvt_pk_bf16_f32 v197, v170, v171\n\t"
        "v_exp_f32 v175, v175\n\t"
        "v_cvt_pk_bf16_f32 v198, v172, v173\n\t"
        "v_cvt_pk_bf16_f32 v199, v174, v175\n\t"
        "v_mfma_f32_32x32x16_bf16 v[160:175], v[96:99], v[80:83], 0\n\t"
        "v_exp_f32 v176, v176\n\t"
        "v_exp_f32 v177, v177\n\t"
        "v_exp_f32 v178, v178\n\t"
        "v_exp_f32 v179, v179\n\t"
        "v_mfma_f32_32x32x16_bf16 v[0:15], v[108:111], v[196:199], v[0:15]\n\t"
        "buffer_load_dwordx4 v[152:155], v246, s[16:19], s47 offen offset:2048 sc1\n\t"
        "v_exp_f32 v180, v180\n\t"
        "v_exp_f32 v181, v181\n\t"
        "v_cvt_pk_bf16_f32 v192, v176, v177\n\t"
        "v_exp_f32 v182, v182\n\t"
        "v_exp_f32 v183, v183\n\t"
        "v_cvt_pk_bf16_f32 v193, v178, v179\n\t"
        "v_mfma_f32_32x32x16_bf16 v[160:175], v[100:103], v[84:87], v[160:175]\n\t"
        "v_exp_f32 v184, v184\n\t"
        "v_exp_f32 v185, v185\n\t"
        "v_cvt_pk_bf16_f32 v194, v180, v181\n\t"
        "v_exp_f32 v186, v186\n\t"
        "v_cvt_pk_bf16_f32 v195, v182, v183\n\t"
        "v_exp_f32 v187, v187\n\t"
        "v_exp_f32 v188, v188\n\t"
        "v_mfma_f32_32x32x16_bf16 v[16:31], v[104:107], v[192:195], v[16:31]\n\t"
        "buffer_load_dwordx4 v[156:159], v246, s[16:19], s47 offen offset:3072 sc1\n\t"
        "v_exp_f32 v189, v189\n\t"
        "v_cvt_pk_bf16_f32 v196, v184, v185\n\t"
        "v_exp_f32 v190, v190\n\t"
        "v_cvt_pk_bf16_f32 v197, v186, v187\n\t"
        "v_exp_f32 v191, v191\n\t"
        "v_cvt_pk_bf16_f32 v198, v188, v189\n\t"
        "v_cvt_pk_bf16_f32 v199, v190, v191\n\t"
        "v_mfma_f32_32x32x16_bf16 v[176:191], v[96:99], v[88:91], 0\n\t"
        "v_exp_f32 v160, v160\n\t"
        "v_exp_f32 v161, v161\n\t"
        "v_exp_f32 v162, v162\n\t"
        "v_exp_f32 v163, v163\n\t"
        "v_mfma_f32_32x32x16_bf16 v[16:31], v[108:111], v[196:199], v[16:31]\n\t"
        "s_cmp_lg_u32 s27, 0\n\t"
        "s_cbranch_scc1 l0_39_%=\n\t"
        "s_waitcnt vmcnt(12)\n\t"
        "v_cmp_ne_u64 vcc, s[10:11], v[232:233]\n\t"
        "s_nop 0\n\t"
        "s_cbranch_vccnz sp_40_%=\n\t"
        "bk_41_%=:\n\t"
        "l0_39_%=:\n\t"
        "s_cmp_lg_u32 s27, 0\n\t"
        "s_cbranch_scc1 l0_42_%=\n\t"
        "buffer_load_dwordx4 a[0:3], v246, s[16:19], s98 offen offset:0 sc1\n\t"
        "buffer_load_dwordx4 a[112:115], v246, s[16:19], s99 offen offset:0 sc1\n\t"
        "buffer_load_dwordx4 a[4:7], v246, s[16:19], s98 offen offset:1024 sc1\n\t"
        "buffer_load_dwordx4 a[116:119], v246, s[16:19], s99 offen offset:1024 sc1\n\t"
        "buffer_load_dwordx4 a[8:11], v246, s[16:19], s98 offen offset:2048 sc1\n\t"
        "l0_42_%=:\n\t"
        "v_exp_f32 v164, v164\n\t"
        "v_exp_f32 v165, v165\n\t"
        "v_cvt_pk_bf16_f32 v192, v160, v161\n\t"
        "v_exp_f32 v166, v166\n\t"
        "v_exp_f32 v167, v167\n\t"
        "v_cvt_pk_bf16_f32 v193, v162, v163\n\t"
        "v_mfma_f32_32x32x16_bf16 v[176:191], v[100:103], v[92:95], v[176:191]\n\t"
        "v_exp_f32 v168, v168\n\t"
        "v_exp_f32 v169, v169\n\t"
        "v_cvt_pk_bf16_f32 v194, v164, v165\n\t"
        "v_exp_f32 v170, v170\n\t"
        "v_cvt_pk_bf16_f32 v195, v166, v167\n\t"
        "v_exp_f32 v171, v171\n\t"
        "v_exp_f32 v172, v172\n\t"
        "v_mfma_f32_32x32x16_bf16 v[32:47], v[104:107], v[192:195], v[32:47]\n\t"
        "s_waitcnt vmcnt(8)\n\t"
        "v_exp_f32 v173, v173\n\t"
        "v_cvt_pk_bf16_f32 v196, v168, v169\n\t"
        "v_exp_f32 v174, v174\n\t"
        "v_cvt_pk_bf16_f32 v197, v170, v171\n\t"
        "v_exp_f32 v175, v175\n\t"
        "v_cvt_pk_bf16_f32 v198, v172, v173\n\t"
        "v_cvt_pk_bf16_f32 v199, v174, v175\n\t"
        "v_mfma_f32_32x32x16_bf16 v[160:175], v[112:115], v[64:67], 0\n\t"
        "v_exp_f32 v176, v176\n\t"
        "v_exp_f32 v177, v177\n\t"
        "v_exp_f32 v178, v178\n\t"
        "v_exp_f32 v179, v179\n\t"
        "v_mfma_f32_32x32x16_bf16 v[32:47], v[108:111], v[196:199], v[32:47]\n\t"
        "v_exp_f32 v180, v180\n\t"
        "v_exp_f32 v181, v181\n\t"
        "v_cvt_pk_bf16_f32 v192, v176, v177\n\t"
        "v_exp_f32 v182, v182\n\t"
        "v_exp_f32 v183, v183\n\t"
        "v_cvt_pk_bf16_f32 v193, v178, v179\n\t"
        "v_mfma_f32_32x32x16_bf16 v[160:175], v[116:119], v[68:71], v[160:175]\n\t"
        "v_exp_f32 v184, v184\n\t"
        "v_exp_f32 v185, v185\n\t"
        "v_cvt_pk_bf16_f32 v194, v180, v181\n\t"
        "v_exp_f32 v186, v186\n\t"
        "v_cvt_pk_bf16_f32 v195, v182, v183\n\t"
        "v_exp_f32 v187, v187\n\t"
        "v_exp_f32 v188, v188\n\t"
        "v_mfma_f32_32x32x16_bf16 v[48:63], v[104:107], v[192:195], v[48:63]\n\t"
        "v_exp_f32 v189, v189\n\t"
        "v_cvt_pk_bf16_f32 v196, v184, v185\n\t"
        "v_exp_f32 v190, v190\n\t"
        "v_cvt_pk_bf16_f32 v197, v186, v187\n\t"
        "v_exp_f32 v191, v191\n\t"
        "v_cvt_pk_bf16_f32 v198, v188, v189\n\t"
        "v_cvt_pk_bf16_f32 v199, v190, v191\n\t"
        "v_mfma_f32_32x32x16_bf16 v[176:191], v[112:115], v[72:75], 0\n\t"
        "v_exp_f32 v160, v160\n\t"
        "v_exp_f32 v161, v161\n\t"
        "v_exp_f32 v162, v162\n\t"
        "v_exp_f32 v163, v163\n\t"
        "v_mfma_f32_32x32x16_bf16 v[48:63], v[108:111], v[196:199], v[48:63]\n\t"
        "s_add_u32 s47, s47, 12288\n\t"
        "buffer_load_dwordx4 v[96:99], v247, s[16:19], s47 offen offset:0 sc1\n\t"
        "v_exp_f32 v164, v164\n\t"
        "v_exp_f32 v165, v165\n\t"
        "v_cvt_pk_bf16_f32 v192, v160, v161\n\t"
        "v_exp_f32 v166, v166\n\t"
        "v_exp_f32 v167, v167\n\t"
        "v_cvt_pk_bf16_f32 v193, v162, v163\n\t"
        "v_mfma_f32_32x32x16_bf16 v[176:191], v[116:119], v[76:79], v[176:191]\n\t"
        "v_exp_f32 v168, v168\n\t"
        "v_exp_f32 v169, v169\n\t"
        "v_cvt_pk_bf16_f32 v194, v164, v165\n\t"
        "v_exp_f32 v170, v170\n\t"
        "v_cvt_pk_bf16_f32 v195, v166, v167\n\t"
        "v_exp_f32 v171, v171\n\t"
        "v_exp_f32 v172, v172\n\t"
        "v_mfma_f32_32x32x16_bf16 v[0:15], v[120:123], v[192:195], v[0:15]\n\t"
        "buffer_load_dwordx4 v[100:103], v247, s[16:19], s47 offen offset:1024 sc1\n\t"
        "v_exp_f32 v173, v173\n\t"
        "v_cvt_pk_bf16_f32 v196, v168, v169\n\t"
        "v_exp_f32 v174, v174\n\t"
        "v_cvt_pk_bf16_f32 v197, v170, v171\n\t"
        "v_exp_f32 v175, v175\n\t"
        "v_cvt_pk_bf16_f32 v198, v172, v173\n\t"
        "v_cvt_pk_bf16_f32 v199, v174, v175\n\t"
        "v_mfma_f32_32x32x16_bf16 v[160:175], v[112:115], v[80:83], 0\n\t"
        "v_exp_f32 v176, v176\n\t"
        "v_exp_f32 v177, v177\n\t"
        "v_exp_f32 v178, v178\n\t"
        "v_exp_f32 v179, v179\n\t"
        "v_mfma_f32_32x32x16_bf16 v[0:15], v[124:127], v[196:199], v[0:15]\n\t"
        "buffer_load_dwordx4 v[104:107], v246, s[16:19], s47 offen offset:2048 sc1\n\t"
        "v_exp_f32 v180, v180\n\t"
        "v_exp_f32 v181, v181\n\t"
        "v_cvt_pk_bf16_f32 v192, v176, v177\n\t"
        "v_exp_f32 v182, v182\n\t"
        "v_exp_f32 v183, v183\n\t"
        "v_cvt_pk_bf16_f32 v193, v178, v179\n\t"
        "v_mfma_f32_32x32x16_bf16 v[160:175], v[116:119], v[84:87], v[160:175]\n\t"
        "v_exp_f32 v184, v184\n\t"
        "v_exp_f32 v185, v185\n\t"
        "v_cvt_pk_bf16_f32 v194, v180, v181\n\t"
        "v_exp_f32 v186, v186\n\t"
        "v_cvt_pk_bf16_f32 v195, v182, v183\n\t"
        "v_exp_f32 v187, v187\n\t"
        "v_exp_f32 v188, v188\n\t"
        "v_mfma_f32_32x32x16_bf16 v[16:31], v[120:123], v[192:195], v[16:31]\n\t"
        "buffer_load_dwordx4 v[108:111], v246, s[16:19], s47 offen offset:3072 sc1\n\t"
        "v_exp_f32 v189, v189\n\t"
        "v_cvt_pk_bf16_f32 v196, v184, v185\n\t"
        "v_exp_f32 v190, v190\n\t"
        "v_cvt_pk_bf16_f32 v197, v186, v187\n\t"
        "v_exp_f32 v191, v191\n\t"
        "v_cvt_pk_bf16_f32 v198, v188, v189\n\t"
        "v_cvt_pk_bf16_f32 v199, v190, v191\n\t"
        "v_mfma_f32_32x32x16_bf16 v[176:191], v[112:115], v[88:91], 0\n\t"
        "v_exp_f32 v160, v160\n\t"
        "v_exp_f32 v161, v161\n\t"
        "v_exp_f32 v162, v162\n\t"
        "v_exp_f32 v163, v163\n\t"
        "v_mfma_f32_32x32x16_bf16 v[16:31], v[124:127], v[196:199], v[16:31]\n\t"
        "s_mul_i32 s35, s25, 114688\n\t"
        "s_mul_i32 s41, s26, 28672\n\t"
        "s_add_u32 s35, s35, s41\n\t"
        "s_add_u32 s35, s35, 2203648\n\t"
        "s_mov_b32 m0, s41\n\t"
        "s_nop 0\n\t"
        "buffer_load_dwordx4 v247, s[16:19], s35 offen offset:0 sc1 lds\n\t"
        "s_cmp_lg_u32 s27, 0\n\t"
        "s_cbranch_scc1 l0_43_%=\n\t"
        "buffer_load_dwordx4 a[120:123], v246, s[16:19], s99 offen offset:2048 sc1\n\t"
        "buffer_load_dwordx4 a[12:15], v246, s[16:19], s98 offen offset:3072 sc1\n\t"
        "buffer_load_dwordx4 a[124:127], v246, s[16:19], s99 offen offset:3072 sc1\n\t"
        "s_add_u32 s98, s98, 4096\n\t"
        "buffer_load_dwordx4 a[16:19], v246, s[16:19], s98 offen offset:0 sc1\n\t"
        "s_add_u32 s99, s99, 4096\n\t"
        "buffer_load_dwordx4 a[128:131], v246, s[16:19], s99 offen offset:0 sc1\n\t"
        "l0_43_%=:\n\t"
        "v_exp_f32 v164, v164\n\t"
        "v_exp_f32 v165, v165\n\t"
        "v_cvt_pk_bf16_f32 v192, v160, v161\n\t"
        "v_exp_f32 v166, v166\n\t"
        "v_exp_f32 v167, v167\n\t"
        "v_cvt_pk_bf16_f32 v193, v162, v163\n\t"
        "v_mfma_f32_32x32x16_bf16 v[176:191], v[116:119], v[92:95], v[176:191]\n\t"
        "buffer_load_dwordx4 v247, s[16:19], s35 offen offset:1024 sc1 lds\n\t"
        "v_exp_f32 v168, v168\n\t"
        "v_exp_f32 v169, v169\n\t"
        "v_cvt_pk_bf16_f32 v194, v164, v165\n\t"
        "v_exp_f32 v170, v170\n\t"
        "v_cvt_pk_bf16_f32 v195, v166, v167\n\t"
        "v_exp_f32 v171, v171\n\t"
        "v_exp_f32 v172, v172\n\t"
        "v_mfma_f32_32x32x16_bf16 v[32:47], v[120:123], v[192:195], v[32:47]\n\t"
        "buffer_load_dwordx4 v247, s[16:19], s35 offen offset:2048 sc1 lds\n\t"
        "s_waitcnt vmcnt(11)\n\t"
        "v_exp_f32 v173, v173\n\t"
        "v_cvt_pk_bf16_f32 v196, v168, v169\n\t"
        "v_exp_f32 v174, v174\n\t"
        "v_cvt_pk_bf16_f32 v197, v170, v171\n\t"
        "v_exp_f32 v175, v175\n\t"
        "v_cvt_pk_bf16_f32 v198, v172, v173\n\t"
        "v_cvt_pk_bf16_f32 v199, v174, v175\n\t"
        "v_mfma_f32_32x32x16_bf16 v[160:175], v[128:131], v[64:67], 0\n\t"
        "v_exp_f32 v176, v176\n\t"
        "v_exp_f32 v177, v177\n\t"
        "v_exp_f32 v178, v178\n\t"
        "v_exp_f32 v179, v179\n\t"
        "v_mfma_f32_32x32x16_bf16 v[32:47], v[124:127], v[196:199], v[32:47]\n\t"
        "buffer_load_dwordx4 v247, s[16:19], s35 offen offset:3072 sc1 lds\n\t"
        "v_exp_f32 v180, v180\n\t"
        "v_exp_f32 v181, v181\n\t"
        "v_cvt_pk_bf16_f32 v192, v176, v177\n\t"
        "v_exp_f32 v182, v182\n\t"
        "v_exp_f32 v183, v183\n\t"
        "v_cvt_pk_bf16_f32 v193, v178, v179\n\t"
        "v_mfma_f32_32x32x16_bf16 v[160:175], v[132:135], v[68:71], v[160:175]\n\t"
        "s_add_u32 s35, s35, 4096\n\t"
        "s_add_u32 m0, m0, 4096\n\t"
        "s_nop 0\n\t"
        "buffer_load_dwordx4 v247, s[16:19], s35 offen offset:0 sc1 lds\n\t"
        "v_exp_f32 v184, v184\n\t"
        "v_exp_f32 v185, v185\n\t"
        "v_cvt_pk_bf16_f32 v194, v180, v181\n\t"
        "v_exp_f32 v186, v186\n\t"
        "v_cvt_pk_bf16_f32 v195, v182, v183\n\t"
        "v_exp_f32 v187, v187\n\t"
        "v_exp_f32 v188, v188\n\t"
        "v_mfma_f32_32x32x16_bf16 v[48:63], v[120:123], v[192:195], v[48:63]\n\t"
        "buffer_load_dwordx4 v247, s[16:19], s35 offen offset:1024 sc1 lds\n\t"
        "v_exp_f32 v189, v189\n\t"
        "v_cvt_pk_bf16_f32 v196, v184, v185\n\t"
        "v_exp_f32 v190, v190\n\t"
        "v_cvt_pk_bf16_f32 v197, v186, v187\n\t"
        "v_exp_f32 v191, v191\n\t"
        "v_cvt_pk_bf16_f32 v198, v188, v189\n\t"
        "v_cvt_pk_bf16_f32 v199, v190, v191\n\t"
        "v_mfma_f32_32x32x16_bf16 v[176:191], v[128:131], v[72:75], 0\n\t"
        "v_exp_f32 v160, v160\n\t"
        "v_exp_f32 v161, v161\n\t"
        "v_exp_f32 v162, v162\n\t"
        "v_exp_f32 v163, v163\n\t"
        "v_mfma_f32_32x32x16_bf16 v[48:63], v[124:127], v[196:199], v[48:63]\n\t"
        "s_add_u32 s47, s47, 4096\n\t"
        "buffer_load_dwordx4 v[112:115], v247, s[16:19], s47 offen offset:0 sc1\n\t"
        "v_exp_f32 v164, v164\n\t"
        "v_exp_f32 v165, v165\n\t"
        "v_cvt_pk_bf16_f32 v192, v160, v161\n\t"
        "v_exp_f32 v166, v166\n\t"
        "v_exp_f32 v167, v167\n\t"
        "v_cvt_pk_bf16_f32 v193, v162, v163\n\t"
        "v_mfma_f32_32x32x16_bf16 v[176:191], v[132:135], v[76:79], v[176:191]\n\t"
        "v_exp_f32 v168, v168\n\t"
        "v_exp_f32 v169, v169\n\t"
        "v_cvt_pk_bf16_f32 v194, v164, v165\n\t"
        "v_exp_f32 v170, v170\n\t"
        "v_cvt_pk_bf16_f32 v195, v166, v167\n\t"
        "v_exp_f32 v171, v171\n\t"
        "v_exp_f32 v172, v172\n\t"
        "v_mfma_f32_32x32x16_bf16 v[0:15], v[136:139], v[192:195], v[0:15]\n\t"
        "buffer_load_dwordx4 v[116:119], v247, s[16:19], s47 offen offset:1024 sc1\n\t"
        "v_exp_f32 v173, v173\n\t"
        "v_cvt_pk_bf16_f32 v196, v168, v169\n\t"
        "v_exp_f32 v174, v174\n\t"
        "v_cvt_pk_bf16_f32 v197, v170, v171\n\t"
        "v_exp_f32 v175, v175\n\t"
        "v_cvt_pk_bf16_f32 v198, v172, v173\n\t"
        "v_cvt_pk_bf16_f32 v199, v174, v175\n\t"
        "v_mfma_f32_32x32x16_bf16 v[160:175], v[128:131], v[80:83], 0\n\t"
        "v_exp_f32 v176, v176\n\t"
        "v_exp_f32 v177, v177\n\t"
        "v_exp_f32 v178, v178\n\t"
        "v_exp_f32 v179, v179\n\t"
        "v_mfma_f32_32x32x16_bf16 v[0:15], v[140:143], v[196:199], v[0:15]\n\t"
        "buffer_load_dwordx4 v[120:123], v246, s[16:19], s47 offen offset:2048 sc1\n\t"
        "v_exp_f32 v180, v180\n\t"
        "v_exp_f32 v181, v181\n\t"
        "v_cvt_pk_bf16_f32 v192, v176, v177\n\t"
        "v_exp_f32 v182, v182\n\t"
        "v_exp_f32 v183, v183\n\t"
        "v_cvt_pk_bf16_f32 v193, v178, v179\n\t"
        "v_mfma_f32_32x32x16_bf16 v[160:175], v[132:135], v[84:87], v[160:175]\n\t"
        "v_exp_f32 v184, v184\n\t"
        "v_exp_f32 v185, v185\n\t"
        "v_cvt_pk_bf16_f32 v194, v180, v181\n\t"
        "v_exp_f32 v186, v186\n\t"
        "v_cvt_pk_bf16_f32 v195, v182, v183\n\t"
        "v_exp_f32 v187, v187\n\t"
        "v_exp_f32 v188, v188\n\t"
        "v_mfma_f32_32x32x16_bf16 v[16:31], v[136:139], v[192:195], v[16:31]\n\t"
        "buffer_load_dwordx4 v[124:127], v246, s[16:19], s47 offen offset:3072 sc1\n\t"
        "v_exp_f32 v189, v189\n\t"
        "v_cvt_pk_bf16_f32 v196, v184, v185\n\t"
        "v_exp_f32 v190, v190\n\t"
        "v_cvt_pk_bf16_f32 v197, v186, v187\n\t"
        "v_exp_f32 v191, v191\n\t"
        "v_cvt_pk_bf16_f32 v198, v188, v189\n\t"
        "v_cvt_pk_bf16_f32 v199, v190, v191\n\t"
        "v_mfma_f32_32x32x16_bf16 v[176:191], v[128:131], v[88:91], 0\n\t"
        "v_exp_f32 v160, v160\n\t"
        "v_exp_f32 v161, v161\n\t"
        "v_exp_f32 v162, v162\n\t"
        "v_exp_f32 v163, v163\n\t"
        "v_mfma_f32_32x32x16_bf16 v[16:31], v[140:143], v[196:199], v[16:31]\n\t"
        "buffer_load_dwordx4 v247, s[16:19], s35 offen offset:2048 sc1 lds\n\t"
        "s_cmp_lg_u32 s27, 0\n\t"
        "s_cbranch_scc1 l0_44_%=\n\t"
        "buffer_load_dwordx4 a[20:23], v246, s[16:19], s98 offen offset:1024 sc1\n\t"
        "buffer_load_dwordx4 a[132:135], v246, s[16:19], s99 offen offset:1024 sc1\n\t"
        "buffer_load_dwordx4 a[24:27], v246, s[16:19], s98 offen offset:2048 sc1\n\t"
        "buffer_load_dwordx4 a[136:139], v246, s[16:19], s99 offen offset:2048 sc1\n\t"
        "buffer_load_dwordx4 a[28:31], v246, s[16:19], s98 offen offset:3072 sc1\n\t"
        "l0_44_%=:\n\t"
        "v_exp_f32 v164, v164\n\t"
        "v_exp_f32 v165, v165\n\t"
        "v_cvt_pk_bf16_f32 v192, v160, v161\n\t"
        "v_exp_f32 v166, v166\n\t"
        "v_exp_f32 v167, v167\n\t"
        "v_cvt_pk_bf16_f32 v193, v162, v163\n\t"
        "v_mfma_f32_32x32x16_bf16 v[176:191], v[132:135], v[92:95], v[176:191]\n\t"
        "buffer_load_dwordx4 v247, s[16:19], s35 offen offset:3072 sc1 lds\n\t"
        "v_exp_f32 v168, v168\n\t"
        "v_exp_f32 v169, v169\n\t"
        "v_cvt_pk_bf16_f32 v194, v164, v165\n\t"
        "v_exp_f32 v170, v170\n\t"
        "v_cvt_pk_bf16_f32 v195, v166, v167\n\t"
        "v_exp_f32 v171, v171\n\t"
        "v_exp_f32 v172, v172\n\t"
        "v_mfma_f32_32x32x16_bf16 v[32:47], v[136:139], v[192:195], v[32:47]\n\t"
        "s_add_u32 s35, s35, 4096\n\t"
        "s_add_u32 m0, m0, 4096\n\t"
        "s_nop 0\n\t"
        "buffer_load_dwordx4 v247, s[16:19], s35 offen offset:0 sc1 lds\n\t"
        "s_waitcnt vmcnt(17)\n\t"
        "v_exp_f32 v173, v173\n\t"
        "v_cvt_pk_bf16_f32 v196, v168, v169\n\t"
        "v_exp_f32 v174, v174\n\t"
        "v_cvt_pk_bf16_f32 v197, v170, v171\n\t"
        "v_exp_f32 v175, v175\n\t"
        "v_cvt_pk_bf16_f32 v198, v172, v173\n\t"
        "v_cvt_pk_bf16_f32 v199, v174, v175\n\t"
        "v_mfma_f32_32x32x16_bf16 v[160:175], v[144:147], v[64:67], 0\n\t"
        "v_exp_f32 v176, v176\n\t"
        "v_exp_f32 v177, v177\n\t"
        "v_exp_f32 v178, v178\n\t"
        "v_exp_f32 v179, v179\n\t"
        "v_mfma_f32_32x32x16_bf16 v[32:47], v[140:143], v[196:199], v[32:47]\n\t"
        "buffer_load_dwordx4 v247, s[16:19], s35 offen offset:1024 sc1 lds\n\t"
        "v_exp_f32 v180, v180\n\t"
        "v_exp_f32 v181, v181\n\t"
        "v_cvt_pk_bf16_f32 v192, v176, v177\n\t"
        "v_exp_f32 v182, v182\n\t"
        "v_exp_f32 v183, v183\n\t"
        "v_cvt_pk_bf16_f32 v193, v178, v179\n\t"
        "v_mfma_f32_32x32x16_bf16 v[160:175], v[148:151], v[68:71], v[160:175]\n\t"
        "buffer_load_dwordx4 v247, s[16:19], s35 offen offset:2048 sc1 lds\n\t"
        "v_exp_f32 v184, v184\n\t"
        "v_exp_f32 v185, v185\n\t"
        "v_cvt_pk_bf16_f32 v194, v180, v181\n\t"
        "v_exp_f32 v186, v186\n\t"
        "v_cvt_pk_bf16_f32 v195, v182, v183\n\t"
        "v_exp_f32 v187, v187\n\t"
        "v_exp_f32 v188, v188\n\t"
        "v_mfma_f32_32x32x16_bf16 v[48:63], v[136:139], v[192:195], v[48:63]\n\t"
        "buffer_load_dwordx4 v247, s[16:19], s35 offen offset:3072 sc1 lds\n\t"
        "v_exp_f32 v189, v189\n\t"
        "v_cvt_pk_bf16_f32 v196, v184, v185\n\t"
        "v_exp_f32 v190, v190\n\t"
        "v_cvt_pk_bf16_f32 v197, v186, v187\n\t"
        "v_exp_f32 v191, v191\n\t"
        "v_cvt_pk_bf16_f32 v198, v188, v189\n\t"
        "v_cvt_pk_bf16_f32 v199, v190, v191\n\t"
        "v_mfma_f32_32x32x16_bf16 v[176:191], v[144:147], v[72:75], 0\n\t"
        "v_exp_f32 v160, v160\n\t"
        "v_exp_f32 v161, v161\n\t"
        "v_exp_f32 v162, v162\n\t"
        "v_exp_f32 v163, v163\n\t"
        "v_mfma_f32_32x32x16_bf16 v[48:63], v[140:143], v[196:199], v[48:63]\n\t"
        "s_add_u32 s47, s47, 12288\n\t"
        "buffer_load_dwordx4 v[128:131], v247, s[16:19], s47 offen offset:0 sc1\n\t"
        "v_exp_f32 v164, v164\n\t"
        "v_exp_f32 v165, v165\n\t"
        "v_cvt_pk_bf16_f32 v192, v160, v161\n\t"
        "v_exp_f32 v166, v166\n\t"
        "v_exp_f32 v167, v167\n\t"
        "v_cvt_pk_bf16_f32 v193, v162, v163\n\t"
        "v_mfma_f32_32x32x16_bf16 v[176:191], v[148:151], v[76:79], v[176:191]\n\t"
        "v_exp_f32 v168, v168\n\t"
        "v_exp_f32 v169, v169\n\t"
        "v_cvt_pk_bf16_f32 v194, v164, v165\n\t"
        "v_exp_f32 v170, v170\n\t"
        "v_cvt_pk_bf16_f32 v195, v166, v167\n\t"
        "v_exp_f32 v171, v171\n\t"
        "v_exp_f32 v172, v172\n\t"
        "v_mfma_f32_32x32x16_bf16 v[0:15], v[152:155], v[192:195], v[0:15]\n\t"
        "buffer_load_dwordx4 v[132:135], v247, s[16:19], s47 offen offset:1024 sc1\n\t"
        "v_exp_f32 v173, v173\n\t"
        "v_cvt_pk_bf16_f32 v196, v168, v169\n\t"
        "v_exp_f32 v174, v174\n\t"
        "v_cvt_pk_bf16_f32 v197, v170, v171\n\t"
        "v_exp_f32 v175, v175\n\t"
        "v_cvt_pk_bf16_f32 v198, v172, v173\n\t"
        "v_cvt_pk_bf16_f32 v199, v174, v175\n\t"
        "v_mfma_f32_32x32x16_bf16 v[160:175], v[144:147], v[80:83], 0\n\t"
        "v_exp_f32 v176, v176\n\t"
        "v_exp_f32 v177, v177\n\t"
        "v_exp_f32 v178, v178\n\t"
        "v_exp_f32 v179, v179\n\t"
        "v_mfma_f32_32x32x16_bf16 v[0:15], v[156:159], v[196:199], v[0:15]\n\t"
        "buffer_load_dwordx4 v[136:139], v246, s[16:19], s47 offen offset:2048 sc1\n\t"
        "v_exp_f32 v180, v180\n\t"
        "v_exp_f32 v181, v181\n\t"
        "v_cvt_pk_bf16_f32 v192, v176, v177\n\t"
        "v_exp_f32 v182, v182\n\t"
        "v_exp_f32 v183, v183\n\t"
        "v_cvt_pk_bf16_f32 v193, v178, v179\n\t"
        "v_mfma_f32_32x32x16_bf16 v[160:175], v[148:151], v[84:87], v[160:175]\n\t"
        "v_exp_f32 v184, v184\n\t"
        "v_exp_f32 v185, v185\n\t"
        "v_cvt_pk_bf16_f32 v194, v180, v181\n\t"
        "v_exp_f32 v186, v186\n\t"
        "v_cvt_pk_bf16_f32 v195, v182, v183\n\t"
        "v_exp_f32 v187, v187\n\t"
        "v_exp_f32 v188, v188\n\t"
        "v_mfma_f32_32x32x16_bf16 v[16:31], v[152:155], v[192:195], v[16:31]\n\t"
        "buffer_load_dwordx4 v[140:143], v246, s[16:19], s47 offen offset:3072 sc1\n\t"
        "v_exp_f32 v189, v189\n\t"
        "v_cvt_pk_bf16_f32 v196, v184, v185\n\t"
        "v_exp_f32 v190, v190\n\t"
        "v_cvt_pk_bf16_f32 v197, v186, v187\n\t"
        "v_exp_f32 v191, v191\n\t"
        "v_cvt_pk_bf16_f32 v198, v188, v189\n\t"
        "v_cvt_pk_bf16_f32 v199, v190, v191\n\t"
        "v_mfma_f32_32x32x16_bf16 v[176:191], v[144:147], v[88:91], 0\n\t"
        "v_exp_f32 v160, v160\n\t"
        "v_exp_f32 v161, v161\n\t"
        "v_exp_f32 v162, v162\n\t"
        "v_exp_f32 v163, v163\n\t"
        "v_mfma_f32_32x32x16_bf16 v[16:31], v[156:159], v[196:199], v[16:31]\n\t"
        "s_add_u32 s35, s35, 4096\n\t"
        "s_add_u32 m0, m0, 4096\n\t"
        "s_nop 0\n\t"
        "buffer_load_dwordx4 v247, s[16:19], s35 offen offset:0 sc1 lds\n\t"
        "s_cmp_lg_u32 s27, 0\n\t"
        "s_cbranch_scc1 l0_45_%=\n\t"
        "buffer_load_dwordx4 a[140:143], v246, s[16:19], s99 offen offset:3072 sc1\n\t"
        "s_add_u32 s98, s98, 4096\n\t"
        "buffer_load_dwordx4 a[32:35], v246, s[16:19], s98 offen offset:0 sc1\n\t"
        "s_add_u32 s99, s99, 4096\n\t"
        "buffer_load_dwordx4 a[144:147], v246, s[16:19], s99 offen offset:0 sc1\n\t"
        "buffer_load_dwordx4 a[36:39], v246, s[16:19], s98 offen offset:1024 sc1\n\t"
        "buffer_load_dwordx4 a[148:151], v246, s[16:19], s99 offen offset:1024 sc1\n\t"
        "l0_45_%=:\n\t"
        "v_exp_f32 v164, v164\n\t"
        "v_exp_f32 v165, v165\n\t"
        "v_cvt_pk_bf16_f32 v192, v160, v161\n\t"
        "v_exp_f32 v166, v166\n\t"
        "v_exp_f32 v167, v167\n\t"
        "v_cvt_pk_bf16_f32 v193, v162, v163\n\t"
        "v_mfma_f32_32x32x16_bf16 v[176:191], v[148:151], v[92:95], v[176:191]\n\t"
        "buffer_load_dwordx4 v247, s[16:19], s35 offen offset:1024 sc1 lds\n\t"
        "v_exp_f32 v168, v168\n\t"
        "v_exp_f32 v169, v169\n\t"
        "v_cvt_pk_bf16_f32 v194, v164, v165\n\t"
        "v_exp_f32 v170, v170\n\t"
        "v_cvt_pk_bf16_f32 v195, v166, v167\n\t"
        "v_exp_f32 v171, v171\n\t"
        "v_exp_f32 v172, v172\n\t"
        "v_mfma_f32_32x32x16_bf16 v[32:47], v[152:155], v[192:195], v[32:47]\n\t"
        "buffer_load_dwordx4 v247, s[16:19], s35 offen offset:2048 sc1 lds\n\t"
        "s_waitcnt vmcnt(23)\n\t"
        "v_exp_f32 v173, v173\n\t"
        "v_cvt_pk_bf16_f32 v196, v168, v169\n\t"
        "v_exp_f32 v174, v174\n\t"
        "v_cvt_pk_bf16_f32 v197, v170, v171\n\t"
        "v_exp_f32 v175, v175\n\t"
        "v_cvt_pk_bf16_f32 v198, v172, v173\n\t"
        "v_cvt_pk_bf16_f32 v199, v174, v175\n\t"
        "v_mfma_f32_32x32x16_bf16 v[160:175], v[96:99], v[64:67], 0\n\t"
        "v_exp_f32 v176, v176\n\t"
        "v_exp_f32 v177, v177\n\t"
        "v_exp_f32 v178, v178\n\t"
        "v_exp_f32 v179, v179\n\t"
        "v_mfma_f32_32x32x16_bf16 v[32:47], v[156:159], v[196:199], v[32:47]\n\t"
        "buffer_load_dwordx4 v247, s[16:19], s35 offen offset:3072 sc1 lds\n\t"
        "v_exp_f32 v180, v180\n\t"
        "v_exp_f32 v181, v181\n\t"
        "v_cvt_pk_bf16_f32 v192, v176, v177\n\t"
        "v_exp_f32 v182, v182\n\t"
        "v_exp_f32 v183, v183\n\t"
        "v_cvt_pk_bf16_f32 v193, v178, v179\n\t"
        "v_mfma_f32_32x32x16_bf16 v[160:175], v[100:103], v[68:71], v[160:175]\n\t"
        "s_add_u32 s35, s35, 4096\n\t"
        "s_add_u32 m0, m0, 4096\n\t"
        "s_nop 0\n\t"
        "buffer_load_dwordx4 v247, s[16:19], s35 offen offset:0 sc1 lds\n\t"
        "v_exp_f32 v184, v184\n\t"
        "v_exp_f32 v185, v185\n\t"
        "v_cvt_pk_bf16_f32 v194, v180, v181\n\t"
        "v_exp_f32 v186, v186\n\t"
        "v_cvt_pk_bf16_f32 v195, v182, v183\n\t"
        "v_exp_f32 v187, v187\n\t"
        "v_exp_f32 v188, v188\n\t"
        "v_mfma_f32_32x32x16_bf16 v[48:63], v[152:155], v[192:195], v[48:63]\n\t"
        "buffer_load_dwordx4 v247, s[16:19], s35 offen offset:1024 sc1 lds\n\t"
        "v_exp_f32 v189, v189\n\t"
        "v_cvt_pk_bf16_f32 v196, v184, v185\n\t"
        "v_exp_f32 v190, v190\n\t"
        "v_cvt_pk_bf16_f32 v197, v186, v187\n\t"
        "v_exp_f32 v191, v191\n\t"
        "v_cvt_pk_bf16_f32 v198, v188, v189\n\t"
        "v_cvt_pk_bf16_f32 v199, v190, v191\n\t"
        "v_mfma_f32_32x32x16_bf16 v[176:191], v[96:99], v[72:75], 0\n\t"
        "v_exp_f32 v160, v160\n\t"
        "v_exp_f32 v161, v161\n\t"
        "v_exp_f32 v162, v162\n\t"
        "v_exp_f32 v163, v163\n\t"
        "v_mfma_f32_32x32x16_bf16 v[48:63], v[156:159], v[196:199], v[48:63]\n\t"
        "s_add_u32 s47, s47, 4096\n\t"
        "buffer_load_dwordx4 v[144:147], v247, s[16:19], s47 offen offset:0 sc1\n\t"
        "v_exp_f32 v164, v164\n\t"
        "v_exp_f32 v165, v165\n\t"
        "v_cvt_pk_bf16_f32 v192, v160, v161\n\t"
        "v_exp_f32 v166, v166\n\t"
        "v_exp_f32 v167, v167\n\t"
        "v_cvt_pk_bf16_f32 v193, v162, v163\n\t"
        "v_mfma_f32_32x32x16_bf16 v[176:191], v[100:103], v[76:79], v[176:191]\n\t"
        "v_exp_f32 v168, v168\n\t"
        "v_exp_f32 v169, v169\n\t"
        "v_cvt_pk_bf16_f32 v194, v164, v165\n\t"
        "v_exp_f32 v170, v170\n\t"
        "v_cvt_pk_bf16_f32 v195, v166, v167\n\t"
        "v_exp_f32 v171, v171\n\t"
        "v_exp_f32 v172, v172\n\t"
        "v_mfma_f32_32x32x16_bf16 v[0:15], v[104:107], v[192:195], v[0:15]\n\t"
        "buffer_load_dwordx4 v[148:151], v247, s[16:19], s47 offen offset:1024 sc1\n\t"
        "v_exp_f32 v173, v173\n\t"
        "v_cvt_pk_bf16_f32 v196, v168, v169\n\t"
        "v_exp_f32 v174, v174\n\t"
        "v_cvt_pk_bf16_f32 v197, v170, v171\n\t"
        "v_exp_f32 v175, v175\n\t"
        "v_cvt_pk_bf16_f32 v198, v172, v173\n\t"
        "v_cvt_pk_bf16_f32 v199, v174, v175\n\t"
        "v_mfma_f32_32x32x16_bf16 v[160:175], v[96:99], v[80:83], 0\n\t"
        "v_exp_f32 v176, v176\n\t"
        "v_exp_f32 v177, v177\n\t"
        "v_exp_f32 v178, v178\n\t"
        "v_exp_f32 v179, v179\n\t"
        "v_mfma_f32_32x32x16_bf16 v[0:15], v[108:111], v[196:199], v[0:15]\n\t"
        "buffer_load_dwordx4 v[152:155], v246, s[16:19], s47 offen offset:2048 sc1\n\t"
        "v_exp_f32 v180, v180\n\t"
        "v_exp_f32 v181, v181\n\t"
        "v_cvt_pk_bf16_f32 v192, v176, v177\n\t"
        "v_exp_f32 v182, v182\n\t"
        "v_exp_f32 v183, v183\n\t"
        "v_cvt_pk_bf16_f32 v193, v178, v179\n\t"
        "v_mfma_f32_32x32x16_bf16 v[160:175], v[100:103], v[84:87], v[160:175]\n\t"
        "v_exp_f32 v184, v184\n\t"
        "v_exp_f32 v185, v185\n\t"
        "v_cvt_pk_bf16_f32 v194, v180, v181\n\t"
        "v_exp_f32 v186, v186\n\t"
        "v_cvt_pk_bf16_f32 v195, v182, v183\n\t"
        "v_exp_f32 v187, v187\n\t"
        "v_exp_f32 v188, v188\n\t"
        "v_mfma_f32_32x32x16_bf16 v[16:31], v[104:107], v[192:195], v[16:31]\n\t"
        "buffer_load_dwordx4 v[156:159], v246, s[16:19], s47 offen offset:3072 sc1\n\t"
        "v_exp_f32 v189, v189\n\t"
        "v_cvt_pk_bf16_f32 v196, v184, v185\n\t"
        "v_exp_f32 v190, v190\n\t"
        "v_cvt_pk_bf16_f32 v197, v186, v187\n\t"
        "v_exp_f32 v191, v191\n\t"
        "v_cvt_pk_bf16_f32 v198, v188, v189\n\t"
        "v_cvt_pk_bf16_f32 v199, v190, v191\n\t"
        "v_mfma_f32_32x32x16_bf16 v[176:191], v[96:99], v[88:91], 0\n\t"
        "v_exp_f32 v160, v160\n\t"
        "v_exp_f32 v161, v161\n\t"
        "v_exp_f32 v162, v162\n\t"
        "v_exp_f32 v163, v163\n\t"
        "v_mfma_f32_32x32x16_bf16 v[16:31], v[108:111], v[196:199], v[16:31]\n\t"
        "buffer_load_dwordx4 v247, s[16:19], s35 offen offset:2048 sc1 lds\n\t"
        "s_cmp_lg_u32 s27, 0\n\t"
        "s_cbranch_scc1 l0_46_%=\n\t"
        "buffer_load_dwordx4 a[40:43], v246, s[16:19], s98 offen offset:2048 sc1\n\t"
        "buffer_load_dwordx4 a[152:155], v246, s[16:19], s99 offen offset:2048 sc1\n\t"
        "buffer_load_dwordx4 a[44:47], v246, s[16:19], s98 offen offset:3072 sc1\n\t"
        "buffer_load_dwordx4 a[156:159], v246, s[16:19], s99 offen offset:3072 sc1\n\t"
        "s_add_u32 s98, s98, 4096\n\t"
        "buffer_load_dwordx4 a[48:51], v246, s[16:19], s98 offen offset:0 sc1\n\t"
        "l0_46_%=:\n\t"
        "v_exp_f32 v164, v164\n\t"
        "v_exp_f32 v165, v165\n\t"
        "v_cvt_pk_bf16_f32 v192, v160, v161\n\t"
        "v_exp_f32 v166, v166\n\t"
        "v_exp_f32 v167, v167\n\t"
        "v_cvt_pk_bf16_f32 v193, v162, v163\n\t"
        "v_mfma_f32_32x32x16_bf16 v[176:191], v[100:103], v[92:95], v[176:191]\n\t"
        "buffer_load_dwordx4 v247, s[16:19], s35 offen offset:3072 sc1 lds\n\t"
        "v_exp_f32 v168, v168\n\t"
        "v_exp_f32 v169, v169\n\t"
        "v_cvt_pk_bf16_f32 v194, v164, v165\n\t"
        "v_exp_f32 v170, v170\n\t"
        "v_cvt_pk_bf16_f32 v195, v166, v167\n\t"
        "v_exp_f32 v171, v171\n\t"
        "v_exp_f32 v172, v172\n\t"
        "v_mfma_f32_32x32x16_bf16 v[32:47], v[104:107], v[192:195], v[32:47]\n\t"
        "s_add_u32 s35, s35, 4096\n\t"
        "s_add_u32 m0, m0, 4096\n\t"
        "s_nop 0\n\t"
        "buffer_load_dwordx4 v247, s[16:19], s35 offen offset:0 sc1 lds\n\t"
        "s_waitcnt vmcnt(23)\n\t"
        "v_exp_f32 v173, v173\n\t"
        "v_cvt_pk_bf16_f32 v196, v168, v169\n\t"
        "v_exp_f32 v174, v174\n\t"
        "v_cvt_pk_bf16_f32 v197, v170, v171\n\t"
        "v_exp_f32 v175, v175\n\t"
        "v_cvt_pk_bf16_f32 v198, v172, v173\n\t"
        "v_cvt_pk_bf16_f32 v199, v174, v175\n\t"
        "v_mfma_f32_32x32x16_bf16 v[160:175], v[112:115], v[64:67], 0\n\t"
        "v_exp_f32 v176, v176\n\t"
        "v_exp_f32 v177, v177\n\t"
        "v_exp_f32 v178, v178\n\t"
        "v_exp_f32 v179, v179\n\t"
        "v_mfma_f32_32x32x16_bf16 v[32:47], v[108:111], v[196:199], v[32:47]\n\t"
        "buffer_load_dwordx4 v247, s[16:19], s35 offen offset:1024 sc1 lds\n\t"
        "v_exp_f32 v180, v180\n\t"
        "v_exp_f32 v181, v181\n\t"
        "v_cvt_pk_bf16_f32 v192, v176, v177\n\t"
        "v_exp_f32 v182, v182\n\t"
        "v_exp_f32 v183, v183\n\t"
        "v_cvt_pk_bf16_f32 v193, v178, v179\n\t"
        "v_mfma_f32_32x32x16_bf16 v[160:175], v[116:119], v[68:71], v[160:175]\n\t"
        "buffer_load_dwordx4 v247, s[16:19], s35 offen offset:2048 sc1 lds\n\t"
        "v_exp_f32 v184, v184\n\t"
        "v_exp_f32 v185, v185\n\t"
        "v_cvt_pk_bf16_f32 v194, v180, v181\n\t"
        "v_exp_f32 v186, v186\n\t"
        "v_cvt_pk_bf16_f32 v195, v182, v183\n\t"
        "v_exp_f32 v187, v187\n\t"
        "v_exp_f32 v188, v188\n\t"
        "v_mfma_f32_32x32x16_bf16 v[48:63], v[104:107], v[192:195], v[48:63]\n\t"
        "buffer_load_dwordx4 v247, s[16:19], s35 offen offset:3072 sc1 lds\n\t"
        "v_exp_f32 v189, v189\n\t"
        "v_cvt_pk_bf16_f32 v196, v184, v185\n\t"
        "v_exp_f32 v190, v190\n\t"
        "v_cvt_pk_bf16_f32 v197, v186, v187\n\t"
        "v_exp_f32 v191, v191\n\t"
        "v_cvt_pk_bf16_f32 v198, v188, v189\n\t"
        "v_cvt_pk_bf16_f32 v199, v190, v191\n\t"
        "v_mfma_f32_32x32x16_bf16 v[176:191], v[112:115], v[72:75], 0\n\t"
        "v_exp_f32 v160, v160\n\t"
        "v_exp_f32 v161, v161\n\t"
        "v_exp_f32 v162, v162\n\t"
        "v_exp_f32 v163, v163\n\t"
        "v_mfma_f32_32x32x16_bf16 v[48:63], v[108:111], v[196:199], v[48:63]\n\t"
        "s_mov_b32 s62, s96\n\t"
        "s_mov_b32 s63, s97\n\t"
        "s_sub_u32 s47, s47, 61440\n\t"
        "buffer_load_dwordx4 v[96:99], v247, s[16:19], s47 offen offset:0 sc1\n\t"
        "v_exp_f32 v164, v164\n\t"
        "v_exp_f32 v165, v165\n\t"
        "v_cvt_pk_bf16_f32 v192, v160, v161\n\t"
        "v_exp_f32 v166, v166\n\t"
        "v_exp_f32 v167, v167\n\t"
        "v_cvt_pk_bf16_f32 v193, v162, v163\n\t"
        "v_mfma_f32_32x32x16_bf16 v[176:191], v[116:119], v[76:79], v[176:191]\n\t"
        "v_exp_f32 v168, v168\n\t"
        "v_exp_f32 v169, v169\n\t"
        "v_cvt_pk_bf16_f32 v194, v164, v165\n\t"
        "v_exp_f32 v170, v170\n\t"
        "v_cvt_pk_bf16_f32 v195, v166, v167\n\t"
        "v_exp_f32 v171, v171\n\t"
        "v_exp_f32 v172, v172\n\t"
        "v_mfma_f32_32x32x16_bf16 v[0:15], v[120:123], v[192:195], v[0:15]\n\t"
        "buffer_load_dwordx4 v[100:103], v247, s[16:19], s47 offen offset:1024 sc1\n\t"
        "v_exp_f32 v173, v173\n\t"
        "v_cvt_pk_bf16_f32 v196, v168, v169\n\t"
        "v_exp_f32 v174, v174\n\t"
        "v_cvt_pk_bf16_f32 v197, v170, v171\n\t"
        "v_exp_f32 v175, v175\n\t"
        "v_cvt_pk_bf16_f32 v198, v172, v173\n\t"
        "v_cvt_pk_bf16_f32 v199, v174, v175\n\t"
        "v_mfma_f32_32x32x16_bf16 v[160:175], v[112:115], v[80:83], 0\n\t"
        "v_exp_f32 v176, v176\n\t"
        "v_exp_f32 v177, v177\n\t"
        "v_exp_f32 v178, v178\n\t"
        "v_exp_f32 v179, v179\n\t"
        "v_mfma_f32_32x32x16_bf16 v[0:15], v[124:127], v[196:199], v[0:15]\n\t"
        "buffer_load_dwordx4 v[104:107], v246, s[16:19], s47 offen offset:2048 sc1\n\t"
        "v_exp_f32 v180, v180\n\t"
        "v_exp_f32 v181, v181\n\t"
        "v_cvt_pk_bf16_f32 v192, v176, v177\n\t"
        "v_exp_f32 v182, v182\n\t"
        "v_exp_f32 v183, v183\n\t"
        "v_cvt_pk_bf16_f32 v193, v178, v179\n\t"
        "v_mfma_f32_32x32x16_bf16 v[160:175], v[116:119], v[84:87], v[160:175]\n\t"
        "v_exp_f32 v184, v184\n\t"
        "v_exp_f32 v185, v185\n\t"
        "v_cvt_pk_bf16_f32 v194, v180, v181\n\t"
        "v_exp_f32 v186, v186\n\t"
        "v_cvt_pk_bf16_f32 v195, v182, v183\n\t"
        "v_exp_f32 v187, v187\n\t"
        "v_exp_f32 v188, v188\n\t"
        "v_mfma_f32_32x32x16_bf16 v[16:31], v[120:123], v[192:195], v[16:31]\n\t"
        "buffer_load_dwordx4 v[108:111], v246, s[16:19], s47 offen offset:3072 sc1\n\t"
        "v_exp_f32 v189, v189\n\t"
        "v_cvt_pk_bf16_f32 v196, v184, v185\n\t"
        "v_exp_f32 v190, v190\n\t"
        "v_cvt_pk_bf16_f32 v197, v186, v187\n\t"
        "v_exp_f32 v191, v191\n\t"
        "v_cvt_pk_bf16_f32 v198, v188, v189\n\t"
        "v_cvt_pk_bf16_f32 v199, v190, v191\n\t"
        "v_mfma_f32_32x32x16_bf16 v[176:191], v[112:115], v[88:91], 0\n\t"
        "v_exp_f32 v160, v160\n\t"
        "v_exp_f32 v161, v161\n\t"
        "v_exp_f32 v162, v162\n\t"
        "v_exp_f32 v163, v163\n\t"
        "v_mfma_f32_32x32x16_bf16 v[16:31], v[124:127], v[196:199], v[16:31]\n\t"
        "buffer_load_dwordx4 v[200:203], v246, s[16:19], s63 offen offset:0 sc1\n\t"
        "s_cmp_lg_u32 s27, 0\n\t"
        "s_cbranch_scc1 l0_47_%=\n\t"
        "s_add_u32 s99, s99, 4096\n\t"
        "buffer_load_dwordx4 a[160:163], v246, s[16:19], s99 offen offset:0 sc1\n\t"
        "buffer_load_dwordx4 a[52:55], v246, s[16:19], s98 offen offset:1024 sc1\n\t"
        "buffer_load_dwordx4 a[164:167], v246, s[16:19], s99 offen offset:1024 sc1\n\t"
        "buffer_load_dwordx4 a[56:59], v246, s[16:19], s98 offen offset:2048 sc1\n\t"
        "buffer_load_dwordx4 a[168:171], v246, s[16:19], s99 offen offset:2048 sc1\n\t"
        "l0_47_%=:\n\t"
        "v_exp_f32 v164, v164\n\t"
        "v_exp_f32 v165, v165\n\t"
        "v_cvt_pk_bf16_f32 v192, v160, v161\n\t"
        "v_exp_f32 v166, v166\n\t"
        "v_exp_f32 v167, v167\n\t"
        "v_cvt_pk_bf16_f32 v193, v162, v163\n\t"
        "v_mfma_f32_32x32x16_bf16 v[176:191], v[116:119], v[92:95], v[176:191]\n\t"
        "buffer_load_dwordx4 v[204:207], v246, s[16:19], s63 offen offset:1024 sc1\n\t"
        "v_exp_f32 v168, v168\n\t"
        "v_exp_f32 v169, v169\n\t"
        "v_cvt_pk_bf16_f32 v194, v164, v165\n\t"
        "v_exp_f32 v170, v170\n\t"
        "v_cvt_pk_bf16_f32 v195, v166, v167\n\t"
        "v_exp_f32 v171, v171\n\t"
        "v_exp_f32 v172, v172\n\t"
        "v_mfma_f32_32x32x16_bf16 v[32:47], v[120:123], v[192:195], v[32:47]\n\t"
        "buffer_load_dwordx4 v[208:211], v246, s[16:19], s63 offen offset:2048 sc1\n\t"
        "s_waitcnt vmcnt(23)\n\t"
        "v_exp_f32 v173, v173\n\t"
        "v_cvt_pk_bf16_f32 v196, v168, v169\n\t"
        "v_exp_f32 v174, v174\n\t"
        "v_cvt_pk_bf16_f32 v197, v170, v171\n\t"
        "v_exp_f32 v175, v175\n\t"
        "v_cvt_pk_bf16_f32 v198, v172, v173\n\t"
        "v_cvt_pk_bf16_f32 v199, v174, v175\n\t"
        "v_mfma_f32_32x32x16_bf16 v[160:175], v[128:131], v[64:67], 0\n\t"
        "v_exp_f32 v176, v176\n\t"
        "v_exp_f32 v177, v177\n\t"
        "v_exp_f32 v178, v178\n\t"
        "v_exp_f32 v179, v179\n\t"
        "v_mfma_f32_32x32x16_bf16 v[32:47], v[124:127], v[196:199], v[32:47]\n\t"
        "buffer_load_dwordx4 v[212:215], v246, s[16:19], s63 offen offset:3072 sc1\n\t"
        "v_exp_f32 v180, v180\n\t"
        "v_exp_f32 v181, v181\n\t"
        "v_cvt_pk_bf16_f32 v192, v176, v177\n\t"
        "v_exp_f32 v182, v182\n\t"
        "v_exp_f32 v183, v183\n\t"
        "v_cvt_pk_bf16_f32 v193, v178, v179\n\t"
        "v_mfma_f32_32x32x16_bf16 v[160:175], v[132:135], v[68:71], v[160:175]\n\t"
        "s_add_u32 s35, s35, 4096\n\t"
        "s_add_u32 m0, m0, 4096\n\t"
        "s_nop 0\n\t"
        "buffer_load_dwordx4 v247, s[16:19], s35 offen offset:0 sc1 lds\n\t"
        "v_exp_f32 v184, v184\n\t"
        "v_exp_f32 v185, v185\n\t"
        "v_cvt_pk_bf16_f32 v194, v180, v181\n\t"
        "v_exp_f32 v186, v186\n\t"
        "v_cvt_pk_bf16_f32 v195, v182, v183\n\t"
        "v_exp_f32 v187, v187\n\t"
        "v_exp_f32 v188, v188\n\t"
        "v_mfma_f32_32x32x16_bf16 v[48:63], v[120:123], v[192:195], v[48:63]\n\t"
        "buffer_load_dwordx4 v247, s[16:19], s35 offen offset:1024 sc1 lds\n\t"
        "v_exp_f32 v189, v189\n\t"
        "v_cvt_pk_bf16_f32 v196, v184, v185\n\t"
        "v_exp_f32 v190, v190\n\t"
        "v_cvt_pk_bf16_f32 v197, v186, v187\n\t"
        "v_exp_f32 v191, v191\n\t"
        "v_cvt_pk_bf16_f32 v198, v188, v189\n\t"
        "v_cvt_pk_bf16_f32 v199, v190, v191\n\t"
        "v_mfma_f32_32x32x16_bf16 v[176:191], v[128:131], v[72:75], 0\n\t"
        "v_exp_f32 v160, v160\n\t"
        "v_exp_f32 v161, v161\n\t"
        "v_exp_f32 v162, v162\n\t"
        "v_exp_f32 v163, v163\n\t"
        "v_mfma_f32_32x32x16_bf16 v[48:63], v[124:127], v[196:199], v[48:63]\n\t"
        "s_add_u32 s47, s47, 4096\n\t"
        "buffer_load_dwordx4 v[112:115], v247, s[16:19], s47 offen offset:0 sc1\n\t"
        "v_exp_f32 v164, v164\n\t"
        "v_exp_f32 v165, v165\n\t"
        "v_cvt_pk_bf16_f32 v192, v160, v161\n\t"
        "v_exp_f32 v166, v166\n\t"
        "v_exp_f32 v167, v167\n\t"
        "v_cvt_pk_bf16_f32 v193, v162, v163\n\t"
        "v_mfma_f32_32x32x16_bf16 v[176:191], v[132:135], v[76:79], v[176:191]\n\t"
        "v_exp_f32 v168, v168\n\t"
        "v_exp_f32 v169, v169\n\t"
        "v_cvt_pk_bf16_f32 v194, v164, v165\n\t"
        "v_exp_f32 v170, v170\n\t"
        "v_cvt_pk_bf16_f32 v195, v166, v167\n\t"
        "v_exp_f32 v171, v171\n\t"
        "v_exp_f32 v172, v172\n\t"
        "v_mfma_f32_32x32x16_bf16 v[0:15], v[136:139], v[192:195], v[0:15]\n\t"
        "buffer_load_dwordx4 v[116:119], v247, s[16:19], s47 offen offset:1024 sc1\n\t"
        "v_exp_f32 v173, v173\n\t"
        "v_cvt_pk_bf16_f32 v196, v168, v169\n\t"
        "v_exp_f32 v174, v174\n\t"
        "v_cvt_pk_bf16_f32 v197, v170, v171\n\t"
        "v_exp_f32 v175, v175\n\t"
        "v_cvt_pk_bf16_f32 v198, v172, v173\n\t"
        "v_cvt_pk_bf16_f32 v199, v174, v175\n\t"
        "v_mfma_f32_32x32x16_bf16 v[160:175], v[128:131], v[80:83], 0\n\t"
        "v_exp_f32 v176, v176\n\t"
        "v_exp_f32 v177, v177\n\t"
        "v_exp_f32 v178, v178\n\t"
        "v_exp_f32 v179, v179\n\t"
        "v_mfma_f32_32x32x16_bf16 v[0:15], v[140:143], v[196:199], v[0:15]\n\t"
        "buffer_load_dwordx4 v[120:123], v246, s[16:19], s47 offen offset:2048 sc1\n\t"
        "v_exp_f32 v180, v180\n\t"
        "v_exp_f32 v181, v181\n\t"
        "v_cvt_pk_bf16_f32 v192, v176, v177\n\t"
        "v_exp_f32 v182, v182\n\t"
        "v_exp_f32 v183, v183\n\t"
        "v_cvt_pk_bf16_f32 v193, v178, v179\n\t"
        "v_mfma_f32_32x32x16_bf16 v[160:175], v[132:135], v[84:87], v[160:175]\n\t"
        "v_exp_f32 v184, v184\n\t"
        "v_exp_f32 v185, v185\n\t"
        "v_cvt_pk_bf16_f32 v194, v180, v181\n\t"
        "v_exp_f32 v186, v186\n\t"
        "v_cvt_pk_bf16_f32 v195, v182, v183\n\t"
        "v_exp_f32 v187, v187\n\t"
        "v_exp_f32 v188, v188\n\t"
        "v_mfma_f32_32x32x16_bf16 v[16:31], v[136:139], v[192:195], v[16:31]\n\t"
        "buffer_load_dwordx4 v[124:127], v246, s[16:19], s47 offen offset:3072 sc1\n\t"
        "v_exp_f32 v189, v189\n\t"
        "v_cvt_pk_bf16_f32 v196, v184, v185\n\t"
        "v_exp_f32 v190, v190\n\t"
        "v_cvt_pk_bf16_f32 v197, v186, v187\n\t"
        "v_exp_f32 v191, v191\n\t"
        "v_cvt_pk_bf16_f32 v198, v188, v189\n\t"
        "v_cvt_pk_bf16_f32 v199, v190, v191\n\t"
        "v_mfma_f32_32x32x16_bf16 v[176:191], v[128:131], v[88:91], 0\n\t"
        "v_exp_f32 v160, v160\n\t"
        "v_exp_f32 v161, v161\n\t"
        "v_exp_f32 v162, v162\n\t"
        "v_exp_f32 v163, v163\n\t"
        "v_mfma_f32_32x32x16_bf16 v[16:31], v[140:143], v[196:199], v[16:31]\n\t"
        "buffer_load_dwordx4 v247, s[16:19], s35 offen offset:2048 sc1 lds\n\t"
        "s_cmp_lg_u32 s27, 0\n\t"
        "s_cbranch_scc1 l0_48_%=\n\t"
        "buffer_load_dwordx4 a[60:63], v246, s[16:19], s98 offen offset:3072 sc1\n\t"
        "buffer_load_dwordx4 a[172:175], v246, s[16:19], s99 offen offset:3072 sc1\n\t"
        "s_add_u32 s98, s98, 4096\n\t"
        "buffer_load_dwordx4 a[64:67], v246, s[16:19], s98 offen offset:0 sc1\n\t"
        "s_add_u32 s99, s99, 4096\n\t"
        "buffer_load_dwordx4 a[176:179], v246, s[16:19], s99 offen offset:0 sc1\n\t"
        "buffer_load_dwordx4 a[68:71], v246, s[16:19], s98 offen offset:1024 sc1\n\t"
        "l0_48_%=:\n\t"
        "v_exp_f32 v164, v164\n\t"
        "v_exp_f32 v165, v165\n\t"
        "v_cvt_pk_bf16_f32 v192, v160, v161\n\t"
        "v_exp_f32 v166, v166\n\t"
        "v_exp_f32 v167, v167\n\t"
        "v_cvt_pk_bf16_f32 v193, v162, v163\n\t"
        "v_mfma_f32_32x32x16_bf16 v[176:191], v[132:135], v[92:95], v[176:191]\n\t"
        "buffer_load_dwordx4 v247, s[16:19], s35 offen offset:3072 sc1 lds\n\t"
        "v_exp_f32 v168, v168\n\t"
        "v_exp_f32 v169, v169\n\t"
        "v_cvt_pk_bf16_f32 v194, v164, v165\n\t"
        "v_exp_f32 v170, v170\n\t"
        "v_cvt_pk_bf16_f32 v195, v166, v167\n\t"
        "v_exp_f32 v171, v171\n\t"
        "v_exp_f32 v172, v172\n\t"
        "v_mfma_f32_32x32x16_bf16 v[32:47], v[136:139], v[192:195], v[32:47]\n\t"
        "s_lshl_b32 s35, s25, 10\n\t"
        "s_add_u32 s35, s35, 5423104\n\t"
        "s_mov_b32 m0, 155648\n\t"
        "s_nop 0\n\t"
        "buffer_load_dwordx4 v247, s[16:19], s35 offen sc1 lds\n\t"
        "s_waitcnt vmcnt(23)\n\t"
        "v_exp_f32 v173, v173\n\t"
        "v_cvt_pk_bf16_f32 v196, v168, v169\n\t"
        "v_exp_f32 v174, v174\n\t"
        "v_cvt_pk_bf16_f32 v197, v170, v171\n\t"
        "v_exp_f32 v175, v175\n\t"
        "v_cvt_pk_bf16_f32 v198, v172, v173\n\t"
        "v_cvt_pk_bf16_f32 v199, v174, v175\n\t"
        "v_mfma_f32_32x32x16_bf16 v[160:175], v[144:147], v[64:67], 0\n\t"
        "v_exp_f32 v176, v176\n\t"
        "v_exp_f32 v177, v177\n\t"
        "v_exp_f32 v178, v178\n\t"
        "v_exp_f32 v179, v179\n\t"
        "v_mfma_f32_32x32x16_bf16 v[32:47], v[140:143], v[196:199], v[32:47]\n\t"
        "v_exp_f32 v180, v180\n\t"
        "v_exp_f32 v181, v181\n\t"
        "v_cvt_pk_bf16_f32 v192, v176, v177\n\t"
        "v_exp_f32 v182, v182\n\t"
        "v_exp_f32 v183, v183\n\t"
        "v_cvt_pk_bf16_f32 v193, v178, v179\n\t"
        "v_mfma_f32_32x32x16_bf16 v[160:175], v[148:151], v[68:71], v[160:175]\n\t"
        "v_exp_f32 v184, v184\n\t"
        "v_exp_f32 v185, v185\n\t"
        "v_cvt_pk_bf16_f32 v194, v180, v181\n\t"
        "v_exp_f32 v186, v186\n\t"
        "v_cvt_pk_bf16_f32 v195, v182, v183\n\t"
        "v_exp_f32 v187, v187\n\t"
        "v_exp_f32 v188, v188\n\t"
        "v_mfma_f32_32x32x16_bf16 v[48:63], v[136:139], v[192:195], v[48:63]\n\t"
        "v_exp_f32 v189, v189\n\t"
        "v_cvt_pk_bf16_f32 v196, v184, v185\n\t"
        "v_exp_f32 v190, v190\n\t"
        "v_cvt_pk_bf16_f32 v197, v186, v187\n\t"
        "v_exp_f32 v191, v191\n\t"
        "v_cvt_pk_bf16_f32 v198, v188, v189\n\t"
        "v_cvt_pk_bf16_f32 v199, v190, v191\n\t"
        "v_mfma_f32_32x32x16_bf16 v[176:191], v[144:147], v[72:75], 0\n\t"
        "v_exp_f32 v160, v160\n\t"
        "v_exp_f32 v161, v161\n\t"
        "v_exp_f32 v162, v162\n\t"
        "v_exp_f32 v163, v163\n\t"
        "v_mfma_f32_32x32x16_bf16 v[48:63], v[140:143], v[196:199], v[48:63]\n\t"
        "s_add_u32 s47, s47, 12288\n\t"
        "buffer_load_dwordx4 v[128:131], v247, s[16:19], s47 offen offset:0 sc1\n\t"
        "v_exp_f32 v164, v164\n\t"
        "v_exp_f32 v165, v165\n\t"
        "v_cvt_pk_bf16_f32 v192, v160, v161\n\t"
        "v_exp_f32 v166, v166\n\t"
        "v_exp_f32 v167, v167\n\t"
        "v_cvt_pk_bf16_f32 v193, v162, v163\n\t"
        "v_mfma_f32_32x32x16_bf16 v[176:191], v[148:151], v[76:79], v[176:191]\n\t"
        "v_exp_f32 v168, v168\n\t"
        "v_exp_f32 v169, v169\n\t"
        "v_cvt_pk_bf16_f32 v194, v164, v165\n\t"
        "v_exp_f32 v170, v170\n\t"
        "v_cvt_pk_bf16_f32 v195, v166, v167\n\t"
        "v_exp_f32 v171, v171\n\t"
        "v_exp_f32 v172, v172\n\t"
        "v_mfma_f32_32x32x16_bf16 v[0:15], v[152:155], v[192:195], v[0:15]\n\t"
        "buffer_load_dwordx4 v[132:135], v247, s[16:19], s47 offen offset:1024 sc1\n\t"
        "v_exp_f32 v173, v173\n\t"
        "v_cvt_pk_bf16_f32 v196, v168, v169\n\t"
        "v_exp_f32 v174, v174\n\t"
        "v_cvt_pk_bf16_f32 v197, v170, v171\n\t"
        "v_exp_f32 v175, v175\n\t"
        "v_cvt_pk_bf16_f32 v198, v172, v173\n\t"
        "v_cvt_pk_bf16_f32 v199, v174, v175\n\t"
        "v_mfma_f32_32x32x16_bf16 v[160:175], v[144:147], v[80:83], 0\n\t"
        "v_exp_f32 v176, v176\n\t"
        "v_exp_f32 v177, v177\n\t"
        "v_exp_f32 v178, v178\n\t"
        "v_exp_f32 v179, v179\n\t"
        "v_mfma_f32_32x32x16_bf16 v[0:15], v[156:159], v[196:199], v[0:15]\n\t"
        "buffer_load_dwordx4 v[136:139], v246, s[16:19], s47 offen offset:2048 sc1\n\t"
        "v_exp_f32 v180, v180\n\t"
        "v_exp_f32 v181, v181\n\t"
        "v_cvt_pk_bf16_f32 v192, v176, v177\n\t"
        "v_exp_f32 v182, v182\n\t"
        "v_exp_f32 v183, v183\n\t"
        "v_cvt_pk_bf16_f32 v193, v178, v179\n\t"
        "v_mfma_f32_32x32x16_bf16 v[160:175], v[148:151], v[84:87], v[160:175]\n\t"
        "v_exp_f32 v184, v184\n\t"
        "v_exp_f32 v185, v185\n\t"
        "v_cvt_pk_bf16_f32 v194, v180, v181\n\t"
        "v_exp_f32 v186, v186\n\t"
        "v_cvt_pk_bf16_f32 v195, v182, v183\n\t"
        "v_exp_f32 v187, v187\n\t"
        "v_exp_f32 v188, v188\n\t"
        "v_mfma_f32_32x32x16_bf16 v[16:31], v[152:155], v[192:195], v[16:31]\n\t"
        "buffer_load_dwordx4 v[140:143], v246, s[16:19], s47 offen offset:3072 sc1\n\t"
        "v_exp_f32 v189, v189\n\t"
        "v_cvt_pk_bf16_f32 v196, v184, v185\n\t"
        "v_exp_f32 v190, v190\n\t"
        "v_cvt_pk_bf16_f32 v197, v186, v187\n\t"
        "v_exp_f32 v191, v191\n\t"
        "v_cvt_pk_bf16_f32 v198, v188, v189\n\t"
        "v_cvt_pk_bf16_f32 v199, v190, v191\n\t"
        "v_mfma_f32_32x32x16_bf16 v[176:191], v[144:147], v[88:91], 0\n\t"
        "v_exp_f32 v160, v160\n\t"
        "v_exp_f32 v161, v161\n\t"
        "v_exp_f32 v162, v162\n\t"
        "v_exp_f32 v163, v163\n\t"
        "v_mfma_f32_32x32x16_bf16 v[16:31], v[156:159], v[196:199], v[16:31]\n\t"
        "s_add_u32 s63, s63, 4096\n\t"
        "buffer_load_dwordx4 v[216:219], v246, s[16:19], s63 offen offset:0 sc1\n\t"
        "s_cmp_lg_u32 s27, 0\n\t"
        "s_cbranch_scc1 l0_49_%=\n\t"
        "buffer_load_dwordx4 a[180:183], v246, s[16:19], s99 offen offset:1024 sc1\n\t"
        "buffer_load_dwordx4 a[72:75], v246, s[16:19], s98 offen offset:2048 sc1\n\t"
        "buffer_load_dwordx4 a[184:187], v246, s[16:19], s99 offen offset:2048 sc1\n\t"
        "buffer_load_dwordx4 a[76:79], v246, s[16:19], s98 offen offset:3072 sc1\n\t"
        "buffer_load_dwordx4 a[188:191], v246, s[16:19], s99 offen offset:3072 sc1\n\t"
        "l0_49_%=:\n\t"
        "v_exp_f32 v164, v164\n\t"
        "v_exp_f32 v165, v165\n\t"
        "v_cvt_pk_bf16_f32 v192, v160, v161\n\t"
        "v_exp_f32 v166, v166\n\t"
        "v_exp_f32 v167, v167\n\t"
        "v_cvt_pk_bf16_f32 v193, v162, v163\n\t"
        "v_mfma_f32_32x32x16_bf16 v[176:191], v[148:151], v[92:95], v[176:191]\n\t"
        "buffer_load_dwordx4 v[220:223], v246, s[16:19], s63 offen offset:1024 sc1\n\t"
        "v_exp_f32 v168, v168\n\t"
        "v_exp_f32 v169, v169\n\t"
        "v_cvt_pk_bf16_f32 v194, v164, v165\n\t"
        "v_exp_f32 v170, v170\n\t"
        "v_cvt_pk_bf16_f32 v195, v166, v167\n\t"
        "v_exp_f32 v171, v171\n\t"
        "v_exp_f32 v172, v172\n\t"
        "v_mfma_f32_32x32x16_bf16 v[32:47], v[152:155], v[192:195], v[32:47]\n\t"
        "buffer_load_dwordx4 v[224:227], v246, s[16:19], s63 offen offset:2048 sc1\n\t"
        "s_waitcnt vmcnt(20)\n\t"
        "v_exp_f32 v173, v173\n\t"
        "v_cvt_pk_bf16_f32 v196, v168, v169\n\t"
        "v_exp_f32 v174, v174\n\t"
        "v_cvt_pk_bf16_f32 v197, v170, v171\n\t"
        "v_exp_f32 v175, v175\n\t"
        "v_cvt_pk_bf16_f32 v198, v172, v173\n\t"
        "v_cvt_pk_bf16_f32 v199, v174, v175\n\t"
        "v_exp_f32 v176, v176\n\t"
        "v_exp_f32 v177, v177\n\t"
        "v_exp_f32 v178, v178\n\t"
        "v_exp_f32 v179, v179\n\t"
        "v_mfma_f32_32x32x16_bf16 v[32:47], v[156:159], v[196:199], v[32:47]\n\t"
        "v_exp_f32 v180, v180\n\t"
        "v_exp_f32 v181, v181\n\t"
        "v_cvt_pk_bf16_f32 v192, v176, v177\n\t"
        "v_exp_f32 v182, v182\n\t"
        "v_exp_f32 v183, v183\n\t"
        "v_cvt_pk_bf16_f32 v193, v178, v179\n\t"
        "v_exp_f32 v184, v184\n\t"
        "v_exp_f32 v185, v185\n\t"
        "v_cvt_pk_bf16_f32 v194, v180, v181\n\t"
        "v_exp_f32 v186, v186\n\t"
        "v_cvt_pk_bf16_f32 v195, v182, v183\n\t"
        "v_exp_f32 v187, v187\n\t"
        "v_exp_f32 v188, v188\n\t"
        "v_mfma_f32_32x32x16_bf16 v[48:63], v[152:155], v[192:195], v[48:63]\n\t"
        "v_exp_f32 v189, v189\n\t"
        "v_cvt_pk_bf16_f32 v196, v184, v185\n\t"
        "v_exp_f32 v190, v190\n\t"
        "v_cvt_pk_bf16_f32 v197, v186, v187\n\t"
        "v_exp_f32 v191, v191\n\t"
        "v_cvt_pk_bf16_f32 v198, v188, v189\n\t"
        "v_cvt_pk_bf16_f32 v199, v190, v191\n\t"
        "s_nop 1\n\t"
        "v_mfma_f32_32x32x16_bf16 v[48:63], v[156:159], v[196:199], v[48:63]\n\t"
        "s_lshr_b32 s38, s26, 1\n\t"
        "s_lshl_b32 s38, s38, 10\n\t"
        "s_lshl_b32 s39, s26, 8\n\t"
        "s_add_u32 s39, s39, 139264\n\t"
        "s_add_u32 s40, s39, s38\n\t"
        "v_add_u32 v188, s40, v239\n\t"
        "s_xor_b32 s40, s38, 1024\n\t"
        "s_add_u32 s40, s40, s39\n\t"
        "v_add_u32 v189, s40, v239\n\t"
        "s_add_u32 s38, s38, 139392\n\t"
        "v_and_b32 v190, 0x7c, v239\n\t"
        "v_cndmask_b32 v184, v12, v4, s[52:53]\n\t"
        "v_add_u32 v190, s38, v190\n\t"
        "ds_write_b128 v243, v[8:11]\n\t"
        "ds_write_b128 v243, v[12:15] offset:1024\n\t"
        "ds_write_b32 v188, v184\n\t"
        "v_cndmask_b32 v185, v28, v20, s[52:53]\n\t"
        "ds_write_b128 v242, v[16:19] offset:0\n\t"
        "ds_write_b128 v242, v[20:23] offset:1024\n\t"
        "ds_write_b128 v241, v[24:27] offset:0\n\t"
        "ds_write_b128 v241, v[28:31] offset:1024\n\t"
        "ds_write_b32 v189, v185\n\t"
        "s_add_u32 s34, s56, s28\n\t"
        "s_add_u32 s34, s34, 8585216\n\t"
        "s_waitcnt lgkmcnt(0)\n\t"
        "s_barrier\n\t"
        "ds_read2_b32 v[184:185], v190 offset0:0 offset1:64\n\t"
        "ds_read2_b32 v[186:187], v190 offset0:128 offset1:192\n\t"
        "ds_read_b128 v[160:163], v240 offset:0\n\t"
        "ds_read_b128 v[168:171], v240 offset:2048\n\t"
        "ds_read_b128 v[176:179], v240 offset:4096\n\t"
        "ds_read_b128 v[164:167], v240 offset:1024\n\t"
        "ds_read_b128 v[172:175], v240 offset:3072\n\t"
        "ds_read_b128 v[180:183], v240 offset:5120\n\t"
        "s_waitcnt lgkmcnt(5)\n\t"
        "v_pk_add_f32 v[0:1], v[0:1], v[160:161]\n\t"
        "s_waitcnt lgkmcnt(3)\n\t"
        "v_pk_add_f32 v[168:169], v[168:169], v[176:177]\n\t"
        "v_pk_add_f32 v[2:3], v[2:3], v[162:163]\n\t"
        "v_pk_add_f32 v[170:171], v[170:171], v[178:179]\n\t"
        "s_waitcnt lgkmcnt(2)\n\t"
        "v_pk_add_f32 v[4:5], v[4:5], v[164:165]\n\t"
        "s_waitcnt lgkmcnt(0)\n\t"
        "v_pk_add_f32 v[172:173], v[172:173], v[180:181]\n\t"
        "v_pk_add_f32 v[6:7], v[6:7], v[166:167]\n\t"
        "v_pk_add_f32 v[174:175], v[174:175], v[182:183]\n\t"
        "v_pk_add_f32 v[0:1], v[0:1], v[168:169]\n\t"
        "v_pk_add_f32 v[2:3], v[2:3], v[170:171]\n\t"
        "v_pk_add_f32 v[4:5], v[4:5], v[172:173]\n\t"
        "v_pk_add_f32 v[6:7], v[6:7], v[174:175]\n\t"
        "v_pk_add_f32 v[184:185], v[184:185], v[186:187]\n\t"
        "s_mov_b32 s43, 0\n\t"
        "v_add_f32 v184, v184, v185\n\t"
        "norm_50_%=:\n\t"
        "v_max3_f32 v186, |v0|, |v1|, |v2|\n\t"
        "v_rcp_f32 v188, v184\n\t"
        "v_max3_f32 v186, v186, |v3|, |v4|\n\t"
        "v_cmp_le_f32 vcc, 0x1f800000, v184\n\t"
        "v_max3_f32 v186, v186, |v5|, |v6|\n\t"
        "v_mov_b32 v187, 0x5f800000\n\t"
        "v_max_f32 v186, v186, |v7|\n\t"
        "v_cmp_ge_f32 s[48:49], v187, v184\n\t"
        "v_mov_b32 v187, 0x7f61b1e6\n\t"
        "v_cmp_ge_f32 s[50:51], v187, v186\n\t"
        "s_and_b64 vcc, vcc, s[48:49]\n\t"
        "s_and_b64 vcc, vcc, s[50:51]\n\t"
        "s_not_b64 vcc, vcc\n\t"
        "s_cmp_eq_u32 s43, 0\n\t"
        "s_cselect_b64 vcc, vcc, 0\n\t"
        "s_cbranch_vccnz fbk_51_%=\n\t"
        "v_pk_mul_f32 v[0:1], v[0:1], v[188:189] op_sel_hi:[1,0]\n\t"
        "v_pk_mul_f32 v[2:3], v[2:3], v[188:189] op_sel_hi:[1,0]\n\t"
        "v_pk_mul_f32 v[4:5], v[4:5], v[188:189] op_sel_hi:[1,0]\n\t"
        "v_pk_mul_f32 v[6:7], v[6:7], v[188:189] op_sel_hi:[1,0]\n\t"
        "v_cvt_pk_bf16_f32 v196, v0, v1\n\t"
        "v_cvt_pk_bf16_f32 v197, v2, v3\n\t"
        "v_cvt_pk_bf16_f32 v198, v4, v5\n\t"
        "v_cvt_pk_bf16_f32 v199, v6, v7\n\t"
        "buffer_store_dwordx4 v[196:199], v247, s[16:19], s34 offen sc1\n\t"
        "v_mfma_f32_32x32x16_bf16 v[160:175], v[96:99], v[80:83], 0\n\t"
        "v_mfma_f32_32x32x16_bf16 v[160:175], v[100:103], v[84:87], v[160:175]\n\t"
        "v_mfma_f32_32x32x16_bf16 v[176:191], v[96:99], v[88:91], 0\n\t"
        "s_nop 10\n\t"
        "v_exp_f32 v160, v160\n\t"
        "v_exp_f32 v161, v161\n\t"
        "v_exp_f32 v162, v162\n\t"
        "v_exp_f32 v163, v163\n\t"
        "s_add_u32 s47, s47, 4096\n\t"
        "buffer_load_dwordx4 v[144:147], v247, s[16:19], s47 offen offset:0 sc1\n\t"
        "v_exp_f32 v164, v164\n\t"
        "v_exp_f32 v165, v165\n\t"
        "v_cvt_pk_bf16_f32 v192, v160, v161\n\t"
        "v_exp_f32 v166, v166\n\t"
        "v_exp_f32 v167, v167\n\t"
        "v_cvt_pk_bf16_f32 v193, v162, v163\n\t"
        "v_mfma_f32_32x32x16_bf16 v[176:191], v[100:103], v[92:95], v[176:191]\n\t"
        "v_exp_f32 v168, v168\n\t"
        "v_exp_f32 v169, v169\n\t"
        "v_cvt_pk_bf16_f32 v194, v164, v165\n\t"
        "v_exp_f32 v170, v170\n\t"
        "v_cvt_pk_bf16_f32 v195, v166, v167\n\t"
        "v_exp_f32 v171, v171\n\t"
        "v_exp_f32 v172, v172\n\t"
        "v_mfma_f32_32x32x16_bf16 v[32:47], v[104:107], v[192:195], v[32:47]\n\t"
        "buffer_load_dwordx4 v[148:151], v247, s[16:19], s47 offen offset:1024 sc1\n\t"
        "s_waitcnt vmcnt(13)\n\t"
        "v_exp_f32 v173, v173\n\t"
        "v_cvt_pk_bf16_f32 v196, v168, v169\n\t"
        "v_exp_f32 v174, v174\n\t"
        "v_cvt_pk_bf16_f32 v197, v170, v171\n\t"
        "v_exp_f32 v175, v175\n\t"
        "v_cvt_pk_bf16_f32 v198, v172, v173\n\t"
        "v_cvt_pk_bf16_f32 v199, v174, v175\n\t"
        "v_mfma_f32_32x32x16_bf16 v[160:175], v[112:115], v[80:83], 0\n\t"
        "v_exp_f32 v176, v176\n\t"
        "v_exp_f32 v177, v177\n\t"
        "v_exp_f32 v178, v178\n\t"
        "v_exp_f32 v179, v179\n\t"
        "v_mfma_f32_32x32x16_bf16 v[32:47], v[108:111], v[196:199], v[32:47]\n\t"
        "buffer_load_dwordx4 v[152:155], v246, s[16:19], s47 offen offset:2048 sc1\n\t"
        "v_exp_f32 v180, v180\n\t"
        "v_exp_f32 v181, v181\n\t"
        "v_cvt_pk_bf16_f32 v192, v176, v177\n\t"
        "v_exp_f32 v182, v182\n\t"
        "v_exp_f32 v183, v183\n\t"
        "v_cvt_pk_bf16_f32 v193, v178, v179\n\t"
        "v_mfma_f32_32x32x16_bf16 v[160:175], v[116:119], v[84:87], v[160:175]\n\t"
        "v_exp_f32 v184, v184\n\t"
        "v_exp_f32 v185, v185\n\t"
        "v_cvt_pk_bf16_f32 v194, v180, v181\n\t"
        "v_exp_f32 v186, v186\n\t"
        "v_cvt_pk_bf16_f32 v195, v182, v183\n\t"
        "v_exp_f32 v187, v187\n\t"
        "v_exp_f32 v188, v188\n\t"
        "v_mfma_f32_32x32x16_bf16 v[48:63], v[104:107], v[192:195], v[48:63]\n\t"
        "buffer_load_dwordx4 v[156:159], v246, s[16:19], s47 offen offset:3072 sc1\n\t"
        "v_exp_f32 v189, v189\n\t"
        "v_cvt_pk_bf16_f32 v196, v184, v185\n\t"
        "v_exp_f32 v190, v190\n\t"
        "v_cvt_pk_bf16_f32 v197, v186, v187\n\t"
        "v_exp_f32 v191, v191\n\t"
        "v_cvt_pk_bf16_f32 v198, v188, v189\n\t"
        "v_cvt_pk_bf16_f32 v199, v190, v191\n\t"
        "v_mfma_f32_32x32x16_bf16 v[176:191], v[112:115], v[88:91], 0\n\t"
        "v_exp_f32 v160, v160\n\t"
        "v_exp_f32 v161, v161\n\t"
        "v_exp_f32 v162, v162\n\t"
        "v_exp_f32 v163, v163\n\t"
        "v_mfma_f32_32x32x16_bf16 v[48:63], v[108:111], v[196:199], v[48:63]\n\t"
        "s_add_u32 s47, s47, 12288\n\t"
        "buffer_load_dwordx4 v[96:99], v247, s[16:19], s47 offen offset:0 sc1\n\t"
        "v_exp_f32 v164, v164\n\t"
        "v_exp_f32 v165, v165\n\t"
        "v_cvt_pk_bf16_f32 v192, v160, v161\n\t"
        "v_exp_f32 v166, v166\n\t"
        "v_exp_f32 v167, v167\n\t"
        "v_cvt_pk_bf16_f32 v193, v162, v163\n\t"
        "v_mfma_f32_32x32x16_bf16 v[176:191], v[116:119], v[92:95], v[176:191]\n\t"
        "v_exp_f32 v168, v168\n\t"
        "v_exp_f32 v169, v169\n\t"
        "v_cvt_pk_bf16_f32 v194, v164, v165\n\t"
        "v_exp_f32 v170, v170\n\t"
        "v_cvt_pk_bf16_f32 v195, v166, v167\n\t"
        "v_exp_f32 v171, v171\n\t"
        "v_exp_f32 v172, v172\n\t"
        "v_mfma_f32_32x32x16_bf16 v[32:47], v[120:123], v[192:195], v[32:47]\n\t"
        "buffer_load_dwordx4 v[100:103], v247, s[16:19], s47 offen offset:1024 sc1\n\t"
        "s_waitcnt vmcnt(10)\n\t"
        "v_exp_f32 v173, v173\n\t"
        "v_cvt_pk_bf16_f32 v196, v168, v169\n\t"
        "v_exp_f32 v174, v174\n\t"
        "v_cvt_pk_bf16_f32 v197, v170, v171\n\t"
        "v_exp_f32 v175, v175\n\t"
        "v_cvt_pk_bf16_f32 v198, v172, v173\n\t"
        "v_cvt_pk_bf16_f32 v199, v174, v175\n\t"
        "v_mfma_f32_32x32x16_bf16 v[160:175], v[128:131], v[80:83], 0\n\t"
        "v_exp_f32 v176, v176\n\t"
        "v_exp_f32 v177, v177\n\t"
        "v_exp_f32 v178, v178\n\t"
        "v_exp_f32 v179, v179\n\t"
        "v_mfma_f32_32x32x16_bf16 v[32:47], v[124:127], v[196:199], v[32:47]\n\t"
        "buffer_load_dwordx4 v[104:107], v246, s[16:19], s47 offen offset:2048 sc1\n\t"
        "s_mul_i32 s38, s27, 147456\n\t"
        "s_lshl_b32 s39, s25, 11\n\t"
        "s_lshl_b32 s40, s24, 3\n\t"
        "v_lshlrev_b32 v236, 4, v238\n\t"
        "s_add_u32 s38, s38, s39\n\t"
        "s_add_u32 s38, s38, s40\n\t"
        "s_add_u32 s38, s38, 73728\n\t"
        "s_waitcnt vmcnt(7)\n\t"
        "s_barrier\n\t"
        "s_cmp_lg_u32 s26, 0\n\t"
        "s_cbranch_scc1 sig_52_%=\n\t"
        "s_mov_b64 exec, 0xffff\n\t"
        "buffer_atomic_swap_x2 v[234:235], v236, s[16:19], s38 offen\n\t"
        "s_mov_b64 exec, -1\n\t"
        "sig_52_%=:\n\t"
        "v_exp_f32 v180, v180\n\t"
        "v_exp_f32 v181, v181\n\t"
        "v_cvt_pk_bf16_f32 v192, v176, v177\n\t"
        "v_exp_f32 v182, v182\n\t"
        "v_exp_f32 v183, v183\n\t"
        "v_cvt_pk_bf16_f32 v193, v178, v179\n\t"
        "v_mfma_f32_32x32x16_bf16 v[160:175], v[132:135], v[84:87], v[160:175]\n\t"
        "v_exp_f32 v184, v184\n\t"
        "v_exp_f32 v185, v185\n\t"
        "v_cvt_pk_bf16_f32 v194, v180, v181\n\t"
        "v_exp_f32 v186, v186\n\t"
        "v_cvt_pk_bf16_f32 v195, v182, v183\n\t"
        "v_exp_f32 v187, v187\n\t"
        "v_exp_f32 v188, v188\n\t"
        "v_mfma_f32_32x32x16_bf16 v[48:63], v[120:123], v[192:195], v[48:63]\n\t"
        "buffer_load_dwordx4 v[108:111], v246, s[16:19], s47 offen offset:3072 sc1\n\t"
        "v_exp_f32 v189, v189\n\t"
        "v_cvt_pk_bf16_f32 v196, v184, v185\n\t"
        "v_exp_f32 v190, v190\n\t"
        "v_cvt_pk_bf16_f32 v197, v186, v187\n\t"
        "v_exp_f32 v191, v191\n\t"
        "v_cvt_pk_bf16_f32 v198, v188, v189\n\t"
        "v_cvt_pk_bf16_f32 v199, v190, v191\n\t"
        "v_mfma_f32_32x32x16_bf16 v[176:191], v[128:131], v[88:91], 0\n\t"
        "v_exp_f32 v160, v160\n\t"
        "v_exp_f32 v161, v161\n\t"
        "v_exp_f32 v162, v162\n\t"
        "v_exp_f32 v163, v163\n\t"
        "v_mfma_f32_32x32x16_bf16 v[48:63], v[124:127], v[196:199], v[48:63]\n\t"
        "s_add_u32 s47, s47, 4096\n\t"
        "buffer_load_dwordx4 v[112:115], v247, s[16:19], s47 offen offset:0 sc1\n\t"
        "v_exp_f32 v164, v164\n\t"
        "v_exp_f32 v165, v165\n\t"
        "v_cvt_pk_bf16_f32 v192, v160, v161\n\t"
        "v_exp_f32 v166, v166\n\t"
        "v_exp_f32 v167, v167\n\t"
        "v_cvt_pk_bf16_f32 v193, v162, v163\n\t"
        "v_mfma_f32_32x32x16_bf16 v[176:191], v[132:135], v[92:95], v[176:191]\n\t"
        "v_exp_f32 v168, v168\n\t"
        "v_exp_f32 v169, v169\n\t"
        "v_cvt_pk_bf16_f32 v194, v164, v165\n\t"
        "v_exp_f32 v170, v170\n\t"
        "v_cvt_pk_bf16_f32 v195, v166, v167\n\t"
        "v_exp_f32 v171, v171\n\t"
        "v_exp_f32 v172, v172\n\t"
        "v_mfma_f32_32x32x16_bf16 v[32:47], v[136:139], v[192:195], v[32:47]\n\t"
        "buffer_load_dwordx4 v[116:119], v247, s[16:19], s47 offen offset:1024 sc1\n\t"
        "s_waitcnt vmcnt(6)\n\t"
        "v_exp_f32 v173, v173\n\t"
        "v_cvt_pk_bf16_f32 v196, v168, v169\n\t"
        "v_exp_f32 v174, v174\n\t"
        "v_cvt_pk_bf16_f32 v197, v170, v171\n\t"
        "v_exp_f32 v175, v175\n\t"
        "v_cvt_pk_bf16_f32 v198, v172, v173\n\t"
        "v_cvt_pk_bf16_f32 v199, v174, v175\n\t"
        "v_mfma_f32_32x32x16_bf16 v[160:175], v[144:147], v[80:83], 0\n\t"
        "v_exp_f32 v176, v176\n\t"
        "v_exp_f32 v177, v177\n\t"
        "v_exp_f32 v178, v178\n\t"
        "v_exp_f32 v179, v179\n\t"
        "v_mfma_f32_32x32x16_bf16 v[32:47], v[140:143], v[196:199], v[32:47]\n\t"
        "buffer_load_dwordx4 v[120:123], v246, s[16:19], s47 offen offset:2048 sc1\n\t"
        "v_exp_f32 v180, v180\n\t"
        "v_exp_f32 v181, v181\n\t"
        "v_cvt_pk_bf16_f32 v192, v176, v177\n\t"
        "v_exp_f32 v182, v182\n\t"
        "v_exp_f32 v183, v183\n\t"
        "v_cvt_pk_bf16_f32 v193, v178, v179\n\t"
        "v_mfma_f32_32x32x16_bf16 v[160:175], v[148:151], v[84:87], v[160:175]\n\t"
        "v_exp_f32 v184, v184\n\t"
        "v_exp_f32 v185, v185\n\t"
        "v_cvt_pk_bf16_f32 v194, v180, v181\n\t"
        "v_exp_f32 v186, v186\n\t"
        "v_cvt_pk_bf16_f32 v195, v182, v183\n\t"
        "v_exp_f32 v187, v187\n\t"
        "v_exp_f32 v188, v188\n\t"
        "v_mfma_f32_32x32x16_bf16 v[48:63], v[136:139], v[192:195], v[48:63]\n\t"
        "buffer_load_dwordx4 v[124:127], v246, s[16:19], s47 offen offset:3072 sc1\n\t"
        "v_exp_f32 v189, v189\n\t"
        "v_cvt_pk_bf16_f32 v196, v184, v185\n\t"
        "v_exp_f32 v190, v190\n\t"
        "v_cvt_pk_bf16_f32 v197, v186, v187\n\t"
        "v_exp_f32 v191, v191\n\t"
        "v_cvt_pk_bf16_f32 v198, v188, v189\n\t"
        "v_cvt_pk_bf16_f32 v199, v190, v191\n\t"
        "v_mfma_f32_32x32x16_bf16 v[176:191], v[144:147], v[88:91], 0\n\t"
        "v_exp_f32 v160, v160\n\t"
        "v_exp_f32 v161, v161\n\t"
        "v_exp_f32 v162, v162\n\t"
        "v_exp_f32 v163, v163\n\t"
        "v_mfma_f32_32x32x16_bf16 v[48:63], v[140:143], v[196:199], v[48:63]\n\t"
        "s_add_u32 s47, s47, 12288\n\t"
        "buffer_load_dwordx4 v[128:131], v247, s[16:19], s47 offen offset:0 sc1\n\t"
        "v_exp_f32 v164, v164\n\t"
        "v_exp_f32 v165, v165\n\t"
        "v_cvt_pk_bf16_f32 v192, v160, v161\n\t"
        "v_exp_f32 v166, v166\n\t"
        "v_exp_f32 v167, v167\n\t"
        "v_cvt_pk_bf16_f32 v193, v162, v163\n\t"
        "v_mfma_f32_32x32x16_bf16 v[176:191], v[148:151], v[92:95], v[176:191]\n\t"
        "v_exp_f32 v168, v168\n\t"
        "v_exp_f32 v169, v169\n\t"
        "v_cvt_pk_bf16_f32 v194, v164, v165\n\t"
        "v_exp_f32 v170, v170\n\t"
        "v_cvt_pk_bf16_f32 v195, v166, v167\n\t"
        "v_exp_f32 v171, v171\n\t"
        "v_exp_f32 v172, v172\n\t"
        "v_mfma_f32_32x32x16_bf16 v[32:47], v[152:155], v[192:195], v[32:47]\n\t"
        "buffer_load_dwordx4 v[132:135], v247, s[16:19], s47 offen offset:1024 sc1\n\t"
        "s_waitcnt vmcnt(6)\n\t"
        "v_exp_f32 v173, v173\n\t"
        "v_cvt_pk_bf16_f32 v196, v168, v169\n\t"
        "v_exp_f32 v174, v174\n\t"
        "v_cvt_pk_bf16_f32 v197, v170, v171\n\t"
        "v_exp_f32 v175, v175\n\t"
        "v_cvt_pk_bf16_f32 v198, v172, v173\n\t"
        "v_cvt_pk_bf16_f32 v199, v174, v175\n\t"
        "v_mfma_f32_32x32x16_bf16 v[160:175], v[96:99], v[80:83], 0\n\t"
        "v_exp_f32 v176, v176\n\t"
        "v_exp_f32 v177, v177\n\t"
        "v_exp_f32 v178, v178\n\t"
        "v_exp_f32 v179, v179\n\t"
        "v_mfma_f32_32x32x16_bf16 v[32:47], v[156:159], v[196:199], v[32:47]\n\t"
        "buffer_load_dwordx4 v[136:139], v246, s[16:19], s47 offen offset:2048 sc1\n\t"
        "v_exp_f32 v180, v180\n\t"
        "v_exp_f32 v181, v181\n\t"
        "v_cvt_pk_bf16_f32 v192, v176, v177\n\t"
        "v_exp_f32 v182, v182\n\t"
        "v_exp_f32 v183, v183\n\t"
        "v_cvt_pk_bf16_f32 v193, v178, v179\n\t"
        "v_mfma_f32_32x32x16_bf16 v[160:175], v[100:103], v[84:87], v[160:175]\n\t"
        "v_exp_f32 v184, v184\n\t"
        "v_exp_f32 v185, v185\n\t"
        "v_cvt_pk_bf16_f32 v194, v180, v181\n\t"
        "v_exp_f32 v186, v186\n\t"
        "v_cvt_pk_bf16_f32 v195, v182, v183\n\t"
        "v_exp_f32 v187, v187\n\t"
        "v_exp_f32 v188, v188\n\t"
        "v_mfma_f32_32x32x16_bf16 v[48:63], v[152:155], v[192:195], v[48:63]\n\t"
        "buffer_load_dwordx4 v[140:143], v246, s[16:19], s47 offen offset:3072 sc1\n\t"
        "v_exp_f32 v189, v189\n\t"
        "v_cvt_pk_bf16_f32 v196, v184, v185\n\t"
        "v_exp_f32 v190, v190\n\t"
        "v_cvt_pk_bf16_f32 v197, v186, v187\n\t"
        "v_exp_f32 v191, v191\n\t"
        "v_cvt_pk_bf16_f32 v198, v188, v189\n\t"
        "v_cvt_pk_bf16_f32 v199, v190, v191\n\t"
        "v_mfma_f32_32x32x16_bf16 v[176:191], v[96:99], v[88:91], 0\n\t"
        "v_exp_f32 v160, v160\n\t"
        "v_exp_f32 v161, v161\n\t"
        "v_exp_f32 v162, v162\n\t"
        "v_exp_f32 v163, v163\n\t"
        "v_mfma_f32_32x32x16_bf16 v[48:63], v[156:159], v[196:199], v[48:63]\n\t"
        "buffer_load_dwordx4 v[0:3], v246, s[16:19], s62 offen offset:0 sc1\n\t"
        "v_exp_f32 v164, v164\n\t"
        "v_exp_f32 v165, v165\n\t"
        "v_cvt_pk_bf16_f32 v192, v160, v161\n\t"
        "v_exp_f32 v166, v166\n\t"
        "v_exp_f32 v167, v167\n\t"
        "v_cvt_pk_bf16_f32 v193, v162, v163\n\t"
        "v_mfma_f32_32x32x16_bf16 v[176:191], v[100:103], v[92:95], v[176:191]\n\t"
        "buffer_load_dwordx4 v[4:7], v246, s[16:19], s62 offen offset:1024 sc1\n\t"
        "v_exp_f32 v168, v168\n\t"
        "v_exp_f32 v169, v169\n\t"
        "v_cvt_pk_bf16_f32 v194, v164, v165\n\t"
        "v_exp_f32 v170, v170\n\t"
        "v_cvt_pk_bf16_f32 v195, v166, v167\n\t"
        "v_exp_f32 v171, v171\n\t"
        "v_exp_f32 v172, v172\n\t"
        "v_mfma_f32_32x32x16_bf16 v[32:47], v[104:107], v[192:195], v[32:47]\n\t"
        "buffer_load_dwordx4 v[8:11], v246, s[16:19], s62 offen offset:2048 sc1\n\t"
        "s_waitcnt vmcnt(7)\n\t"
        "v_exp_f32 v173, v173\n\t"
        "v_cvt_pk_bf16_f32 v196, v168, v169\n\t"
        "v_exp_f32 v174, v174\n\t"
        "v_cvt_pk_bf16_f32 v197, v170, v171\n\t"
        "v_exp_f32 v175, v175\n\t"
        "v_cvt_pk_bf16_f32 v198, v172, v173\n\t"
        "v_cvt_pk_bf16_f32 v199, v174, v175\n\t"
        "v_mfma_f32_32x32x16_bf16 v[160:175], v[112:115], v[80:83], 0\n\t"
        "v_exp_f32 v176, v176\n\t"
        "v_exp_f32 v177, v177\n\t"
        "v_exp_f32 v178, v178\n\t"
        "v_exp_f32 v179, v179\n\t"
        "v_mfma_f32_32x32x16_bf16 v[32:47], v[108:111], v[196:199], v[32:47]\n\t"
        "buffer_load_dwordx4 v[12:15], v246, s[16:19], s62 offen offset:3072 sc1\n\t"
        "v_exp_f32 v180, v180\n\t"
        "v_exp_f32 v181, v181\n\t"
        "v_cvt_pk_bf16_f32 v192, v176, v177\n\t"
        "v_exp_f32 v182, v182\n\t"
        "v_exp_f32 v183, v183\n\t"
        "v_cvt_pk_bf16_f32 v193, v178, v179\n\t"
        "v_mfma_f32_32x32x16_bf16 v[160:175], v[116:119], v[84:87], v[160:175]\n\t"
        "s_add_u32 s62, s62, 4096\n\t"
        "buffer_load_dwordx4 v[16:19], v246, s[16:19], s62 offen offset:0 sc1\n\t"
        "v_exp_f32 v184, v184\n\t"
        "v_exp_f32 v185, v185\n\t"
        "v_cvt_pk_bf16_f32 v194, v180, v181\n\t"
        "v_exp_f32 v186, v186\n\t"
        "v_cvt_pk_bf16_f32 v195, v182, v183\n\t"
        "v_exp_f32 v187, v187\n\t"
        "v_exp_f32 v188, v188\n\t"
        "v_mfma_f32_32x32x16_bf16 v[48:63], v[104:107], v[192:195], v[48:63]\n\t"
        "buffer_load_dwordx4 v[20:23], v246, s[16:19], s62 offen offset:1024 sc1\n\t"
        "v_exp_f32 v189, v189\n\t"
        "v_cvt_pk_bf16_f32 v196, v184, v185\n\t"
        "v_exp_f32 v190, v190\n\t"
        "v_cvt_pk_bf16_f32 v197, v186, v187\n\t"
        "v_exp_f32 v191, v191\n\t"
        "v_cvt_pk_bf16_f32 v198, v188, v189\n\t"
        "v_cvt_pk_bf16_f32 v199, v190, v191\n\t"
        "v_mfma_f32_32x32x16_bf16 v[176:191], v[112:115], v[88:91], 0\n\t"
        "v_exp_f32 v160, v160\n\t"
        "v_exp_f32 v161, v161\n\t"
        "v_exp_f32 v162, v162\n\t"
        "v_exp_f32 v163, v163\n\t"
        "v_mfma_f32_32x32x16_bf16 v[48:63], v[108:111], v[196:199], v[48:63]\n\t"
        "s_mul_i32 s9, s27, 147456\n\t"
        "s_add_u32 s9, s9, s12\n\t"
        "s_add_u32 s9, s9, 73728\n\t"
        "buffer_load_dwordx2 v[232:233], v238, s[16:19], s9 offen sc1\n\t"
        "buffer_load_dwordx4 v[24:27], v246, s[16:19], s62 offen offset:2048 sc1\n\t"
        "v_exp_f32 v164, v164\n\t"
        "v_exp_f32 v165, v165\n\t"
        "v_cvt_pk_bf16_f32 v192, v160, v161\n\t"
        "v_exp_f32 v166, v166\n\t"
        "v_exp_f32 v167, v167\n\t"
        "v_cvt_pk_bf16_f32 v193, v162, v163\n\t"
        "v_mfma_f32_32x32x16_bf16 v[176:191], v[116:119], v[92:95], v[176:191]\n\t"
        "buffer_load_dwordx4 v[28:31], v246, s[16:19], s62 offen offset:3072 sc1\n\t"
        "v_exp_f32 v168, v168\n\t"
        "v_exp_f32 v169, v169\n\t"
        "v_cvt_pk_bf16_f32 v194, v164, v165\n\t"
        "v_exp_f32 v170, v170\n\t"
        "v_cvt_pk_bf16_f32 v195, v166, v167\n\t"
        "v_exp_f32 v171, v171\n\t"
        "v_exp_f32 v172, v172\n\t"
        "v_mfma_f32_32x32x16_bf16 v[32:47], v[120:123], v[192:195], v[32:47]\n\t"
        "s_waitcnt vmcnt(9)\n\t"
        "v_exp_f32 v173, v173\n\t"
        "v_cvt_pk_bf16_f32 v196, v168, v169\n\t"
        "v_exp_f32 v174, v174\n\t"
        "v_cvt_pk_bf16_f32 v197, v170, v171\n\t"
        "v_exp_f32 v175, v175\n\t"
        "v_cvt_pk_bf16_f32 v198, v172, v173\n\t"
        "v_cvt_pk_bf16_f32 v199, v174, v175\n\t"
        "v_mfma_f32_32x32x16_bf16 v[160:175], v[128:131], v[80:83], 0\n\t"
        "v_exp_f32 v176, v176\n\t"
        "v_exp_f32 v177, v177\n\t"
        "v_exp_f32 v178, v178\n\t"
        "v_exp_f32 v179, v179\n\t"
        "v_mfma_f32_32x32x16_bf16 v[32:47], v[124:127], v[196:199], v[32:47]\n\t"
        "v_exp_f32 v180, v180\n\t"
        "v_exp_f32 v181, v181\n\t"
        "v_cvt_pk_bf16_f32 v192, v176, v177\n\t"
        "v_exp_f32 v182, v182\n\t"
        "v_exp_f32 v183, v183\n\t"
        "v_cvt_pk_bf16_f32 v193, v178, v179\n\t"
        "v_mfma_f32_32x32x16_bf16 v[160:175], v[132:135], v[84:87], v[160:175]\n\t"
        "v_exp_f32 v184, v184\n\t"
        "v_exp_f32 v185, v185\n\t"
        "v_cvt_pk_bf16_f32 v194, v180, v181\n\t"
        "v_exp_f32 v186, v186\n\t"
        "v_cvt_pk_bf16_f32 v195, v182, v183\n\t"
        "v_exp_f32 v187, v187\n\t"
        "v_exp_f32 v188, v188\n\t"
        "v_mfma_f32_32x32x16_bf16 v[48:63], v[120:123], v[192:195], v[48:63]\n\t"
        "v_exp_f32 v189, v189\n\t"
        "v_cvt_pk_bf16_f32 v196, v184, v185\n\t"
        "v_exp_f32 v190, v190\n\t"
        "v_cvt_pk_bf16_f32 v197, v186, v187\n\t"
        "v_exp_f32 v191, v191\n\t"
        "v_cvt_pk_bf16_f32 v198, v188, v189\n\t"
        "v_cvt_pk_bf16_f32 v199, v190, v191\n\t"
        "v_mfma_f32_32x32x16_bf16 v[176:191], v[128:131], v[88:91], 0\n\t"
        "v_exp_f32 v160, v160\n\t"
        "v_exp_f32 v161, v161\n\t"
        "v_exp_f32 v162, v162\n\t"
        "v_exp_f32 v163, v163\n\t"
        "v_mfma_f32_32x32x16_bf16 v[48:63], v[124:127], v[196:199], v[48:63]\n\t"
        "s_waitcnt vmcnt(2)\n\t"
        "v_cmp_ne_u64 vcc, s[10:11], v[232:233]\n\t"
        "s_nop 0\n\t"
        "s_cbranch_vccnz sp_53_%=\n\t"
        "bk_54_%=:\n\t"
        "s_or_b32 s38, s46, s26\n\t"
        "s_or_b32 s38, s38, s27\n\t"
        "s_cbranch_scc1 bz_55_%=\n\t"
        "s_mov_b64 exec, 1\n\t"
        "buffer_store_dwordx2 v[234:235], off, s[16:19], 0 offset:0 sc1\n\t"
        "s_mov_b64 exec, -1\n\t"
        "bz_55_%=:\n\t"
        "s_add_u32 s33, s60, s28\n\t"
        "s_add_u32 s33, s33, 8585216\n\t"
        "buffer_load_dwordx4 v[144:147], v247, s[16:19], s33 offen offset:0 sc1\n\t"
        "buffer_load_dwordx4 v[148:151], v247, s[16:19], s33 offen offset:1024 sc1\n\t"
        "buffer_load_dwordx4 v[152:155], v247, s[16:19], s33 offen offset:2048 sc1\n\t"
        "buffer_load_dwordx4 v[156:159], v247, s[16:19], s33 offen offset:3072 sc1\n\t"
        "s_add_u32 s33, s33, 4096\n\t"
        "buffer_load_dwordx4 v[96:99], v247, s[16:19], s33 offen offset:0 sc1\n\t"
        "buffer_load_dwordx4 v[100:103], v247, s[16:19], s33 offen offset:1024 sc1\n\t"
        "buffer_load_dwordx4 v[104:107], v247, s[16:19], s33 offen offset:2048 sc1\n\t"
        "buffer_load_dwordx4 v[108:111], v247, s[16:19], s33 offen offset:3072 sc1\n\t"
        "v_exp_f32 v164, v164\n\t"
        "v_exp_f32 v165, v165\n\t"
        "v_cvt_pk_bf16_f32 v192, v160, v161\n\t"
        "v_exp_f32 v166, v166\n\t"
        "v_exp_f32 v167, v167\n\t"
        "v_cvt_pk_bf16_f32 v193, v162, v163\n\t"
        "v_mfma_f32_32x32x16_bf16 v[176:191], v[132:135], v[92:95], v[176:191]\n\t"
        "v_exp_f32 v168, v168\n\t"
        "v_exp_f32 v169, v169\n\t"
        "v_cvt_pk_bf16_f32 v194, v164, v165\n\t"
        "v_exp_f32 v170, v170\n\t"
        "v_cvt_pk_bf16_f32 v195, v166, v167\n\t"
        "v_exp_f32 v171, v171\n\t"
        "v_exp_f32 v172, v172\n\t"
        "v_mfma_f32_32x32x16_bf16 v[32:47], v[136:139], v[192:195], v[32:47]\n\t"
        "v_exp_f32 v173, v173\n\t"
        "v_cvt_pk_bf16_f32 v196, v168, v169\n\t"
        "v_exp_f32 v174, v174\n\t"
        "v_cvt_pk_bf16_f32 v197, v170, v171\n\t"
        "v_exp_f32 v175, v175\n\t"
        "v_cvt_pk_bf16_f32 v198, v172, v173\n\t"
        "v_cvt_pk_bf16_f32 v199, v174, v175\n\t"
        "v_exp_f32 v176, v176\n\t"
        "v_exp_f32 v177, v177\n\t"
        "v_exp_f32 v178, v178\n\t"
        "v_exp_f32 v179, v179\n\t"
        "v_mfma_f32_32x32x16_bf16 v[32:47], v[140:143], v[196:199], v[32:47]\n\t"
        "v_exp_f32 v180, v180\n\t"
        "v_exp_f32 v181, v181\n\t"
        "v_cvt_pk_bf16_f32 v192, v176, v177\n\t"
        "v_exp_f32 v182, v182\n\t"
        "v_exp_f32 v183, v183\n\t"
        "v_cvt_pk_bf16_f32 v193, v178, v179\n\t"
        "v_exp_f32 v184, v184\n\t"
        "v_exp_f32 v185, v185\n\t"
        "v_cvt_pk_bf16_f32 v194, v180, v181\n\t"
        "v_exp_f32 v186, v186\n\t"
        "v_cvt_pk_bf16_f32 v195, v182, v183\n\t"
        "v_exp_f32 v187, v187\n\t"
        "v_exp_f32 v188, v188\n\t"
        "v_mfma_f32_32x32x16_bf16 v[48:63], v[136:139], v[192:195], v[48:63]\n\t"
        "v_exp_f32 v189, v189\n\t"
        "v_cvt_pk_bf16_f32 v196, v184, v185\n\t"
        "v_exp_f32 v190, v190\n\t"
        "v_cvt_pk_bf16_f32 v197, v186, v187\n\t"
        "v_exp_f32 v191, v191\n\t"
        "v_cvt_pk_bf16_f32 v198, v188, v189\n\t"
        "v_cvt_pk_bf16_f32 v199, v190, v191\n\t"
        "s_nop 1\n\t"
        "v_mfma_f32_32x32x16_bf16 v[48:63], v[140:143], v[196:199], v[48:63]\n\t"
        "buffer_load_dwordx4 v[88:91], v246, s[16:19], s63 offen offset:3072 sc1\n\t"
        "s_lshr_b32 s38, s26, 1\n\t"
        "s_lshl_b32 s38, s38, 10\n\t"
        "s_lshl_b32 s39, s26, 8\n\t"
        "s_add_u32 s39, s39, 139264\n\t"
        "s_add_u32 s40, s39, s38\n\t"
        "v_add_u32 v188, s40, v239\n\t"
        "s_xor_b32 s40, s38, 1024\n\t"
        "s_add_u32 s40, s40, s39\n\t"
        "v_add_u32 v189, s40, v239\n\t"
        "s_add_u32 s38, s38, 139392\n\t"
        "v_and_b32 v190, 0x7c, v239\n\t"
        "v_cndmask_b32 v184, v44, v36, s[52:53]\n\t"
        "v_add_u32 v190, s38, v190\n\t"
        "ds_write_b128 v243, v[40:43]\n\t"
        "ds_write_b128 v243, v[44:47] offset:1024\n\t"
        "ds_write_b32 v188, v184\n\t"
        "v_cndmask_b32 v185, v60, v52, s[52:53]\n\t"
        "ds_write_b128 v242, v[48:51] offset:0\n\t"
        "ds_write_b128 v242, v[52:55] offset:1024\n\t"
        "ds_write_b128 v241, v[56:59] offset:0\n\t"
        "ds_write_b128 v241, v[60:63] offset:1024\n\t"
        "ds_write_b32 v189, v185\n\t"
        "s_add_u32 s34, s56, s28\n\t"
        "s_add_u32 s34, s34, 8650752\n\t"
        "s_waitcnt lgkmcnt(0)\n\t"
        "s_barrier\n\t"
        "ds_read2_b32 v[184:185], v190 offset0:0 offset1:64\n\t"
        "ds_read2_b32 v[186:187], v190 offset0:128 offset1:192\n\t"
        "ds_read_b128 v[160:163], v240 offset:0\n\t"
        "ds_read_b128 v[168:171], v240 offset:2048\n\t"
        "ds_read_b128 v[176:179], v240 offset:4096\n\t"
        "ds_read_b128 v[164:167], v240 offset:1024\n\t"
        "ds_read_b128 v[172:175], v240 offset:3072\n\t"
        "ds_read_b128 v[180:183], v240 offset:5120\n\t"
        "s_waitcnt lgkmcnt(5)\n\t"
        "v_pk_add_f32 v[32:33], v[32:33], v[160:161]\n\t"
        "s_waitcnt lgkmcnt(3)\n\t"
        "v_pk_add_f32 v[168:169], v[168:169], v[176:177]\n\t"
        "v_pk_add_f32 v[34:35], v[34:35], v[162:163]\n\t"
        "v_pk_add_f32 v[170:171], v[170:171], v[178:179]\n\t"
        "s_waitcnt lgkmcnt(2)\n\t"
        "v_pk_add_f32 v[36:37], v[36:37], v[164:165]\n\t"
        "s_waitcnt lgkmcnt(0)\n\t"
        "v_pk_add_f32 v[172:173], v[172:173], v[180:181]\n\t"
        "v_pk_add_f32 v[38:39], v[38:39], v[166:167]\n\t"
        "v_pk_add_f32 v[174:175], v[174:175], v[182:183]\n\t"
        "v_pk_add_f32 v[32:33], v[32:33], v[168:169]\n\t"
        "v_pk_add_f32 v[34:35], v[34:35], v[170:171]\n\t"
        "v_pk_add_f32 v[36:37], v[36:37], v[172:173]\n\t"
        "v_pk_add_f32 v[38:39], v[38:39], v[174:175]\n\t"
        "v_pk_add_f32 v[184:185], v[184:185], v[186:187]\n\t"
        "s_mov_b32 s43, 0\n\t"
        "v_add_f32 v184, v184, v185\n\t"
        "norm_56_%=:\n\t"
        "v_max3_f32 v186, |v32|, |v33|, |v34|\n\t"
        "v_rcp_f32 v188, v184\n\t"
        "v_max3_f32 v186, v186, |v35|, |v36|\n\t"
        "v_cmp_le_f32 vcc, 0x1f800000, v184\n\t"
        "v_max3_f32 v186, v186, |v37|, |v38|\n\t"
        "v_mov_b32 v187, 0x5f800000\n\t"
        "v_max_f32 v186, v186, |v39|\n\t"
        "v_cmp_ge_f32 s[48:49], v187, v184\n\t"
        "v_mov_b32 v187, 0x7f61b1e6\n\t"
        "v_cmp_ge_f32 s[50:51], v187, v186\n\t"
        "s_and_b64 vcc, vcc, s[48:49]\n\t"
        "s_and_b64 vcc, vcc, s[50:51]\n\t"
        "s_not_b64 vcc, vcc\n\t"
        "s_cmp_eq_u32 s43, 0\n\t"
        "s_cselect_b64 vcc, vcc, 0\n\t"
        "s_cbranch_vccnz fbk_57_%=\n\t"
        "v_pk_mul_f32 v[32:33], v[32:33], v[188:189] op_sel_hi:[1,0]\n\t"
        "v_pk_mul_f32 v[34:35], v[34:35], v[188:189] op_sel_hi:[1,0]\n\t"
        "v_pk_mul_f32 v[36:37], v[36:37], v[188:189] op_sel_hi:[1,0]\n\t"
        "v_pk_mul_f32 v[38:39], v[38:39], v[188:189] op_sel_hi:[1,0]\n\t"
        "v_cvt_pk_bf16_f32 v196, v32, v33\n\t"
        "v_cvt_pk_bf16_f32 v197, v34, v35\n\t"
        "v_cvt_pk_bf16_f32 v198, v36, v37\n\t"
        "v_cvt_pk_bf16_f32 v199, v38, v39\n\t"
        "buffer_store_dwordx4 v[196:199], v247, s[16:19], s34 offen sc1\n\t"
        "s_cmp_ge_u32 s25, 14\n\t"
        "s_cbranch_scc1 idle_2_%=\n\t"
        "s_waitcnt vmcnt(9)\n\t"
        "v_mfma_f32_32x32x16_bf16 v[160:175], v[0:3], v[144:147], 0\n\t"
        "v_lshlrev_b32 v192, 16, v248\n\t"
        "v_mfma_f32_32x32x16_bf16 v[176:191], v[200:203], v[144:147], 0\n\t"
        "v_and_b32 v193, 0xffff0000, v248\n\t"
        "s_waitcnt vmcnt(8)\n\t"
        "v_mfma_f32_32x32x16_bf16 v[160:175], v[4:7], v[148:151], v[160:175]\n\t"
        "v_lshlrev_b32 v194, 16, v249\n\t"
        "v_mfma_f32_32x32x16_bf16 v[176:191], v[204:207], v[148:151], v[176:191]\n\t"
        "v_and_b32 v195, 0xffff0000, v249\n\t"
        "s_waitcnt vmcnt(7)\n\t"
        "v_mfma_f32_32x32x16_bf16 v[160:175], v[8:11], v[152:155], v[160:175]\n\t"
        "v_lshlrev_b32 v196, 16, v250\n\t"
        "v_mfma_f32_32x32x16_bf16 v[176:191], v[208:211], v[152:155], v[176:191]\n\t"
        "v_and_b32 v197, 0xffff0000, v250\n\t"
        "s_waitcnt vmcnt(6)\n\t"
        "v_mfma_f32_32x32x16_bf16 v[160:175], v[12:15], v[156:159], v[160:175]\n\t"
        "v_lshlrev_b32 v198, 16, v251\n\t"
        "v_mfma_f32_32x32x16_bf16 v[176:191], v[212:215], v[156:159], v[176:191]\n\t"
        "v_and_b32 v199, 0xffff0000, v251\n\t"
        "s_waitcnt vmcnt(5)\n\t"
        "v_mfma_f32_32x32x16_bf16 v[160:175], v[16:19], v[96:99], v[160:175]\n\t"
        "v_mfma_f32_32x32x16_bf16 v[176:191], v[216:219], v[96:99], v[176:191]\n\t"
        "s_waitcnt vmcnt(4)\n\t"
        "v_mfma_f32_32x32x16_bf16 v[160:175], v[20:23], v[100:103], v[160:175]\n\t"
        "v_mfma_f32_32x32x16_bf16 v[176:191], v[220:223], v[100:103], v[176:191]\n\t"
        "s_waitcnt vmcnt(3)\n\t"
        "v_mfma_f32_32x32x16_bf16 v[160:175], v[24:27], v[104:107], v[160:175]\n\t"
        "v_mfma_f32_32x32x16_bf16 v[176:191], v[224:227], v[104:107], v[176:191]\n\t"
        "s_waitcnt vmcnt(1)\n\t"
        "v_mfma_f32_32x32x16_bf16 v[176:191], v[88:91], v[108:111], v[176:191]\n\t"
        "v_mfma_f32_32x32x16_bf16 v[160:175], v[28:31], v[108:111], v[160:175]\n\t"
        "s_mul_i32 s38, s27, 147456\n\t"
        "s_lshl_b32 s39, s25, 11\n\t"
        "s_lshl_b32 s40, s24, 3\n\t"
        "v_lshlrev_b32 v236, 4, v238\n\t"
        "s_add_u32 s38, s38, s39\n\t"
        "s_add_u32 s38, s38, s40\n\t"
        "s_add_u32 s38, s38, 106496\n\t"
        "s_waitcnt vmcnt(0)\n\t"
        "s_barrier\n\t"
        "s_cmp_lg_u32 s26, 0\n\t"
        "s_cbranch_scc1 sig_58_%=\n\t"
        "s_mov_b64 exec, 0xffff\n\t"
        "buffer_atomic_swap_x2 v[234:235], v236, s[16:19], s38 offen\n\t"
        "s_mov_b64 exec, -1\n\t"
        "sig_58_%=:\n\t"
        "ds_write_b128 v242, v[176:179] offset:0\n\t"
        "ds_write_b128 v242, v[180:183] offset:1024\n\t"
        "ds_write_b128 v241, v[184:187] offset:0\n\t"
        "ds_write_b128 v241, v[188:191] offset:1024\n\t"
        "ds_write_b128 v243, v[168:171] offset:0\n\t"
        "ds_write_b128 v243, v[172:175] offset:1024\n\t"
        "s_waitcnt lgkmcnt(0)\n\t"
        "s_barrier\n\t"
        "ds_read_b128 v[32:35], v240 offset:0\n\t"
        "ds_read_b128 v[40:43], v240 offset:2048\n\t"
        "ds_read_b128 v[48:51], v240 offset:4096\n\t"
        "ds_read_b128 v[36:39], v240 offset:1024\n\t"
        "ds_read_b128 v[44:47], v240 offset:3072\n\t"
        "ds_read_b128 v[52:55], v240 offset:5120\n\t"
        "s_waitcnt lgkmcnt(5)\n\t"
        "v_pk_add_f32 v[160:161], v[160:161], v[32:33]\n\t"
        "s_waitcnt lgkmcnt(3)\n\t"
        "v_pk_add_f32 v[40:41], v[40:41], v[48:49]\n\t"
        "v_pk_add_f32 v[162:163], v[162:163], v[34:35]\n\t"
        "v_pk_add_f32 v[42:43], v[42:43], v[50:51]\n\t"
        "s_waitcnt lgkmcnt(2)\n\t"
        "v_pk_add_f32 v[164:165], v[164:165], v[36:37]\n\t"
        "s_waitcnt lgkmcnt(0)\n\t"
        "v_pk_add_f32 v[44:45], v[44:45], v[52:53]\n\t"
        "v_pk_add_f32 v[166:167], v[166:167], v[38:39]\n\t"
        "v_pk_add_f32 v[46:47], v[46:47], v[54:55]\n\t"
        "v_pk_add_f32 v[160:161], v[160:161], v[40:41]\n\t"
        "v_pk_add_f32 v[162:163], v[162:163], v[42:43]\n\t"
        "v_pk_add_f32 v[164:165], v[164:165], v[44:45]\n\t"
        "v_pk_add_f32 v[166:167], v[166:167], v[46:47]\n\t"
        "s_add_u32 s47, s57, s28\n\t"
        "s_add_u32 s47, s47, 10682368\n\t"
        "s_add_u32 s33, s58, s28\n\t"
        "s_add_u32 s33, s33, 12517376\n\t"
        "s_mov_b32 s82, s47\n\t"
        "s_mov_b32 s83, s33\n\t"
        "v_pk_add_f32 v[160:161], v[160:161], v[192:193]\n\t"
        "v_pk_add_f32 v[162:163], v[162:163], v[194:195]\n\t"
        "v_pk_add_f32 v[164:165], v[164:165], v[196:197]\n\t"
        "v_pk_add_f32 v[166:167], v[166:167], v[198:199]\n\t"
        "v_cvt_pk_bf16_f32 v248, v160, v161\n\t"
        "v_cvt_pk_bf16_f32 v249, v162, v163\n\t"
        "v_cvt_pk_bf16_f32 v250, v164, v165\n\t"
        "v_cvt_pk_bf16_f32 v251, v166, v167\n\t"
        "buffer_store_dwordx4 v[248:251], v247, s[16:19], s47 offen sc1\n\t"
        "v_pk_add_f32 v[40:41], v[160:161], v[162:163]\n\t"
        "v_pk_mul_f32 v[42:43], v[160:161], v[160:161]\n\t"
        "v_pk_add_f32 v[44:45], v[164:165], v[166:167]\n\t"
        "v_pk_mul_f32 v[46:47], v[162:163], v[162:163]\n\t"
        "v_pk_fma_f32 v[42:43], v[164:165], v[164:165], v[42:43]\n\t"
        "v_pk_fma_f32 v[46:47], v[166:167], v[166:167], v[46:47]\n\t"
        "v_pk_add_f32 v[40:41], v[40:41], v[44:45]\n\t"
        "v_pk_add_f32 v[42:43], v[42:43], v[46:47]\n\t"
        "v_add_f32 v40, v40, v41\n\t"
        "v_add_f32 v41, v42, v43\n\t"
        "v_mov_b32 v42, v40\n\t"
        "v_mov_b32 v43, v41\n\t"
        "s_nop 0\n\t"
        "v_permlane32_swap_b32 v40, v42\n\t"
        "v_permlane32_swap_b32 v41, v43\n\t"
        "v_pk_add_f32 v[40:41], v[40:41], v[42:43]\n\t"
        "v_lshlrev_b32 v44, 1, v239\n\t"
        "s_mov_b64 exec, 0xffffffff\n\t"
        "buffer_store_dwordx2 v[40:41], v44, s[16:19], s33 offen sc1\n\t"
        "s_mov_b64 exec, -1\n\t"
        "s_add_u32 s34, s60, s28\n\t"
        "s_add_u32 s34, s34, 8650752\n\t"
        "s_mul_i32 s38, s27, 147456\n\t"
        "s_lshl_b32 s39, s24, 7\n\t"
        "s_lshl_b32 s40, s25, 3\n\t"
        "s_add_u32 s38, s38, s39\n\t"
        "s_add_u32 s38, s38, s40\n\t"
        "s_add_u32 s38, s38, 139264\n\t"
        "s_waitcnt vmcnt(0)\n\t"
        "s_barrier\n\t"
        "s_cmp_lg_u32 s26, 0\n\t"
        "s_cbranch_scc1 sig_59_%=\n\t"
        "s_mov_b64 exec, 1\n\t"
        "buffer_atomic_swap_x2 v[234:235], off, s[16:19], s38\n\t"
        "s_mov_b64 exec, -1\n\t"
        "sig_59_%=:\n\t"
        "s_cmp_lg_u32 s27, 0\n\t"
        "s_cbranch_scc1 l0_60_%=\n\t"
        "s_add_u32 s98, s98, 4096\n\t"
        "buffer_load_dwordx4 a[80:83], v246, s[16:19], s98 offen offset:0 sc1\n\t"
        "s_add_u32 s99, s99, 4096\n\t"
        "buffer_load_dwordx4 a[192:195], v246, s[16:19], s99 offen offset:0 sc1\n\t"
        "buffer_load_dwordx4 a[84:87], v246, s[16:19], s98 offen offset:1024 sc1\n\t"
        "buffer_load_dwordx4 a[196:199], v246, s[16:19], s99 offen offset:1024 sc1\n\t"
        "buffer_load_dwordx4 a[88:91], v246, s[16:19], s98 offen offset:2048 sc1\n\t"
        "buffer_load_dwordx4 a[200:203], v246, s[16:19], s99 offen offset:2048 sc1\n\t"
        "buffer_load_dwordx4 a[92:95], v246, s[16:19], s98 offen offset:3072 sc1\n\t"
        "buffer_load_dwordx4 a[204:207], v246, s[16:19], s99 offen offset:3072 sc1\n\t"
        "s_add_u32 s98, s98, 4096\n\t"
        "buffer_load_dwordx4 a[96:99], v246, s[16:19], s98 offen offset:0 sc1\n\t"
        "s_add_u32 s99, s99, 4096\n\t"
        "buffer_load_dwordx4 a[208:211], v246, s[16:19], s99 offen offset:0 sc1\n\t"
        "buffer_load_dwordx4 a[100:103], v246, s[16:19], s98 offen offset:1024 sc1\n\t"
        "buffer_load_dwordx4 a[212:215], v246, s[16:19], s99 offen offset:1024 sc1\n\t"
        "buffer_load_dwordx4 a[104:107], v246, s[16:19], s98 offen offset:2048 sc1\n\t"
        "buffer_load_dwordx4 a[216:219], v246, s[16:19], s99 offen offset:2048 sc1\n\t"
        "buffer_load_dwordx4 a[108:111], v246, s[16:19], s98 offen offset:3072 sc1\n\t"
        "buffer_load_dwordx4 a[220:223], v246, s[16:19], s99 offen offset:3072 sc1\n\t"
        "l0_60_%=:\n\t"
        "s_mul_i32 s9, s27, 147456\n\t"
        "s_add_u32 s9, s9, s12\n\t"
        "s_add_u32 s9, s9, 106496\n\t"
        "s_mov_b32 s42, 400000\n\t"
        "pl_61_%=:\n\t"
        "buffer_load_dwordx2 v[232:233], v238, s[16:19], s9 offen sc1\n\t"
        "s_waitcnt vmcnt(0)\n\t"
        "v_cmp_ne_u64 vcc, s[10:11], v[232:233]\n\t"
        "s_nop 0\n\t"
        "s_cbranch_vccz pd_62_%=\n\t"
        "s_sub_u32 s42, s42, 1\n\t"
        "s_cbranch_scc0 pl_61_%=\n\t"
        "pd_62_%=:\n\t"
        "buffer_load_dwordx4 v[112:115], v247, s[16:19], s34 offen offset:0 sc1\n\t"
        "buffer_load_dwordx4 v[116:119], v247, s[16:19], s34 offen offset:1024 sc1\n\t"
        "buffer_load_dwordx4 v[120:123], v247, s[16:19], s34 offen offset:2048 sc1\n\t"
        "buffer_load_dwordx4 v[124:127], v247, s[16:19], s34 offen offset:3072 sc1\n\t"
        "s_add_u32 s34, s34, 4096\n\t"
        "buffer_load_dwordx4 v[128:131], v247, s[16:19], s34 offen offset:0 sc1\n\t"
        "buffer_load_dwordx4 v[132:135], v247, s[16:19], s34 offen offset:1024 sc1\n\t"
        "buffer_load_dwordx4 v[136:139], v247, s[16:19], s34 offen offset:2048 sc1\n\t"
        "buffer_load_dwordx4 v[140:143], v247, s[16:19], s34 offen offset:3072 sc1\n\t"
        "s_sleep 6\n\t"
        "s_mul_i32 s9, s27, 147456\n\t"
        "s_add_u32 s9, s9, s14\n\t"
        "s_add_u32 s9, s9, 139264\n\t"
        "buffer_load_dwordx2 v[232:233], v237, s[16:19], s9 offen sc1\n\t"
        "s_waitcnt vmcnt(8)\n\t"
        "v_mfma_f32_32x32x16_bf16 v[160:175], v[0:3], v[112:115], 0\n\t"
        "v_lshlrev_b32 v192, 16, v252\n\t"
        "v_mfma_f32_32x32x16_bf16 v[176:191], v[200:203], v[112:115], 0\n\t"
        "v_and_b32 v193, 0xffff0000, v252\n\t"
        "s_waitcnt vmcnt(7)\n\t"
        "v_mfma_f32_32x32x16_bf16 v[160:175], v[4:7], v[116:119], v[160:175]\n\t"
        "v_lshlrev_b32 v194, 16, v253\n\t"
        "v_mfma_f32_32x32x16_bf16 v[176:191], v[204:207], v[116:119], v[176:191]\n\t"
        "v_and_b32 v195, 0xffff0000, v253\n\t"
        "s_waitcnt vmcnt(6)\n\t"
        "v_mfma_f32_32x32x16_bf16 v[160:175], v[8:11], v[120:123], v[160:175]\n\t"
        "v_lshlrev_b32 v196, 16, v254\n\t"
        "v_mfma_f32_32x32x16_bf16 v[176:191], v[208:211], v[120:123], v[176:191]\n\t"
        "v_and_b32 v197, 0xffff0000, v254\n\t"
        "s_waitcnt vmcnt(5)\n\t"
        "v_mfma_f32_32x32x16_bf16 v[160:175], v[12:15], v[124:127], v[160:175]\n\t"
        "v_lshlrev_b32 v198, 16, v255\n\t"
        "v_mfma_f32_32x32x16_bf16 v[176:191], v[212:215], v[124:127], v[176:191]\n\t"
        "v_and_b32 v199, 0xffff0000, v255\n\t"
        "s_waitcnt vmcnt(4)\n\t"
        "v_mfma_f32_32x32x16_bf16 v[160:175], v[16:19], v[128:131], v[160:175]\n\t"
        "v_mfma_f32_32x32x16_bf16 v[176:191], v[216:219], v[128:131], v[176:191]\n\t"
        "s_waitcnt vmcnt(3)\n\t"
        "v_mfma_f32_32x32x16_bf16 v[160:175], v[20:23], v[132:135], v[160:175]\n\t"
        "v_mfma_f32_32x32x16_bf16 v[176:191], v[220:223], v[132:135], v[176:191]\n\t"
        "s_waitcnt vmcnt(2)\n\t"
        "v_mfma_f32_32x32x16_bf16 v[160:175], v[24:27], v[136:139], v[160:175]\n\t"
        "v_mfma_f32_32x32x16_bf16 v[176:191], v[224:227], v[136:139], v[176:191]\n\t"
        "s_waitcnt vmcnt(1)\n\t"
        "v_mfma_f32_32x32x16_bf16 v[176:191], v[88:91], v[140:143], v[176:191]\n\t"
        "v_mfma_f32_32x32x16_bf16 v[160:175], v[28:31], v[140:143], v[160:175]\n\t"
        "s_waitcnt vmcnt(0)\n\t"
        "v_cmp_ne_u64 vcc, s[10:11], v[232:233]\n\t"
        "s_nop 0\n\t"
        "s_cbranch_vccnz sp_63_%=\n\t"
        "bk_64_%=:\n\t"
        "s_add_u32 s62, s29, s28\n\t"
        "s_add_u32 s62, s62, 10682368\n\t"
        "buffer_load_dwordx4 v[56:59], v231, s[16:19], s62 offen offset:0 sc1\n\t"
        "buffer_load_dwordx4 v[60:63], v231, s[16:19], s62 offen offset:2048 sc1\n\t"
        "s_add_u32 s62, s62, 4096\n\t"
        "buffer_load_dwordx4 v[64:67], v231, s[16:19], s62 offen offset:0 sc1\n\t"
        "buffer_load_dwordx4 v[68:71], v231, s[16:19], s62 offen offset:2048 sc1\n\t"
        "s_add_u32 s62, s62, 4096\n\t"
        "buffer_load_dwordx4 v[72:75], v231, s[16:19], s62 offen offset:0 sc1\n\t"
        "buffer_load_dwordx4 v[76:79], v231, s[16:19], s62 offen offset:2048 sc1\n\t"
        "s_add_u32 s62, s62, 4096\n\t"
        "buffer_load_dwordx4 v[80:83], v231, s[16:19], s62 offen offset:0 sc1\n\t"
        "buffer_load_dwordx4 v[84:87], v231, s[16:19], s62 offen offset:2048 sc1\n\t"
        "ds_write_b128 v242, v[176:179] offset:0\n\t"
        "ds_write_b128 v242, v[180:183] offset:1024\n\t"
        "ds_write_b128 v241, v[184:187] offset:0\n\t"
        "ds_write_b128 v241, v[188:191] offset:1024\n\t"
        "ds_write_b128 v243, v[168:171] offset:0\n\t"
        "ds_write_b128 v243, v[172:175] offset:1024\n\t"
        "s_waitcnt lgkmcnt(0)\n\t"
        "s_barrier\n\t"
        "ds_read_b128 v[32:35], v240 offset:0\n\t"
        "ds_read_b128 v[40:43], v240 offset:2048\n\t"
        "ds_read_b128 v[48:51], v240 offset:4096\n\t"
        "ds_read_b128 v[36:39], v240 offset:1024\n\t"
        "ds_read_b128 v[44:47], v240 offset:3072\n\t"
        "ds_read_b128 v[52:55], v240 offset:5120\n\t"
        "s_waitcnt lgkmcnt(5)\n\t"
        "v_pk_add_f32 v[160:161], v[160:161], v[32:33]\n\t"
        "s_waitcnt lgkmcnt(3)\n\t"
        "v_pk_add_f32 v[40:41], v[40:41], v[48:49]\n\t"
        "v_pk_add_f32 v[162:163], v[162:163], v[34:35]\n\t"
        "v_pk_add_f32 v[42:43], v[42:43], v[50:51]\n\t"
        "s_waitcnt lgkmcnt(2)\n\t"
        "v_pk_add_f32 v[164:165], v[164:165], v[36:37]\n\t"
        "s_waitcnt lgkmcnt(0)\n\t"
        "v_pk_add_f32 v[44:45], v[44:45], v[52:53]\n\t"
        "v_pk_add_f32 v[166:167], v[166:167], v[38:39]\n\t"
        "v_pk_add_f32 v[46:47], v[46:47], v[54:55]\n\t"
        "v_pk_add_f32 v[160:161], v[160:161], v[40:41]\n\t"
        "v_pk_add_f32 v[162:163], v[162:163], v[42:43]\n\t"
        "v_pk_add_f32 v[164:165], v[164:165], v[44:45]\n\t"
        "v_pk_add_f32 v[166:167], v[166:167], v[46:47]\n\t"
        "s_add_u32 s47, s57, s28\n\t"
        "s_add_u32 s47, s47, 10739712\n\t"
        "s_add_u32 s33, s58, s28\n\t"
        "s_add_u32 s33, s33, 12531712\n\t"
        "s_mov_b32 s82, s47\n\t"
        "s_mov_b32 s83, s33\n\t"
        "v_pk_add_f32 v[160:161], v[160:161], v[192:193]\n\t"
        "v_pk_add_f32 v[162:163], v[162:163], v[194:195]\n\t"
        "v_pk_add_f32 v[164:165], v[164:165], v[196:197]\n\t"
        "v_pk_add_f32 v[166:167], v[166:167], v[198:199]\n\t"
        "v_cvt_pk_bf16_f32 v252, v160, v161\n\t"
        "v_cvt_pk_bf16_f32 v253, v162, v163\n\t"
        "v_cvt_pk_bf16_f32 v254, v164, v165\n\t"
        "v_cvt_pk_bf16_f32 v255, v166, v167\n\t"
        "buffer_store_dwordx4 v[252:255], v247, s[16:19], s47 offen sc1\n\t"
        "v_pk_add_f32 v[40:41], v[160:161], v[162:163]\n\t"
        "v_pk_mul_f32 v[42:43], v[160:161], v[160:161]\n\t"
        "v_pk_add_f32 v[44:45], v[164:165], v[166:167]\n\t"
        "v_pk_mul_f32 v[46:47], v[162:163], v[162:163]\n\t"
        "v_pk_fma_f32 v[42:43], v[164:165], v[164:165], v[42:43]\n\t"
        "v_pk_fma_f32 v[46:47], v[166:167], v[166:167], v[46:47]\n\t"
        "v_pk_add_f32 v[40:41], v[40:41], v[44:45]\n\t"
        "v_pk_add_f32 v[42:43], v[42:43], v[46:47]\n\t"
        "v_add_f32 v40, v40, v41\n\t"
        "v_add_f32 v41, v42, v43\n\t"
        "v_mov_b32 v42, v40\n\t"
        "v_mov_b32 v43, v41\n\t"
        "s_nop 0\n\t"
        "v_permlane32_swap_b32 v40, v42\n\t"
        "v_permlane32_swap_b32 v41, v43\n\t"
        "v_pk_add_f32 v[40:41], v[40:41], v[42:43]\n\t"
        "v_lshlrev_b32 v44, 1, v239\n\t"
        "s_mov_b64 exec, 0xffffffff\n\t"
        "buffer_store_dwordx2 v[40:41], v44, s[16:19], s33 offen sc1\n\t"
        "s_mov_b64 exec, -1\n\t"
        "s_lshl_b32 s38, s54, 8\n\t"
        "v_subrev_u32 v228, s38, v231\n\t"
        "v_lshrrev_b32 v230, 2, v239\n\t"
        "v_add_u32 v229, 57344, v228\n\t"
        "v_and_b32 v230, 0x30, v230\n\t"
        "v_add_u32 v230, 155648, v230\n\t"
        "v_mov_b32 v226, 0xbfb17218\n\t"
        "v_mov_b32 v227, 0x3f317218\n\t"
        "ds_read_b128 v[144:147], v228 offset:0\n\t"
        "ds_read_b128 v[148:151], v228 offset:256\n\t"
        "ds_read_b128 v[152:155], v228 offset:28672\n\t"
        "ds_read_b128 v[156:159], v228 offset:28928\n\t"
        "ds_read_b128 v[160:163], v229 offset:0\n\t"
        "ds_read_b128 v[164:167], v229 offset:256\n\t"
        "ds_read_b128 v[168:171], v229 offset:28672\n\t"
        "ds_read_b128 v[172:175], v229 offset:28928\n\t"
        "ds_read_b128 v[176:179], v228 offset:2048\n\t"
        "ds_read_b128 v[180:183], v228 offset:2304\n\t"
        "ds_read_b128 v[184:187], v228 offset:30720\n\t"
        "s_waitcnt lgkmcnt(7)\n\t"
        "s_waitcnt vmcnt(9)\n\t"
        "v_mfma_f32_16x16x32_bf16 v[112:115], v[144:147], v[56:59], 0\n\t"
        "ds_read_b128 v[188:191], v228 offset:30976\n\t"
        "s_add_u32 s62, s62, 4096\n\t"
        "buffer_load_dwordx4 v[88:91], v231, s[16:19], s62 offen offset:0 sc1\n\t"
        "v_mfma_f32_16x16x32_bf16 v[116:119], v[148:151], v[56:59], 0\n\t"
        "ds_read_b128 v[144:147], v229 offset:2048\n\t"
        "v_mfma_f32_16x16x32_bf16 v[120:123], v[152:155], v[56:59], 0\n\t"
        "ds_read_b128 v[148:151], v229 offset:2304\n\t"
        "v_mfma_f32_16x16x32_bf16 v[124:127], v[156:159], v[56:59], 0\n\t"
        "ds_read_b128 v[152:155], v229 offset:30720\n\t"
        "s_waitcnt lgkmcnt(7)\n\t"
        "v_mfma_f32_16x16x32_bf16 v[128:131], v[160:163], v[56:59], 0\n\t"
        "ds_read_b128 v[156:159], v229 offset:30976\n\t"
        "buffer_load_dwordx4 v[92:95], v231, s[16:19], s62 offen offset:2048 sc1\n\t"
        "v_mfma_f32_16x16x32_bf16 v[132:135], v[164:167], v[56:59], 0\n\t"
        "ds_read_b128 v[160:163], v228 offset:4096\n\t"
        "v_mfma_f32_16x16x32_bf16 v[136:139], v[168:171], v[56:59], 0\n\t"
        "ds_read_b128 v[164:167], v228 offset:4352\n\t"
        "v_mfma_f32_16x16x32_bf16 v[140:143], v[172:175], v[56:59], 0\n\t"
        "ds_read_b128 v[168:171], v228 offset:32768\n\t"
        "s_waitcnt lgkmcnt(7)\n\t"
        "s_waitcnt vmcnt(10)\n\t"
        "v_mfma_f32_16x16x32_bf16 v[112:115], v[176:179], v[60:63], v[112:115]\n\t"
        "ds_read_b128 v[172:175], v228 offset:33024\n\t"
        "s_add_u32 s62, s62, 4096\n\t"
        "buffer_load_dwordx4 v[96:99], v231, s[16:19], s62 offen offset:0 sc1\n\t"
        "v_mfma_f32_16x16x32_bf16 v[116:119], v[180:183], v[60:63], v[116:119]\n\t"
        "ds_read_b128 v[176:179], v229 offset:4096\n\t"
        "v_mfma_f32_16x16x32_bf16 v[120:123], v[184:187], v[60:63], v[120:123]\n\t"
        "ds_read_b128 v[180:183], v229 offset:4352\n\t"
        "v_mfma_f32_16x16x32_bf16 v[124:127], v[188:191], v[60:63], v[124:127]\n\t"
        "ds_read_b128 v[184:187], v229 offset:32768\n\t"
        "s_waitcnt lgkmcnt(7)\n\t"
        "v_mfma_f32_16x16x32_bf16 v[128:131], v[144:147], v[60:63], v[128:131]\n\t"
        "ds_read_b128 v[188:191], v229 offset:33024\n\t"
        "buffer_load_dwordx4 v[100:103], v231, s[16:19], s62 offen offset:2048 sc1\n\t"
        "v_mfma_f32_16x16x32_bf16 v[132:135], v[148:151], v[60:63], v[132:135]\n\t"
        "ds_read_b128 v[144:147], v228 offset:6144\n\t"
        "v_mfma_f32_16x16x32_bf16 v[136:139], v[152:155], v[60:63], v[136:139]\n\t"
        "ds_read_b128 v[148:151], v228 offset:6400\n\t"
        "v_mfma_f32_16x16x32_bf16 v[140:143], v[156:159], v[60:63], v[140:143]\n\t"
        "ds_read_b128 v[152:155], v228 offset:34816\n\t"
        "s_waitcnt lgkmcnt(7)\n\t"
        "s_waitcnt vmcnt(11)\n\t"
        "v_mfma_f32_16x16x32_bf16 v[112:115], v[160:163], v[64:67], v[112:115]\n\t"
        "ds_read_b128 v[156:159], v228 offset:35072\n\t"
        "s_add_u32 s62, s62, 4096\n\t"
        "buffer_load_dwordx4 v[104:107], v231, s[16:19], s62 offen offset:0 sc1\n\t"
        "v_mfma_f32_16x16x32_bf16 v[116:119], v[164:167], v[64:67], v[116:119]\n\t"
        "ds_read_b128 v[160:163], v229 offset:6144\n\t"
        "v_mfma_f32_16x16x32_bf16 v[120:123], v[168:171], v[64:67], v[120:123]\n\t"
        "ds_read_b128 v[164:167], v229 offset:6400\n\t"
        "v_mfma_f32_16x16x32_bf16 v[124:127], v[172:175], v[64:67], v[124:127]\n\t"
        "ds_read_b128 v[168:171], v229 offset:34816\n\t"
        "s_waitcnt lgkmcnt(7)\n\t"
        "v_mfma_f32_16x16x32_bf16 v[128:131], v[176:179], v[64:67], v[128:131]\n\t"
        "ds_read_b128 v[172:175], v229 offset:35072\n\t"
        "buffer_load_dwordx4 v[108:111], v231, s[16:19], s62 offen offset:2048 sc1\n\t"
        "v_mfma_f32_16x16x32_bf16 v[132:135], v[180:183], v[64:67], v[132:135]\n\t"
        "ds_read_b128 v[176:179], v228 offset:8192\n\t"
        "v_mfma_f32_16x16x32_bf16 v[136:139], v[184:187], v[64:67], v[136:139]\n\t"
        "ds_read_b128 v[180:183], v228 offset:8448\n\t"
        "v_mfma_f32_16x16x32_bf16 v[140:143], v[188:191], v[64:67], v[140:143]\n\t"
        "ds_read_b128 v[184:187], v228 offset:36864\n\t"
        "s_waitcnt lgkmcnt(7)\n\t"
        "s_waitcnt vmcnt(12)\n\t"
        "v_mfma_f32_16x16x32_bf16 v[112:115], v[144:147], v[68:71], v[112:115]\n\t"
        "ds_read_b128 v[188:191], v228 offset:37120\n\t"
        "s_add_u32 s33, s30, s28\n\t"
        "s_add_u32 s33, s33, 12517376\n\t"
        "buffer_load_dwordx2 v[0:1], v245, s[16:19], s33 offen offset:0 sc1\n\t"
        "v_mfma_f32_16x16x32_bf16 v[116:119], v[148:151], v[68:71], v[116:119]\n\t"
        "ds_read_b128 v[144:147], v229 offset:8192\n\t"
        "v_mfma_f32_16x16x32_bf16 v[120:123], v[152:155], v[68:71], v[120:123]\n\t"
        "ds_read_b128 v[148:151], v229 offset:8448\n\t"
        "v_mfma_f32_16x16x32_bf16 v[124:127], v[156:159], v[68:71], v[124:127]\n\t"
        "ds_read_b128 v[152:155], v229 offset:36864\n\t"
        "s_waitcnt lgkmcnt(7)\n\t"
        "v_mfma_f32_16x16x32_bf16 v[128:131], v[160:163], v[68:71], v[128:131]\n\t"
        "ds_read_b128 v[156:159], v229 offset:37120\n\t"
        "buffer_load_dwordx2 v[2:3], v245, s[16:19], s33 offen offset:256 sc1\n\t"
        "v_mfma_f32_16x16x32_bf16 v[132:135], v[164:167], v[68:71], v[132:135]\n\t"
        "ds_read_b128 v[160:163], v228 offset:10240\n\t"
        "v_mfma_f32_16x16x32_bf16 v[136:139], v[168:171], v[68:71], v[136:139]\n\t"
        "ds_read_b128 v[164:167], v228 offset:10496\n\t"
        "v_mfma_f32_16x16x32_bf16 v[140:143], v[172:175], v[68:71], v[140:143]\n\t"
        "ds_read_b128 v[168:171], v228 offset:38912\n\t"
        "s_waitcnt lgkmcnt(7)\n\t"
        "s_waitcnt vmcnt(13)\n\t"
        "v_mfma_f32_16x16x32_bf16 v[112:115], v[176:179], v[72:75], v[112:115]\n\t"
        "ds_read_b128 v[172:175], v228 offset:39168\n\t"
        "buffer_load_dwordx2 v[4:5], v245, s[16:19], s33 offen offset:512 sc1\n\t"
        "v_mfma_f32_16x16x32_bf16 v[116:119], v[180:183], v[72:75], v[116:119]\n\t"
        "ds_read_b128 v[176:179], v229 offset:10240\n\t"
        "v_mfma_f32_16x16x32_bf16 v[120:123], v[184:187], v[72:75], v[120:123]\n\t"
        "ds_read_b128 v[180:183], v229 offset:10496\n\t"
        "v_mfma_f32_16x16x32_bf16 v[124:127], v[188:191], v[72:75], v[124:127]\n\t"
        "ds_read_b128 v[184:187], v229 offset:38912\n\t"
        "s_waitcnt lgkmcnt(7)\n\t"
        "v_mfma_f32_16x16x32_bf16 v[128:131], v[144:147], v[72:75], v[128:131]\n\t"
        "ds_read_b128 v[188:191], v229 offset:39168\n\t"
        "buffer_load_dwordx2 v[6:7], v245, s[16:19], s33 offen offset:768 sc1\n\t"
        "v_mfma_f32_16x16x32_bf16 v[132:135], v[148:151], v[72:75], v[132:135]\n\t"
        "ds_read_b128 v[144:147], v228 offset:12288\n\t"
        "v_mfma_f32_16x16x32_bf16 v[136:139], v[152:155], v[72:75], v[136:139]\n\t"
        "ds_read_b128 v[148:151], v228 offset:12544\n\t"
        "v_mfma_f32_16x16x32_bf16 v[140:143], v[156:159], v[72:75], v[140:143]\n\t"
        "ds_read_b128 v[152:155], v228 offset:40960\n\t"
        "s_mul_i32 s38, s27, 147456\n\t"
        "s_lshl_b32 s39, s24, 7\n\t"
        "s_lshl_b32 s40, s25, 3\n\t"
        "s_add_u32 s38, s38, s39\n\t"
        "s_add_u32 s38, s38, s40\n\t"
        "s_add_u32 s38, s38, 141312\n\t"
        "s_waitcnt vmcnt(10)\n\t"
        "s_barrier\n\t"
        "s_cmp_lg_u32 s26, 0\n\t"
        "s_cbranch_scc1 sig_65_%=\n\t"
        "s_mov_b64 exec, 1\n\t"
        "buffer_atomic_swap_x2 v[234:235], off, s[16:19], s38\n\t"
        "s_mov_b64 exec, -1\n\t"
        "sig_65_%=:\n\t"
        "s_waitcnt lgkmcnt(7)\n\t"
        "v_mfma_f32_16x16x32_bf16 v[112:115], v[160:163], v[76:79], v[112:115]\n\t"
        "ds_read_b128 v[156:159], v228 offset:41216\n\t"
        "buffer_load_dwordx2 v[8:9], v245, s[16:19], s33 offen offset:1024 sc1\n\t"
        "v_mfma_f32_16x16x32_bf16 v[116:119], v[164:167], v[76:79], v[116:119]\n\t"
        "ds_read_b128 v[160:163], v229 offset:12288\n\t"
        "v_mfma_f32_16x16x32_bf16 v[120:123], v[168:171], v[76:79], v[120:123]\n\t"
        "ds_read_b128 v[164:167], v229 offset:12544\n\t"
        "v_mfma_f32_16x16x32_bf16 v[124:127], v[172:175], v[76:79], v[124:127]\n\t"
        "ds_read_b128 v[168:171], v229 offset:40960\n\t"
        "s_waitcnt lgkmcnt(7)\n\t"
        "v_mfma_f32_16x16x32_bf16 v[128:131], v[176:179], v[76:79], v[128:131]\n\t"
        "ds_read_b128 v[172:175], v229 offset:41216\n\t"
        "buffer_load_dwordx2 v[10:11], v245, s[16:19], s33 offen offset:1280 sc1\n\t"
        "v_mfma_f32_16x16x32_bf16 v[132:135], v[180:183], v[76:79], v[132:135]\n\t"
        "ds_read_b128 v[176:179], v228 offset:14336\n\t"
        "v_mfma_f32_16x16x32_bf16 v[136:139], v[184:187], v[76:79], v[136:139]\n\t"
        "ds_read_b128 v[180:183], v228 offset:14592\n\t"
        "v_mfma_f32_16x16x32_bf16 v[140:143], v[188:191], v[76:79], v[140:143]\n\t"
        "ds_read_b128 v[184:187], v228 offset:43008\n\t"
        "s_waitcnt lgkmcnt(7)\n\t"
        "v_mfma_f32_16x16x32_bf16 v[112:115], v[144:147], v[80:83], v[112:115]\n\t"
        "ds_read_b128 v[188:191], v228 offset:43264\n\t"
        "buffer_load_dwordx2 v[12:13], v245, s[16:19], s33 offen offset:1536 sc1\n\t"
        "v_mfma_f32_16x16x32_bf16 v[116:119], v[148:151], v[80:83], v[116:119]\n\t"
        "ds_read_b128 v[144:147], v229 offset:14336\n\t"
        "v_mfma_f32_16x16x32_bf16 v[120:123], v[152:155], v[80:83], v[120:123]\n\t"
        "ds_read_b128 v[148:151], v229 offset:14592\n\t"
        "v_mfma_f32_16x16x32_bf16 v[124:127], v[156:159], v[80:83], v[124:127]\n\t"
        "ds_read_b128 v[152:155], v229 offset:43008\n\t"
        "s_waitcnt lgkmcnt(7)\n\t"
        "v_mfma_f32_16x16x32_bf16 v[128:131], v[160:163], v[80:83], v[128:131]\n\t"
        "ds_read_b128 v[156:159], v229 offset:43264\n\t"
        "v_mfma_f32_16x16x32_bf16 v[132:135], v[164:167], v[80:83], v[132:135]\n\t"
        "ds_read_b128 v[160:163], v228 offset:16384\n\t"
        "v_mfma_f32_16x16x32_bf16 v[136:139], v[168:171], v[80:83], v[136:139]\n\t"
        "ds_read_b128 v[164:167], v228 offset:16640\n\t"
        "v_mfma_f32_16x16x32_bf16 v[140:143], v[172:175], v[80:83], v[140:143]\n\t"
        "ds_read_b128 v[168:171], v228 offset:45056\n\t"
        "s_waitcnt lgkmcnt(7)\n\t"
        "v_mfma_f32_16x16x32_bf16 v[112:115], v[176:179], v[84:87], v[112:115]\n\t"
        "ds_read_b128 v[172:175], v228 offset:45312\n\t"
        "v_mfma_f32_16x16x32_bf16 v[116:119], v[180:183], v[84:87], v[116:119]\n\t"
        "ds_read_b128 v[176:179], v229 offset:16384\n\t"
        "v_mfma_f32_16x16x32_bf16 v[120:123], v[184:187], v[84:87], v[120:123]\n\t"
        "ds_read_b128 v[180:183], v229 offset:16640\n\t"
        "v_mfma_f32_16x16x32_bf16 v[124:127], v[188:191], v[84:87], v[124:127]\n\t"
        "ds_read_b128 v[184:187], v229 offset:45056\n\t"
        "s_waitcnt lgkmcnt(7)\n\t"
        "v_mfma_f32_16x16x32_bf16 v[128:131], v[144:147], v[84:87], v[128:131]\n\t"
        "ds_read_b128 v[188:191], v229 offset:45312\n\t"
        "v_mfma_f32_16x16x32_bf16 v[132:135], v[148:151], v[84:87], v[132:135]\n\t"
        "ds_read_b128 v[144:147], v228 offset:18432\n\t"
        "v_mfma_f32_16x16x32_bf16 v[136:139], v[152:155], v[84:87], v[136:139]\n\t"
        "ds_read_b128 v[148:151], v228 offset:18688\n\t"
        "v_mfma_f32_16x16x32_bf16 v[140:143], v[156:159], v[84:87], v[140:143]\n\t"
        "ds_read_b128 v[152:155], v228 offset:47104\n\t"
        "s_waitcnt lgkmcnt(7)\n\t"
        "s_waitcnt vmcnt(12)\n\t"
        "v_mfma_f32_16x16x32_bf16 v[112:115], v[160:163], v[88:91], v[112:115]\n\t"
        "ds_read_b128 v[156:159], v228 offset:47360\n\t"
        "v_mfma_f32_16x16x32_bf16 v[116:119], v[164:167], v[88:91], v[116:119]\n\t"
        "ds_read_b128 v[160:163], v229 offset:18432\n\t"
        "v_mfma_f32_16x16x32_bf16 v[120:123], v[168:171], v[88:91], v[120:123]\n\t"
        "ds_read_b128 v[164:167], v229 offset:18688\n\t"
        "v_mfma_f32_16x16x32_bf16 v[124:127], v[172:175], v[88:91], v[124:127]\n\t"
        "ds_read_b128 v[168:171], v229 offset:47104\n\t"
        "s_waitcnt lgkmcnt(7)\n\t"
        "v_mfma_f32_16x16x32_bf16 v[128:131], v[176:179], v[88:91], v[128:131]\n\t"
        "ds_read_b128 v[172:175], v229 offset:47360\n\t"
        "v_mfma_f32_16x16x32_bf16 v[132:135], v[180:183], v[88:91], v[132:135]\n\t"
        "ds_read_b128 v[176:179], v228 offset:20480\n\t"
        "v_mfma_f32_16x16x32_bf16 v[136:139], v[184:187], v[88:91], v[136:139]\n\t"
        "ds_read_b128 v[180:183], v228 offset:20736\n\t"
        "v_mfma_f32_16x16x32_bf16 v[140:143], v[188:191], v[88:91], v[140:143]\n\t"
        "ds_read_b128 v[184:187], v228 offset:49152\n\t"
        "s_waitcnt lgkmcnt(7)\n\t"
        "s_waitcnt vmcnt(11)\n\t"
        "v_mfma_f32_16x16x32_bf16 v[112:115], v[144:147], v[92:95], v[112:115]\n\t"
        "ds_read_b128 v[188:191], v228 offset:49408\n\t"
        "v_mfma_f32_16x16x32_bf16 v[116:119], v[148:151], v[92:95], v[116:119]\n\t"
        "ds_read_b128 v[144:147], v229 offset:20480\n\t"
        "v_mfma_f32_16x16x32_bf16 v[120:123], v[152:155], v[92:95], v[120:123]\n\t"
        "ds_read_b128 v[148:151], v229 offset:20736\n\t"
        "v_mfma_f32_16x16x32_bf16 v[124:127], v[156:159], v[92:95], v[124:127]\n\t"
        "ds_read_b128 v[152:155], v229 offset:49152\n\t"
        "s_waitcnt lgkmcnt(7)\n\t"
        "v_mfma_f32_16x16x32_bf16 v[128:131], v[160:163], v[92:95], v[128:131]\n\t"
        "ds_read_b128 v[156:159], v229 offset:49408\n\t"
        "v_mfma_f32_16x16x32_bf16 v[132:135], v[164:167], v[92:95], v[132:135]\n\t"
        "ds_read_b128 v[160:163], v228 offset:22528\n\t"
        "v_mfma_f32_16x16x32_bf16 v[136:139], v[168:171], v[92:95], v[136:139]\n\t"
        "ds_read_b128 v[164:167], v228 offset:22784\n\t"
        "v_mfma_f32_16x16x32_bf16 v[140:143], v[172:175], v[92:95], v[140:143]\n\t"
        "ds_read_b128 v[168:171], v228 offset:51200\n\t"
        "s_waitcnt lgkmcnt(7)\n\t"
        "s_waitcnt vmcnt(10)\n\t"
        "v_mfma_f32_16x16x32_bf16 v[112:115], v[176:179], v[96:99], v[112:115]\n\t"
        "ds_read_b128 v[172:175], v228 offset:51456\n\t"
        "s_waitcnt vmcnt(5)\n\t"
        "v_add_f32 v0, v0, v2\n\t"
        "v_mfma_f32_16x16x32_bf16 v[116:119], v[180:183], v[96:99], v[116:119]\n\t"
        "ds_read_b128 v[176:179], v229 offset:22528\n\t"
        "v_add_f32 v1, v1, v3\n\t"
        "v_mfma_f32_16x16x32_bf16 v[120:123], v[184:187], v[96:99], v[120:123]\n\t"
        "ds_read_b128 v[180:183], v229 offset:22784\n\t"
        "s_waitcnt vmcnt(3)\n\t"
        "v_add_f32 v4, v4, v6\n\t"
        "v_mfma_f32_16x16x32_bf16 v[124:127], v[188:191], v[96:99], v[124:127]\n\t"
        "ds_read_b128 v[184:187], v229 offset:51200\n\t"
        "v_add_f32 v5, v5, v7\n\t"
        "s_waitcnt lgkmcnt(7)\n\t"
        "v_mfma_f32_16x16x32_bf16 v[128:131], v[144:147], v[96:99], v[128:131]\n\t"
        "ds_read_b128 v[188:191], v229 offset:51456\n\t"
        "s_waitcnt vmcnt(1)\n\t"
        "v_add_f32 v8, v8, v10\n\t"
        "v_mfma_f32_16x16x32_bf16 v[132:135], v[148:151], v[96:99], v[132:135]\n\t"
        "ds_read_b128 v[144:147], v228 offset:24576\n\t"
        "v_add_f32 v9, v9, v11\n\t"
        "v_mfma_f32_16x16x32_bf16 v[136:139], v[152:155], v[96:99], v[136:139]\n\t"
        "ds_read_b128 v[148:151], v228 offset:24832\n\t"
        "s_waitcnt vmcnt(0)\n\t"
        "v_add_f32 v0, v0, v12\n\t"
        "v_mfma_f32_16x16x32_bf16 v[140:143], v[156:159], v[96:99], v[140:143]\n\t"
        "ds_read_b128 v[152:155], v228 offset:53248\n\t"
        "v_add_f32 v1, v1, v13\n\t"
        "s_waitcnt lgkmcnt(7)\n\t"
        "v_mfma_f32_16x16x32_bf16 v[112:115], v[160:163], v[100:103], v[112:115]\n\t"
        "ds_read_b128 v[156:159], v228 offset:53504\n\t"
        "v_add_f32 v4, v4, v8\n\t"
        "v_mfma_f32_16x16x32_bf16 v[116:119], v[164:167], v[100:103], v[116:119]\n\t"
        "ds_read_b128 v[160:163], v229 offset:24576\n\t"
        "v_add_f32 v5, v5, v9\n\t"
        "v_mfma_f32_16x16x32_bf16 v[120:123], v[168:171], v[100:103], v[120:123]\n\t"
        "ds_read_b128 v[164:167], v229 offset:24832\n\t"
        "v_add_f32 v0, v0, v4\n\t"
        "v_mfma_f32_16x16x32_bf16 v[124:127], v[172:175], v[100:103], v[124:127]\n\t"
        "ds_read_b128 v[168:171], v229 offset:53248\n\t"
        "v_add_f32 v1, v1, v5\n\t"
        "s_waitcnt lgkmcnt(7)\n\t"
        "v_mfma_f32_16x16x32_bf16 v[128:131], v[176:179], v[100:103], v[128:131]\n\t"
        "ds_read_b128 v[172:175], v229 offset:53504\n\t"
        "v_mov_b32 v2, v0\n\t"
        "v_mfma_f32_16x16x32_bf16 v[132:135], v[180:183], v[100:103], v[132:135]\n\t"
        "ds_read_b128 v[176:179], v228 offset:26624\n\t"
        "v_mov_b32 v3, v1\n\t"
        "v_mfma_f32_16x16x32_bf16 v[136:139], v[184:187], v[100:103], v[136:139]\n\t"
        "ds_read_b128 v[180:183], v228 offset:26880\n\t"
        "v_permlane32_swap_b32 v0, v2\n\t"
        "v_mfma_f32_16x16x32_bf16 v[140:143], v[188:191], v[100:103], v[140:143]\n\t"
        "ds_read_b128 v[184:187], v228 offset:55296\n\t"
        "s_mul_i32 s9, s27, 147456\n\t"
        "s_add_u32 s9, s9, s14\n\t"
        "s_add_u32 s9, s9, 141312\n\t"
        "buffer_load_dwordx2 v[232:233], v237, s[16:19], s9 offen sc1\n\t"
        "v_permlane32_swap_b32 v1, v3\n\t"
        "s_waitcnt lgkmcnt(7)\n\t"
        "v_mfma_f32_16x16x32_bf16 v[112:115], v[144:147], v[104:107], v[112:115]\n\t"
        "ds_read_b128 v[188:191], v228 offset:55552\n\t"
        "v_add_f32 v0, v0, v2\n\t"
        "v_mfma_f32_16x16x32_bf16 v[116:119], v[148:151], v[104:107], v[116:119]\n\t"
        "ds_read_b128 v[144:147], v229 offset:26624\n\t"
        "v_add_f32 v1, v1, v3\n\t"
        "v_mfma_f32_16x16x32_bf16 v[120:123], v[152:155], v[104:107], v[120:123]\n\t"
        "ds_read_b128 v[148:151], v229 offset:26880\n\t"
        "v_mov_b32 v2, v0\n\t"
        "v_mfma_f32_16x16x32_bf16 v[124:127], v[156:159], v[104:107], v[124:127]\n\t"
        "ds_read_b128 v[152:155], v229 offset:55296\n\t"
        "v_mov_b32 v3, v1\n\t"
        "s_waitcnt lgkmcnt(7)\n\t"
        "v_mfma_f32_16x16x32_bf16 v[128:131], v[160:163], v[104:107], v[128:131]\n\t"
        "ds_read_b128 v[156:159], v229 offset:55552\n\t"
        "v_permlane16_swap_b32 v0, v2\n\t"
        "v_mfma_f32_16x16x32_bf16 v[132:135], v[164:167], v[104:107], v[132:135]\n\t"
        "v_permlane16_swap_b32 v1, v3\n\t"
        "v_mfma_f32_16x16x32_bf16 v[136:139], v[168:171], v[104:107], v[136:139]\n\t"
        "v_add_f32 v0, v0, v2\n\t"
        "v_mfma_f32_16x16x32_bf16 v[140:143], v[172:175], v[104:107], v[140:143]\n\t"
        "v_add_f32 v1, v1, v3\n\t"
        "s_waitcnt lgkmcnt(4)\n\t"
        "v_mfma_f32_16x16x32_bf16 v[112:115], v[176:179], v[108:111], v[112:115]\n\t"
        "v_mul_f32 v0, 0x3b124925, v0\n\t"
        "v_mfma_f32_16x16x32_bf16 v[116:119], v[180:183], v[108:111], v[116:119]\n\t"
        "v_mul_f32 v1, 0x3b124925, v1\n\t"
        "v_mfma_f32_16x16x32_bf16 v[120:123], v[184:187], v[108:111], v[120:123]\n\t"
        "v_fma_f32 v1, -v0, v0, v1\n\t"
        "v_mfma_f32_16x16x32_bf16 v[124:127], v[188:191], v[108:111], v[124:127]\n\t"
        "v_max_f32 v1, 0, v1\n\t"
        "s_waitcnt lgkmcnt(0)\n\t"
        "v_mfma_f32_16x16x32_bf16 v[128:131], v[144:147], v[108:111], v[128:131]\n\t"
        "v_add_f32 v1, 0x3727c5ac, v1\n\t"
        "v_mfma_f32_16x16x32_bf16 v[132:135], v[148:151], v[108:111], v[132:135]\n\t"
        "v_rsq_f32 v224, v1\n\t"
        "v_mfma_f32_16x16x32_bf16 v[136:139], v[152:155], v[108:111], v[136:139]\n\t"
        "v_mul_f32 v224, 0x3fb8aa3b, v224\n\t"
        "v_mfma_f32_16x16x32_bf16 v[140:143], v[156:159], v[108:111], v[140:143]\n\t"
        "v_mul_f32 v225, -v0, v224\n\t"
        "ds_read_b128 v[56:59], v230 offset:0\n\t"
        "ds_read_b128 v[60:63], v230 offset:64\n\t"
        "ds_read_b128 v[64:67], v230 offset:128\n\t"
        "ds_read_b128 v[68:71], v230 offset:192\n\t"
        "ds_read_b128 v[72:75], v230 offset:256\n\t"
        "ds_read_b128 v[76:79], v230 offset:320\n\t"
        "ds_read_b128 v[80:83], v230 offset:384\n\t"
        "ds_read_b128 v[84:87], v230 offset:448\n\t"
        "s_add_u32 s47, s59, s28\n\t"
        "s_add_u32 s47, s47, 12976128\n\t"
        "s_mov_b32 s82, s47\n\t"
        "s_add_u32 s83, s47, 4096\n\t"
        "s_waitcnt vmcnt(0)\n\t"
        "v_cmp_ne_u64 vcc, s[10:11], v[232:233]\n\t"
        "s_nop 0\n\t"
        "s_cbranch_vccnz sp_66_%=\n\t"
        "bk_67_%=:\n\t"
        "s_add_u32 s62, s29, s28\n\t"
        "s_add_u32 s62, s62, 10739712\n\t"
        "buffer_load_dwordx4 v[0:3], v231, s[16:19], s62 offen offset:0 sc1\n\t"
        "buffer_load_dwordx4 v[4:7], v231, s[16:19], s62 offen offset:2048 sc1\n\t"
        "ds_read_b128 v[88:91], v230 offset:512\n\t"
        "ds_read_b128 v[92:95], v230 offset:576\n\t"
        "ds_read_b128 v[96:99], v230 offset:640\n\t"
        "ds_read_b128 v[100:103], v230 offset:704\n\t"
        "s_waitcnt lgkmcnt(9)\n\t"
        "v_pk_fma_f32 v[56:57], v[56:57], v[224:225], v[64:65] op_sel:[0,1,0] op_sel_hi:[1,1,1]\n\t"
        "v_pk_fma_f32 v[58:59], v[58:59], v[224:225], v[66:67] op_sel:[0,1,0] op_sel_hi:[1,1,1]\n\t"
        "s_waitcnt lgkmcnt(8)\n\t"
        "v_pk_fma_f32 v[60:61], v[60:61], v[224:225], v[68:69] op_sel:[0,1,0] op_sel_hi:[1,1,1]\n\t"
        "v_pk_fma_f32 v[62:63], v[62:63], v[224:225], v[70:71] op_sel:[0,1,0] op_sel_hi:[1,1,1]\n\t"
        "v_pk_fma_f32 v[112:113], v[112:113], v[224:225], v[56:57] op_sel_hi:[1,0,1]\n\t"
        "v_pk_fma_f32 v[114:115], v[114:115], v[224:225], v[58:59] op_sel_hi:[1,0,1]\n\t"
        "v_pk_fma_f32 v[116:117], v[116:117], v[224:225], v[60:61] op_sel_hi:[1,0,1]\n\t"
        "v_pk_fma_f32 v[118:119], v[118:119], v[224:225], v[62:63] op_sel_hi:[1,0,1]\n\t"
        "v_exp_f32 v208, v112\n\t"
        "v_exp_f32 v209, v113\n\t"
        "v_exp_f32 v210, v114\n\t"
        "v_exp_f32 v211, v115\n\t"
        "v_exp_f32 v212, v116\n\t"
        "v_exp_f32 v213, v117\n\t"
        "v_exp_f32 v214, v118\n\t"
        "v_exp_f32 v215, v119\n\t"
        "s_add_u32 s62, s62, 4096\n\t"
        "buffer_load_dwordx4 v[8:11], v231, s[16:19], s62 offen offset:0 sc1\n\t"
        "buffer_load_dwordx4 v[12:15], v231, s[16:19], s62 offen offset:2048 sc1\n\t"
        "v_pk_add_f32 v[208:209], v[208:209], 1.0 op_sel_hi:[1,0]\n\t"
        "v_pk_add_f32 v[210:211], v[210:211], 1.0 op_sel_hi:[1,0]\n\t"
        "v_pk_add_f32 v[212:213], v[212:213], 1.0 op_sel_hi:[1,0]\n\t"
        "v_pk_add_f32 v[214:215], v[214:215], 1.0 op_sel_hi:[1,0]\n\t"
        "v_pk_fma_f32 v[208:209], v[208:209], v[208:209], 1.0 op_sel_hi:[1,1,0]\n\t"
        "v_pk_fma_f32 v[210:211], v[210:211], v[210:211], 1.0 op_sel_hi:[1,1,0]\n\t"
        "v_pk_fma_f32 v[212:213], v[212:213], v[212:213], 1.0 op_sel_hi:[1,1,0]\n\t"
        "v_pk_fma_f32 v[214:215], v[214:215], v[214:215], 1.0 op_sel_hi:[1,1,0]\n\t"
        "v_rcp_f32 v208, v208\n\t"
        "v_rcp_f32 v209, v209\n\t"
        "v_rcp_f32 v210, v210\n\t"
        "v_rcp_f32 v211, v211\n\t"
        "v_rcp_f32 v212, v212\n\t"
        "v_rcp_f32 v213, v213\n\t"
        "v_rcp_f32 v214, v214\n\t"
        "v_rcp_f32 v215, v215\n\t"
        "s_add_u32 s62, s62, 4096\n\t"
        "buffer_load_dwordx4 v[16:19], v231, s[16:19], s62 offen offset:0 sc1\n\t"
        "buffer_load_dwordx4 v[20:23], v231, s[16:19], s62 offen offset:2048 sc1\n\t"
        "v_pk_fma_f32 v[208:209], v[208:209], v[226:227], v[226:227] op_sel:[0,0,1] op_sel_hi:[1,0,1]\n\t"
        "v_pk_fma_f32 v[210:211], v[210:211], v[226:227], v[226:227] op_sel:[0,0,1] op_sel_hi:[1,0,1]\n\t"
        "v_pk_fma_f32 v[212:213], v[212:213], v[226:227], v[226:227] op_sel:[0,0,1] op_sel_hi:[1,0,1]\n\t"
        "v_pk_fma_f32 v[214:215], v[214:215], v[226:227], v[226:227] op_sel:[0,0,1] op_sel_hi:[1,0,1]\n\t"
        "v_pk_mul_f32 v[112:113], v[112:113], v[208:209]\n\t"
        "v_pk_mul_f32 v[114:115], v[114:115], v[210:211]\n\t"
        "v_pk_mul_f32 v[116:117], v[116:117], v[212:213]\n\t"
        "v_pk_mul_f32 v[118:119], v[118:119], v[214:215]\n\t"
        "v_cvt_pk_bf16_f32 v56, v112, v113\n\t"
        "v_cvt_pk_bf16_f32 v57, v114, v115\n\t"
        "v_cvt_pk_bf16_f32 v58, v116, v117\n\t"
        "v_cvt_pk_bf16_f32 v59, v118, v119\n\t"
        "buffer_store_dwordx4 v[56:59], v231, s[16:19], s47 offen offset:0 sc1\n\t"
        "ds_read_b128 v[104:107], v230 offset:768\n\t"
        "ds_read_b128 v[108:111], v230 offset:832\n\t"
        "ds_read_b128 v[192:195], v230 offset:896\n\t"
        "ds_read_b128 v[196:199], v230 offset:960\n\t"
        "s_waitcnt lgkmcnt(9)\n\t"
        "v_pk_fma_f32 v[72:73], v[72:73], v[224:225], v[80:81] op_sel:[0,1,0] op_sel_hi:[1,1,1]\n\t"
        "v_pk_fma_f32 v[74:75], v[74:75], v[224:225], v[82:83] op_sel:[0,1,0] op_sel_hi:[1,1,1]\n\t"
        "s_waitcnt lgkmcnt(8)\n\t"
        "v_pk_fma_f32 v[76:77], v[76:77], v[224:225], v[84:85] op_sel:[0,1,0] op_sel_hi:[1,1,1]\n\t"
        "v_pk_fma_f32 v[78:79], v[78:79], v[224:225], v[86:87] op_sel:[0,1,0] op_sel_hi:[1,1,1]\n\t"
        "v_pk_fma_f32 v[120:121], v[120:121], v[224:225], v[72:73] op_sel_hi:[1,0,1]\n\t"
        "v_pk_fma_f32 v[122:123], v[122:123], v[224:225], v[74:75] op_sel_hi:[1,0,1]\n\t"
        "v_pk_fma_f32 v[124:125], v[124:125], v[224:225], v[76:77] op_sel_hi:[1,0,1]\n\t"
        "v_pk_fma_f32 v[126:127], v[126:127], v[224:225], v[78:79] op_sel_hi:[1,0,1]\n\t"
        "v_exp_f32 v208, v120\n\t"
        "v_exp_f32 v209, v121\n\t"
        "v_exp_f32 v210, v122\n\t"
        "v_exp_f32 v211, v123\n\t"
        "v_exp_f32 v212, v124\n\t"
        "v_exp_f32 v213, v125\n\t"
        "v_exp_f32 v214, v126\n\t"
        "v_exp_f32 v215, v127\n\t"
        "s_add_u32 s62, s62, 4096\n\t"
        "buffer_load_dwordx4 v[24:27], v231, s[16:19], s62 offen offset:0 sc1\n\t"
        "buffer_load_dwordx4 v[28:31], v231, s[16:19], s62 offen offset:2048 sc1\n\t"
        "v_pk_add_f32 v[208:209], v[208:209], 1.0 op_sel_hi:[1,0]\n\t"
        "v_pk_add_f32 v[210:211], v[210:211], 1.0 op_sel_hi:[1,0]\n\t"
        "v_pk_add_f32 v[212:213], v[212:213], 1.0 op_sel_hi:[1,0]\n\t"
        "v_pk_add_f32 v[214:215], v[214:215], 1.0 op_sel_hi:[1,0]\n\t"
        "v_pk_fma_f32 v[208:209], v[208:209], v[208:209], 1.0 op_sel_hi:[1,1,0]\n\t"
        "v_pk_fma_f32 v[210:211], v[210:211], v[210:211], 1.0 op_sel_hi:[1,1,0]\n\t"
        "v_pk_fma_f32 v[212:213], v[212:213], v[212:213], 1.0 op_sel_hi:[1,1,0]\n\t"
        "v_pk_fma_f32 v[214:215], v[214:215], v[214:215], 1.0 op_sel_hi:[1,1,0]\n\t"
        "v_rcp_f32 v208, v208\n\t"
        "v_rcp_f32 v209, v209\n\t"
        "v_rcp_f32 v210, v210\n\t"
        "v_rcp_f32 v211, v211\n\t"
        "v_rcp_f32 v212, v212\n\t"
        "v_rcp_f32 v213, v213\n\t"
        "v_rcp_f32 v214, v214\n\t"
        "v_rcp_f32 v215, v215\n\t"
        "s_add_u32 s62, s62, 4096\n\t"
        "buffer_load_dwordx4 v[32:35], v231, s[16:19], s62 offen offset:0 sc1\n\t"
        "buffer_load_dwordx4 v[36:39], v231, s[16:19], s62 offen offset:2048 sc1\n\t"
        "v_pk_fma_f32 v[208:209], v[208:209], v[226:227], v[226:227] op_sel:[0,0,1] op_sel_hi:[1,0,1]\n\t"
        "v_pk_fma_f32 v[210:211], v[210:211], v[226:227], v[226:227] op_sel:[0,0,1] op_sel_hi:[1,0,1]\n\t"
        "v_pk_fma_f32 v[212:213], v[212:213], v[226:227], v[226:227] op_sel:[0,0,1] op_sel_hi:[1,0,1]\n\t"
        "v_pk_fma_f32 v[214:215], v[214:215], v[226:227], v[226:227] op_sel:[0,0,1] op_sel_hi:[1,0,1]\n\t"
        "v_pk_mul_f32 v[120:121], v[120:121], v[208:209]\n\t"
        "v_pk_mul_f32 v[122:123], v[122:123], v[210:211]\n\t"
        "v_pk_mul_f32 v[124:125], v[124:125], v[212:213]\n\t"
        "v_pk_mul_f32 v[126:127], v[126:127], v[214:215]\n\t"
        "v_cvt_pk_bf16_f32 v72, v120, v121\n\t"
        "v_cvt_pk_bf16_f32 v73, v122, v123\n\t"
        "v_cvt_pk_bf16_f32 v74, v124, v125\n\t"
        "v_cvt_pk_bf16_f32 v75, v126, v127\n\t"
        "buffer_store_dwordx4 v[72:75], v231, s[16:19], s47 offen offset:2048 sc1\n\t"
        "s_waitcnt lgkmcnt(5)\n\t"
        "v_pk_fma_f32 v[88:89], v[88:89], v[224:225], v[96:97] op_sel:[0,1,0] op_sel_hi:[1,1,1]\n\t"
        "v_pk_fma_f32 v[90:91], v[90:91], v[224:225], v[98:99] op_sel:[0,1,0] op_sel_hi:[1,1,1]\n\t"
        "s_waitcnt lgkmcnt(4)\n\t"
        "v_pk_fma_f32 v[92:93], v[92:93], v[224:225], v[100:101] op_sel:[0,1,0] op_sel_hi:[1,1,1]\n\t"
        "v_pk_fma_f32 v[94:95], v[94:95], v[224:225], v[102:103] op_sel:[0,1,0] op_sel_hi:[1,1,1]\n\t"
        "v_pk_fma_f32 v[128:129], v[128:129], v[224:225], v[88:89] op_sel_hi:[1,0,1]\n\t"
        "v_pk_fma_f32 v[130:131], v[130:131], v[224:225], v[90:91] op_sel_hi:[1,0,1]\n\t"
        "v_pk_fma_f32 v[132:133], v[132:133], v[224:225], v[92:93] op_sel_hi:[1,0,1]\n\t"
        "v_pk_fma_f32 v[134:135], v[134:135], v[224:225], v[94:95] op_sel_hi:[1,0,1]\n\t"
        "v_exp_f32 v208, v128\n\t"
        "v_exp_f32 v209, v129\n\t"
        "v_exp_f32 v210, v130\n\t"
        "v_exp_f32 v211, v131\n\t"
        "v_exp_f32 v212, v132\n\t"
        "v_exp_f32 v213, v133\n\t"
        "v_exp_f32 v214, v134\n\t"
        "v_exp_f32 v215, v135\n\t"
        "s_add_u32 s62, s62, 4096\n\t"
        "buffer_load_dwordx4 v[40:43], v231, s[16:19], s62 offen offset:0 sc1\n\t"
        "buffer_load_dwordx4 v[44:47], v231, s[16:19], s62 offen offset:2048 sc1\n\t"
        "v_pk_add_f32 v[208:209], v[208:209], 1.0 op_sel_hi:[1,0]\n\t"
        "v_pk_add_f32 v[210:211], v[210:211], 1.0 op_sel_hi:[1,0]\n\t"
        "v_pk_add_f32 v[212:213], v[212:213], 1.0 op_sel_hi:[1,0]\n\t"
        "v_pk_add_f32 v[214:215], v[214:215], 1.0 op_sel_hi:[1,0]\n\t"
        "v_pk_fma_f32 v[208:209], v[208:209], v[208:209], 1.0 op_sel_hi:[1,1,0]\n\t"
        "v_pk_fma_f32 v[210:211], v[210:211], v[210:211], 1.0 op_sel_hi:[1,1,0]\n\t"
        "v_pk_fma_f32 v[212:213], v[212:213], v[212:213], 1.0 op_sel_hi:[1,1,0]\n\t"
        "v_pk_fma_f32 v[214:215], v[214:215], v[214:215], 1.0 op_sel_hi:[1,1,0]\n\t"
        "v_rcp_f32 v208, v208\n\t"
        "v_rcp_f32 v209, v209\n\t"
        "v_rcp_f32 v210, v210\n\t"
        "v_rcp_f32 v211, v211\n\t"
        "v_rcp_f32 v212, v212\n\t"
        "v_rcp_f32 v213, v213\n\t"
        "v_rcp_f32 v214, v214\n\t"
        "v_rcp_f32 v215, v215\n\t"
        "s_add_u32 s62, s62, 4096\n\t"
        "buffer_load_dwordx4 v[48:51], v231, s[16:19], s62 offen offset:0 sc1\n\t"
        "buffer_load_dwordx4 v[52:55], v231, s[16:19], s62 offen offset:2048 sc1\n\t"
        "v_pk_fma_f32 v[208:209], v[208:209], v[226:227], v[226:227] op_sel:[0,0,1] op_sel_hi:[1,0,1]\n\t"
        "v_pk_fma_f32 v[210:211], v[210:211], v[226:227], v[226:227] op_sel:[0,0,1] op_sel_hi:[1,0,1]\n\t"
        "v_pk_fma_f32 v[212:213], v[212:213], v[226:227], v[226:227] op_sel:[0,0,1] op_sel_hi:[1,0,1]\n\t"
        "v_pk_fma_f32 v[214:215], v[214:215], v[226:227], v[226:227] op_sel:[0,0,1] op_sel_hi:[1,0,1]\n\t"
        "v_pk_mul_f32 v[128:129], v[128:129], v[208:209]\n\t"
        "v_pk_mul_f32 v[130:131], v[130:131], v[210:211]\n\t"
        "v_pk_mul_f32 v[132:133], v[132:133], v[212:213]\n\t"
        "v_pk_mul_f32 v[134:135], v[134:135], v[214:215]\n\t"
        "v_cvt_pk_bf16_f32 v88, v128, v129\n\t"
        "v_cvt_pk_bf16_f32 v89, v130, v131\n\t"
        "v_cvt_pk_bf16_f32 v90, v132, v133\n\t"
        "v_cvt_pk_bf16_f32 v91, v134, v135\n\t"
        "s_add_u32 s47, s47, 4096\n\t"
        "buffer_store_dwordx4 v[88:91], v231, s[16:19], s47 offen offset:0 sc1\n\t"
        "s_waitcnt lgkmcnt(1)\n\t"
        "v_pk_fma_f32 v[104:105], v[104:105], v[224:225], v[192:193] op_sel:[0,1,0] op_sel_hi:[1,1,1]\n\t"
        "v_pk_fma_f32 v[106:107], v[106:107], v[224:225], v[194:195] op_sel:[0,1,0] op_sel_hi:[1,1,1]\n\t"
        "s_waitcnt lgkmcnt(0)\n\t"
        "v_pk_fma_f32 v[108:109], v[108:109], v[224:225], v[196:197] op_sel:[0,1,0] op_sel_hi:[1,1,1]\n\t"
        "v_pk_fma_f32 v[110:111], v[110:111], v[224:225], v[198:199] op_sel:[0,1,0] op_sel_hi:[1,1,1]\n\t"
        "v_pk_fma_f32 v[136:137], v[136:137], v[224:225], v[104:105] op_sel_hi:[1,0,1]\n\t"
        "v_pk_fma_f32 v[138:139], v[138:139], v[224:225], v[106:107] op_sel_hi:[1,0,1]\n\t"
        "v_pk_fma_f32 v[140:141], v[140:141], v[224:225], v[108:109] op_sel_hi:[1,0,1]\n\t"
        "v_pk_fma_f32 v[142:143], v[142:143], v[224:225], v[110:111] op_sel_hi:[1,0,1]\n\t"
        "v_exp_f32 v208, v136\n\t"
        "v_exp_f32 v209, v137\n\t"
        "v_exp_f32 v210, v138\n\t"
        "v_exp_f32 v211, v139\n\t"
        "v_exp_f32 v212, v140\n\t"
        "v_exp_f32 v213, v141\n\t"
        "v_exp_f32 v214, v142\n\t"
        "v_exp_f32 v215, v143\n\t"
        "v_pk_add_f32 v[208:209], v[208:209], 1.0 op_sel_hi:[1,0]\n\t"
        "v_pk_add_f32 v[210:211], v[210:211], 1.0 op_sel_hi:[1,0]\n\t"
        "v_pk_add_f32 v[212:213], v[212:213], 1.0 op_sel_hi:[1,0]\n\t"
        "v_pk_add_f32 v[214:215], v[214:215], 1.0 op_sel_hi:[1,0]\n\t"
        "v_pk_fma_f32 v[208:209], v[208:209], v[208:209], 1.0 op_sel_hi:[1,1,0]\n\t"
        "v_pk_fma_f32 v[210:211], v[210:211], v[210:211], 1.0 op_sel_hi:[1,1,0]\n\t"
        "v_pk_fma_f32 v[212:213], v[212:213], v[212:213], 1.0 op_sel_hi:[1,1,0]\n\t"
        "v_pk_fma_f32 v[214:215], v[214:215], v[214:215], 1.0 op_sel_hi:[1,1,0]\n\t"
        "v_rcp_f32 v208, v208\n\t"
        "v_rcp_f32 v209, v209\n\t"
        "v_rcp_f32 v210, v210\n\t"
        "v_rcp_f32 v211, v211\n\t"
        "v_rcp_f32 v212, v212\n\t"
        "v_rcp_f32 v213, v213\n\t"
        "v_rcp_f32 v214, v214\n\t"
        "v_rcp_f32 v215, v215\n\t"
        "v_pk_fma_f32 v[208:209], v[208:209], v[226:227], v[226:227] op_sel:[0,0,1] op_sel_hi:[1,0,1]\n\t"
        "v_pk_fma_f32 v[210:211], v[210:211], v[226:227], v[226:227] op_sel:[0,0,1] op_sel_hi:[1,0,1]\n\t"
        "v_pk_fma_f32 v[212:213], v[212:213], v[226:227], v[226:227] op_sel:[0,0,1] op_sel_hi:[1,0,1]\n\t"
        "v_pk_fma_f32 v[214:215], v[214:215], v[226:227], v[226:227] op_sel:[0,0,1] op_sel_hi:[1,0,1]\n\t"
        "v_pk_mul_f32 v[136:137], v[136:137], v[208:209]\n\t"
        "v_pk_mul_f32 v[138:139], v[138:139], v[210:211]\n\t"
        "v_pk_mul_f32 v[140:141], v[140:141], v[212:213]\n\t"
        "v_pk_mul_f32 v[142:143], v[142:143], v[214:215]\n\t"
        "v_cvt_pk_bf16_f32 v104, v136, v137\n\t"
        "v_cvt_pk_bf16_f32 v105, v138, v139\n\t"
        "v_cvt_pk_bf16_f32 v106, v140, v141\n\t"
        "v_cvt_pk_bf16_f32 v107, v142, v143\n\t"
        "buffer_store_dwordx4 v[104:107], v231, s[16:19], s47 offen offset:2048 sc1\n\t"
        "ds_read_b128 v[144:147], v228 offset:0\n\t"
        "ds_read_b128 v[148:151], v228 offset:256\n\t"
        "ds_read_b128 v[152:155], v228 offset:28672\n\t"
        "ds_read_b128 v[156:159], v228 offset:28928\n\t"
        "ds_read_b128 v[160:163], v229 offset:0\n\t"
        "ds_read_b128 v[164:167], v229 offset:256\n\t"
        "ds_read_b128 v[168:171], v229 offset:28672\n\t"
        "ds_read_b128 v[172:175], v229 offset:28928\n\t"
        "ds_read_b128 v[176:179], v228 offset:2048\n\t"
        "ds_read_b128 v[180:183], v228 offset:2304\n\t"
        "ds_read_b128 v[184:187], v228 offset:30720\n\t"
        "s_waitcnt lgkmcnt(7)\n\t"
        "s_waitcnt vmcnt(17)\n\t"
        "v_mfma_f32_16x16x32_bf16 v[112:115], v[144:147], v[0:3], 0\n\t"
        "ds_read_b128 v[188:191], v228 offset:30976\n\t"
        "s_add_u32 s33, s30, s28\n\t"
        "s_add_u32 s33, s33, 12531712\n\t"
        "buffer_load_dwordx2 v[56:57], v245, s[16:19], s33 offen offset:0 sc1\n\t"
        "v_mfma_f32_16x16x32_bf16 v[116:119], v[148:151], v[0:3], 0\n\t"
        "ds_read_b128 v[144:147], v229 offset:2048\n\t"
        "v_mfma_f32_16x16x32_bf16 v[120:123], v[152:155], v[0:3], 0\n\t"
        "ds_read_b128 v[148:151], v229 offset:2304\n\t"
        "v_mfma_f32_16x16x32_bf16 v[124:127], v[156:159], v[0:3], 0\n\t"
        "ds_read_b128 v[152:155], v229 offset:30720\n\t"
        "s_waitcnt lgkmcnt(7)\n\t"
        "v_mfma_f32_16x16x32_bf16 v[128:131], v[160:163], v[0:3], 0\n\t"
        "ds_read_b128 v[156:159], v229 offset:30976\n\t"
        "buffer_load_dwordx2 v[58:59], v245, s[16:19], s33 offen offset:256 sc1\n\t"
        "v_mfma_f32_16x16x32_bf16 v[132:135], v[164:167], v[0:3], 0\n\t"
        "ds_read_b128 v[160:163], v228 offset:4096\n\t"
        "v_mfma_f32_16x16x32_bf16 v[136:139], v[168:171], v[0:3], 0\n\t"
        "ds_read_b128 v[164:167], v228 offset:4352\n\t"
        "v_mfma_f32_16x16x32_bf16 v[140:143], v[172:175], v[0:3], 0\n\t"
        "ds_read_b128 v[168:171], v228 offset:32768\n\t"
        "s_waitcnt lgkmcnt(7)\n\t"
        "s_waitcnt vmcnt(18)\n\t"
        "v_mfma_f32_16x16x32_bf16 v[112:115], v[176:179], v[4:7], v[112:115]\n\t"
        "ds_read_b128 v[172:175], v228 offset:33024\n\t"
        "buffer_load_dwordx2 v[60:61], v245, s[16:19], s33 offen offset:512 sc1\n\t"
        "v_mfma_f32_16x16x32_bf16 v[116:119], v[180:183], v[4:7], v[116:119]\n\t"
        "ds_read_b128 v[176:179], v229 offset:4096\n\t"
        "v_mfma_f32_16x16x32_bf16 v[120:123], v[184:187], v[4:7], v[120:123]\n\t"
        "ds_read_b128 v[180:183], v229 offset:4352\n\t"
        "v_mfma_f32_16x16x32_bf16 v[124:127], v[188:191], v[4:7], v[124:127]\n\t"
        "ds_read_b128 v[184:187], v229 offset:32768\n\t"
        "s_waitcnt lgkmcnt(7)\n\t"
        "v_mfma_f32_16x16x32_bf16 v[128:131], v[144:147], v[4:7], v[128:131]\n\t"
        "ds_read_b128 v[188:191], v229 offset:33024\n\t"
        "buffer_load_dwordx2 v[62:63], v245, s[16:19], s33 offen offset:768 sc1\n\t"
        "v_mfma_f32_16x16x32_bf16 v[132:135], v[148:151], v[4:7], v[132:135]\n\t"
        "ds_read_b128 v[144:147], v228 offset:6144\n\t"
        "v_mfma_f32_16x16x32_bf16 v[136:139], v[152:155], v[4:7], v[136:139]\n\t"
        "ds_read_b128 v[148:151], v228 offset:6400\n\t"
        "v_mfma_f32_16x16x32_bf16 v[140:143], v[156:159], v[4:7], v[140:143]\n\t"
        "ds_read_b128 v[152:155], v228 offset:34816\n\t"
        "s_waitcnt lgkmcnt(7)\n\t"
        "s_waitcnt vmcnt(19)\n\t"
        "v_mfma_f32_16x16x32_bf16 v[112:115], v[160:163], v[8:11], v[112:115]\n\t"
        "ds_read_b128 v[156:159], v228 offset:35072\n\t"
        "buffer_load_dwordx2 v[64:65], v245, s[16:19], s33 offen offset:1024 sc1\n\t"
        "v_mfma_f32_16x16x32_bf16 v[116:119], v[164:167], v[8:11], v[116:119]\n\t"
        "ds_read_b128 v[160:163], v229 offset:6144\n\t"
        "v_mfma_f32_16x16x32_bf16 v[120:123], v[168:171], v[8:11], v[120:123]\n\t"
        "ds_read_b128 v[164:167], v229 offset:6400\n\t"
        "v_mfma_f32_16x16x32_bf16 v[124:127], v[172:175], v[8:11], v[124:127]\n\t"
        "ds_read_b128 v[168:171], v229 offset:34816\n\t"
        "s_waitcnt lgkmcnt(7)\n\t"
        "v_mfma_f32_16x16x32_bf16 v[128:131], v[176:179], v[8:11], v[128:131]\n\t"
        "ds_read_b128 v[172:175], v229 offset:35072\n\t"
        "buffer_load_dwordx2 v[66:67], v245, s[16:19], s33 offen offset:1280 sc1\n\t"
        "v_mfma_f32_16x16x32_bf16 v[132:135], v[180:183], v[8:11], v[132:135]\n\t"
        "ds_read_b128 v[176:179], v228 offset:8192\n\t"
        "v_mfma_f32_16x16x32_bf16 v[136:139], v[184:187], v[8:11], v[136:139]\n\t"
        "ds_read_b128 v[180:183], v228 offset:8448\n\t"
        "v_mfma_f32_16x16x32_bf16 v[140:143], v[188:191], v[8:11], v[140:143]\n\t"
        "ds_read_b128 v[184:187], v228 offset:36864\n\t"
        "s_waitcnt lgkmcnt(7)\n\t"
        "s_waitcnt vmcnt(20)\n\t"
        "v_mfma_f32_16x16x32_bf16 v[112:115], v[144:147], v[12:15], v[112:115]\n\t"
        "ds_read_b128 v[188:191], v228 offset:37120\n\t"
        "buffer_load_dwordx2 v[68:69], v245, s[16:19], s33 offen offset:1536 sc1\n\t"
        "v_mfma_f32_16x16x32_bf16 v[116:119], v[148:151], v[12:15], v[116:119]\n\t"
        "ds_read_b128 v[144:147], v229 offset:8192\n\t"
        "v_mfma_f32_16x16x32_bf16 v[120:123], v[152:155], v[12:15], v[120:123]\n\t"
        "ds_read_b128 v[148:151], v229 offset:8448\n\t"
        "v_mfma_f32_16x16x32_bf16 v[124:127], v[156:159], v[12:15], v[124:127]\n\t"
        "ds_read_b128 v[152:155], v229 offset:36864\n\t"
        "s_waitcnt lgkmcnt(7)\n\t"
        "v_mfma_f32_16x16x32_bf16 v[128:131], v[160:163], v[12:15], v[128:131]\n\t"
        "ds_read_b128 v[156:159], v229 offset:37120\n\t"
        "v_mfma_f32_16x16x32_bf16 v[132:135], v[164:167], v[12:15], v[132:135]\n\t"
        "ds_read_b128 v[160:163], v228 offset:10240\n\t"
        "v_mfma_f32_16x16x32_bf16 v[136:139], v[168:171], v[12:15], v[136:139]\n\t"
        "ds_read_b128 v[164:167], v228 offset:10496\n\t"
        "v_mfma_f32_16x16x32_bf16 v[140:143], v[172:175], v[12:15], v[140:143]\n\t"
        "ds_read_b128 v[168:171], v228 offset:38912\n\t"
        "s_waitcnt lgkmcnt(7)\n\t"
        "s_waitcnt vmcnt(20)\n\t"
        "v_mfma_f32_16x16x32_bf16 v[112:115], v[176:179], v[16:19], v[112:115]\n\t"
        "ds_read_b128 v[172:175], v228 offset:39168\n\t"
        "v_mfma_f32_16x16x32_bf16 v[116:119], v[180:183], v[16:19], v[116:119]\n\t"
        "ds_read_b128 v[176:179], v229 offset:10240\n\t"
        "v_mfma_f32_16x16x32_bf16 v[120:123], v[184:187], v[16:19], v[120:123]\n\t"
        "ds_read_b128 v[180:183], v229 offset:10496\n\t"
        "v_mfma_f32_16x16x32_bf16 v[124:127], v[188:191], v[16:19], v[124:127]\n\t"
        "ds_read_b128 v[184:187], v229 offset:38912\n\t"
        "s_waitcnt lgkmcnt(7)\n\t"
        "v_mfma_f32_16x16x32_bf16 v[128:131], v[144:147], v[16:19], v[128:131]\n\t"
        "ds_read_b128 v[188:191], v229 offset:39168\n\t"
        "v_mfma_f32_16x16x32_bf16 v[132:135], v[148:151], v[16:19], v[132:135]\n\t"
        "ds_read_b128 v[144:147], v228 offset:12288\n\t"
        "v_mfma_f32_16x16x32_bf16 v[136:139], v[152:155], v[16:19], v[136:139]\n\t"
        "ds_read_b128 v[148:151], v228 offset:12544\n\t"
        "v_mfma_f32_16x16x32_bf16 v[140:143], v[156:159], v[16:19], v[140:143]\n\t"
        "ds_read_b128 v[152:155], v228 offset:40960\n\t"
        "s_mul_i32 s38, s27, 147456\n\t"
        "s_lshl_b32 s39, s24, 7\n\t"
        "s_lshl_b32 s40, s25, 3\n\t"
        "s_add_u32 s38, s38, s39\n\t"
        "s_add_u32 s38, s38, s40\n\t"
        "s_add_u32 s38, s38, 143360\n\t"
        "s_waitcnt vmcnt(7)\n\t"
        "s_barrier\n\t"
        "s_cmp_lg_u32 s26, 0\n\t"
        "s_cbranch_scc1 sig_68_%=\n\t"
        "s_mov_b64 exec, 1\n\t"
        "buffer_atomic_swap_x2 v[234:235], off, s[16:19], s38\n\t"
        "s_mov_b64 exec, -1\n\t"
        "sig_68_%=:\n\t"
        "s_waitcnt lgkmcnt(7)\n\t"
        "v_mfma_f32_16x16x32_bf16 v[112:115], v[160:163], v[20:23], v[112:115]\n\t"
        "ds_read_b128 v[156:159], v228 offset:41216\n\t"
        "v_mfma_f32_16x16x32_bf16 v[116:119], v[164:167], v[20:23], v[116:119]\n\t"
        "ds_read_b128 v[160:163], v229 offset:12288\n\t"
        "v_mfma_f32_16x16x32_bf16 v[120:123], v[168:171], v[20:23], v[120:123]\n\t"
        "ds_read_b128 v[164:167], v229 offset:12544\n\t"
        "v_mfma_f32_16x16x32_bf16 v[124:127], v[172:175], v[20:23], v[124:127]\n\t"
        "ds_read_b128 v[168:171], v229 offset:40960\n\t"
        "s_waitcnt lgkmcnt(7)\n\t"
        "v_mfma_f32_16x16x32_bf16 v[128:131], v[176:179], v[20:23], v[128:131]\n\t"
        "ds_read_b128 v[172:175], v229 offset:41216\n\t"
        "v_mfma_f32_16x16x32_bf16 v[132:135], v[180:183], v[20:23], v[132:135]\n\t"
        "ds_read_b128 v[176:179], v228 offset:14336\n\t"
        "v_mfma_f32_16x16x32_bf16 v[136:139], v[184:187], v[20:23], v[136:139]\n\t"
        "ds_read_b128 v[180:183], v228 offset:14592\n\t"
        "v_mfma_f32_16x16x32_bf16 v[140:143], v[188:191], v[20:23], v[140:143]\n\t"
        "ds_read_b128 v[184:187], v228 offset:43008\n\t"
        "s_waitcnt lgkmcnt(7)\n\t"
        "v_mfma_f32_16x16x32_bf16 v[112:115], v[144:147], v[24:27], v[112:115]\n\t"
        "ds_read_b128 v[188:191], v228 offset:43264\n\t"
        "v_mfma_f32_16x16x32_bf16 v[116:119], v[148:151], v[24:27], v[116:119]\n\t"
        "ds_read_b128 v[144:147], v229 offset:14336\n\t"
        "v_mfma_f32_16x16x32_bf16 v[120:123], v[152:155], v[24:27], v[120:123]\n\t"
        "ds_read_b128 v[148:151], v229 offset:14592\n\t"
        "v_mfma_f32_16x16x32_bf16 v[124:127], v[156:159], v[24:27], v[124:127]\n\t"
        "ds_read_b128 v[152:155], v229 offset:43008\n\t"
        "s_waitcnt lgkmcnt(7)\n\t"
        "v_mfma_f32_16x16x32_bf16 v[128:131], v[160:163], v[24:27], v[128:131]\n\t"
        "ds_read_b128 v[156:159], v229 offset:43264\n\t"
        "v_mfma_f32_16x16x32_bf16 v[132:135], v[164:167], v[24:27], v[132:135]\n\t"
        "ds_read_b128 v[160:163], v228 offset:16384\n\t"
        "v_mfma_f32_16x16x32_bf16 v[136:139], v[168:171], v[24:27], v[136:139]\n\t"
        "ds_read_b128 v[164:167], v228 offset:16640\n\t"
        "v_mfma_f32_16x16x32_bf16 v[140:143], v[172:175], v[24:27], v[140:143]\n\t"
        "ds_read_b128 v[168:171], v228 offset:45056\n\t"
        "s_waitcnt lgkmcnt(7)\n\t"
        "v_mfma_f32_16x16x32_bf16 v[112:115], v[176:179], v[28:31], v[112:115]\n\t"
        "ds_read_b128 v[172:175], v228 offset:45312\n\t"
        "v_mfma_f32_16x16x32_bf16 v[116:119], v[180:183], v[28:31], v[116:119]\n\t"
        "ds_read_b128 v[176:179], v229 offset:16384\n\t"
        "v_mfma_f32_16x16x32_bf16 v[120:123], v[184:187], v[28:31], v[120:123]\n\t"
        "ds_read_b128 v[180:183], v229 offset:16640\n\t"
        "v_mfma_f32_16x16x32_bf16 v[124:127], v[188:191], v[28:31], v[124:127]\n\t"
        "ds_read_b128 v[184:187], v229 offset:45056\n\t"
        "s_waitcnt lgkmcnt(7)\n\t"
        "v_mfma_f32_16x16x32_bf16 v[128:131], v[144:147], v[28:31], v[128:131]\n\t"
        "ds_read_b128 v[188:191], v229 offset:45312\n\t"
        "v_mfma_f32_16x16x32_bf16 v[132:135], v[148:151], v[28:31], v[132:135]\n\t"
        "ds_read_b128 v[144:147], v228 offset:18432\n\t"
        "v_mfma_f32_16x16x32_bf16 v[136:139], v[152:155], v[28:31], v[136:139]\n\t"
        "ds_read_b128 v[148:151], v228 offset:18688\n\t"
        "v_mfma_f32_16x16x32_bf16 v[140:143], v[156:159], v[28:31], v[140:143]\n\t"
        "ds_read_b128 v[152:155], v228 offset:47104\n\t"
        "s_waitcnt lgkmcnt(7)\n\t"
        "v_mfma_f32_16x16x32_bf16 v[112:115], v[160:163], v[32:35], v[112:115]\n\t"
        "ds_read_b128 v[156:159], v228 offset:47360\n\t"
        "v_mfma_f32_16x16x32_bf16 v[116:119], v[164:167], v[32:35], v[116:119]\n\t"
        "ds_read_b128 v[160:163], v229 offset:18432\n\t"
        "v_mfma_f32_16x16x32_bf16 v[120:123], v[168:171], v[32:35], v[120:123]\n\t"
        "ds_read_b128 v[164:167], v229 offset:18688\n\t"
        "v_mfma_f32_16x16x32_bf16 v[124:127], v[172:175], v[32:35], v[124:127]\n\t"
        "ds_read_b128 v[168:171], v229 offset:47104\n\t"
        "s_waitcnt lgkmcnt(7)\n\t"
        "v_mfma_f32_16x16x32_bf16 v[128:131], v[176:179], v[32:35], v[128:131]\n\t"
        "ds_read_b128 v[172:175], v229 offset:47360\n\t"
        "v_mfma_f32_16x16x32_bf16 v[132:135], v[180:183], v[32:35], v[132:135]\n\t"
        "ds_read_b128 v[176:179], v228 offset:20480\n\t"
        "v_mfma_f32_16x16x32_bf16 v[136:139], v[184:187], v[32:35], v[136:139]\n\t"
        "ds_read_b128 v[180:183], v228 offset:20736\n\t"
        "v_mfma_f32_16x16x32_bf16 v[140:143], v[188:191], v[32:35], v[140:143]\n\t"
        "ds_read_b128 v[184:187], v228 offset:49152\n\t"
        "s_waitcnt lgkmcnt(7)\n\t"
        "v_mfma_f32_16x16x32_bf16 v[112:115], v[144:147], v[36:39], v[112:115]\n\t"
        "ds_read_b128 v[188:191], v228 offset:49408\n\t"
        "v_mfma_f32_16x16x32_bf16 v[116:119], v[148:151], v[36:39], v[116:119]\n\t"
        "ds_read_b128 v[144:147], v229 offset:20480\n\t"
        "v_mfma_f32_16x16x32_bf16 v[120:123], v[152:155], v[36:39], v[120:123]\n\t"
        "ds_read_b128 v[148:151], v229 offset:20736\n\t"
        "v_mfma_f32_16x16x32_bf16 v[124:127], v[156:159], v[36:39], v[124:127]\n\t"
        "ds_read_b128 v[152:155], v229 offset:49152\n\t"
        "s_waitcnt lgkmcnt(7)\n\t"
        "v_mfma_f32_16x16x32_bf16 v[128:131], v[160:163], v[36:39], v[128:131]\n\t"
        "ds_read_b128 v[156:159], v229 offset:49408\n\t"
        "v_mfma_f32_16x16x32_bf16 v[132:135], v[164:167], v[36:39], v[132:135]\n\t"
        "ds_read_b128 v[160:163], v228 offset:22528\n\t"
        "v_mfma_f32_16x16x32_bf16 v[136:139], v[168:171], v[36:39], v[136:139]\n\t"
        "ds_read_b128 v[164:167], v228 offset:22784\n\t"
        "v_mfma_f32_16x16x32_bf16 v[140:143], v[172:175], v[36:39], v[140:143]\n\t"
        "ds_read_b128 v[168:171], v228 offset:51200\n\t"
        "s_waitcnt lgkmcnt(7)\n\t"
        "v_mfma_f32_16x16x32_bf16 v[112:115], v[176:179], v[40:43], v[112:115]\n\t"
        "ds_read_b128 v[172:175], v228 offset:51456\n\t"
        "s_waitcnt vmcnt(5)\n\t"
        "v_add_f32 v56, v56, v58\n\t"
        "v_mfma_f32_16x16x32_bf16 v[116:119], v[180:183], v[40:43], v[116:119]\n\t"
        "ds_read_b128 v[176:179], v229 offset:22528\n\t"
        "v_add_f32 v57, v57, v59\n\t"
        "v_mfma_f32_16x16x32_bf16 v[120:123], v[184:187], v[40:43], v[120:123]\n\t"
        "ds_read_b128 v[180:183], v229 offset:22784\n\t"
        "s_waitcnt vmcnt(3)\n\t"
        "v_add_f32 v60, v60, v62\n\t"
        "v_mfma_f32_16x16x32_bf16 v[124:127], v[188:191], v[40:43], v[124:127]\n\t"
        "ds_read_b128 v[184:187], v229 offset:51200\n\t"
        "v_add_f32 v61, v61, v63\n\t"
        "s_waitcnt lgkmcnt(7)\n\t"
        "v_mfma_f32_16x16x32_bf16 v[128:131], v[144:147], v[40:43], v[128:131]\n\t"
        "ds_read_b128 v[188:191], v229 offset:51456\n\t"
        "s_waitcnt vmcnt(1)\n\t"
        "v_add_f32 v64, v64, v66\n\t"
        "v_mfma_f32_16x16x32_bf16 v[132:135], v[148:151], v[40:43], v[132:135]\n\t"
        "ds_read_b128 v[144:147], v228 offset:24576\n\t"
        "v_add_f32 v65, v65, v67\n\t"
        "v_mfma_f32_16x16x32_bf16 v[136:139], v[152:155], v[40:43], v[136:139]\n\t"
        "ds_read_b128 v[148:151], v228 offset:24832\n\t"
        "s_waitcnt vmcnt(0)\n\t"
        "v_add_f32 v56, v56, v68\n\t"
        "v_mfma_f32_16x16x32_bf16 v[140:143], v[156:159], v[40:43], v[140:143]\n\t"
        "ds_read_b128 v[152:155], v228 offset:53248\n\t"
        "v_add_f32 v57, v57, v69\n\t"
        "s_waitcnt lgkmcnt(7)\n\t"
        "v_mfma_f32_16x16x32_bf16 v[112:115], v[160:163], v[44:47], v[112:115]\n\t"
        "ds_read_b128 v[156:159], v228 offset:53504\n\t"
        "v_add_f32 v60, v60, v64\n\t"
        "v_mfma_f32_16x16x32_bf16 v[116:119], v[164:167], v[44:47], v[116:119]\n\t"
        "ds_read_b128 v[160:163], v229 offset:24576\n\t"
        "v_add_f32 v61, v61, v65\n\t"
        "v_mfma_f32_16x16x32_bf16 v[120:123], v[168:171], v[44:47], v[120:123]\n\t"
        "ds_read_b128 v[164:167], v229 offset:24832\n\t"
        "v_add_f32 v56, v56, v60\n\t"
        "v_mfma_f32_16x16x32_bf16 v[124:127], v[172:175], v[44:47], v[124:127]\n\t"
        "ds_read_b128 v[168:171], v229 offset:53248\n\t"
        "v_add_f32 v57, v57, v61\n\t"
        "s_waitcnt lgkmcnt(7)\n\t"
        "v_mfma_f32_16x16x32_bf16 v[128:131], v[176:179], v[44:47], v[128:131]\n\t"
        "ds_read_b128 v[172:175], v229 offset:53504\n\t"
        "v_mov_b32 v58, v56\n\t"
        "v_mfma_f32_16x16x32_bf16 v[132:135], v[180:183], v[44:47], v[132:135]\n\t"
        "ds_read_b128 v[176:179], v228 offset:26624\n\t"
        "v_mov_b32 v59, v57\n\t"
        "v_mfma_f32_16x16x32_bf16 v[136:139], v[184:187], v[44:47], v[136:139]\n\t"
        "ds_read_b128 v[180:183], v228 offset:26880\n\t"
        "v_permlane32_swap_b32 v56, v58\n\t"
        "v_mfma_f32_16x16x32_bf16 v[140:143], v[188:191], v[44:47], v[140:143]\n\t"
        "ds_read_b128 v[184:187], v228 offset:55296\n\t"
        "s_mul_i32 s9, s27, 147456\n\t"
        "s_add_u32 s9, s9, s14\n\t"
        "s_add_u32 s9, s9, 143360\n\t"
        "buffer_load_dwordx2 v[232:233], v237, s[16:19], s9 offen sc1\n\t"
        "v_permlane32_swap_b32 v57, v59\n\t"
        "s_waitcnt lgkmcnt(7)\n\t"
        "v_mfma_f32_16x16x32_bf16 v[112:115], v[144:147], v[48:51], v[112:115]\n\t"
        "ds_read_b128 v[188:191], v228 offset:55552\n\t"
        "v_add_f32 v56, v56, v58\n\t"
        "v_mfma_f32_16x16x32_bf16 v[116:119], v[148:151], v[48:51], v[116:119]\n\t"
        "ds_read_b128 v[144:147], v229 offset:26624\n\t"
        "v_add_f32 v57, v57, v59\n\t"
        "v_mfma_f32_16x16x32_bf16 v[120:123], v[152:155], v[48:51], v[120:123]\n\t"
        "ds_read_b128 v[148:151], v229 offset:26880\n\t"
        "v_mov_b32 v58, v56\n\t"
        "v_mfma_f32_16x16x32_bf16 v[124:127], v[156:159], v[48:51], v[124:127]\n\t"
        "ds_read_b128 v[152:155], v229 offset:55296\n\t"
        "v_mov_b32 v59, v57\n\t"
        "s_waitcnt lgkmcnt(7)\n\t"
        "v_mfma_f32_16x16x32_bf16 v[128:131], v[160:163], v[48:51], v[128:131]\n\t"
        "ds_read_b128 v[156:159], v229 offset:55552\n\t"
        "v_permlane16_swap_b32 v56, v58\n\t"
        "v_mfma_f32_16x16x32_bf16 v[132:135], v[164:167], v[48:51], v[132:135]\n\t"
        "v_permlane16_swap_b32 v57, v59\n\t"
        "v_mfma_f32_16x16x32_bf16 v[136:139], v[168:171], v[48:51], v[136:139]\n\t"
        "v_add_f32 v56, v56, v58\n\t"
        "v_mfma_f32_16x16x32_bf16 v[140:143], v[172:175], v[48:51], v[140:143]\n\t"
        "v_add_f32 v57, v57, v59\n\t"
        "s_waitcnt lgkmcnt(4)\n\t"
        "v_mfma_f32_16x16x32_bf16 v[112:115], v[176:179], v[52:55], v[112:115]\n\t"
        "v_mul_f32 v56, 0x3b124925, v56\n\t"
        "v_mfma_f32_16x16x32_bf16 v[116:119], v[180:183], v[52:55], v[116:119]\n\t"
        "v_mul_f32 v57, 0x3b124925, v57\n\t"
        "v_mfma_f32_16x16x32_bf16 v[120:123], v[184:187], v[52:55], v[120:123]\n\t"
        "v_fma_f32 v57, -v56, v56, v57\n\t"
        "v_mfma_f32_16x16x32_bf16 v[124:127], v[188:191], v[52:55], v[124:127]\n\t"
        "v_max_f32 v57, 0, v57\n\t"
        "s_waitcnt lgkmcnt(0)\n\t"
        "v_mfma_f32_16x16x32_bf16 v[128:131], v[144:147], v[52:55], v[128:131]\n\t"
        "v_add_f32 v57, 0x3727c5ac, v57\n\t"
        "v_mfma_f32_16x16x32_bf16 v[132:135], v[148:151], v[52:55], v[132:135]\n\t"
        "v_rsq_f32 v224, v57\n\t"
        "v_mfma_f32_16x16x32_bf16 v[136:139], v[152:155], v[52:55], v[136:139]\n\t"
        "v_mul_f32 v224, 0x3fb8aa3b, v224\n\t"
        "v_mfma_f32_16x16x32_bf16 v[140:143], v[156:159], v[52:55], v[140:143]\n\t"
        "v_mul_f32 v225, -v56, v224\n\t"
        "ds_read_b128 v[0:3], v230 offset:0\n\t"
        "ds_read_b128 v[4:7], v230 offset:64\n\t"
        "ds_read_b128 v[8:11], v230 offset:128\n\t"
        "ds_read_b128 v[12:15], v230 offset:192\n\t"
        "ds_read_b128 v[16:19], v230 offset:256\n\t"
        "ds_read_b128 v[20:23], v230 offset:320\n\t"
        "ds_read_b128 v[24:27], v230 offset:384\n\t"
        "ds_read_b128 v[28:31], v230 offset:448\n\t"
        "s_add_u32 s47, s59, s28\n\t"
        "s_add_u32 s47, s47, 13205504\n\t"
        "s_mov_b32 s82, s47\n\t"
        "s_add_u32 s83, s47, 4096\n\t"
        "s_waitcnt vmcnt(0)\n\t"
        "v_cmp_ne_u64 vcc, s[10:11], v[232:233]\n\t"
        "s_nop 0\n\t"
        "s_cbranch_vccnz sp_69_%=\n\t"
        "bk_70_%=:\n\t"
        "s_cmp_lg_u32 s27, 0\n\t"
        "s_cbranch_scc1 l0_71_%=\n\t"
        "s_add_u32 s9, s12, 303136\n\t"
        "v_min_u32 v236, 16, v238\n\t"
        "buffer_load_dwordx2 v[232:233], v236, s[16:19], s9 offen sc1\n\t"
        "l0_71_%=:\n\t"
        "s_add_u32 s62, s61, s28\n\t"
        "s_add_u32 s62, s62, 12976128\n\t"
        "buffer_load_dwordx4 v[56:59], v247, s[16:19], s62 offen offset:0 sc1\n\t"
        "buffer_load_dwordx4 v[60:63], v247, s[16:19], s62 offen offset:1024 sc1\n\t"
        "ds_read_b128 v[32:35], v230 offset:512\n\t"
        "ds_read_b128 v[36:39], v230 offset:576\n\t"
        "ds_read_b128 v[40:43], v230 offset:640\n\t"
        "ds_read_b128 v[44:47], v230 offset:704\n\t"
        "s_waitcnt lgkmcnt(9)\n\t"
        "v_pk_fma_f32 v[0:1], v[0:1], v[224:225], v[8:9] op_sel:[0,1,0] op_sel_hi:[1,1,1]\n\t"
        "v_pk_fma_f32 v[2:3], v[2:3], v[224:225], v[10:11] op_sel:[0,1,0] op_sel_hi:[1,1,1]\n\t"
        "s_waitcnt lgkmcnt(8)\n\t"
        "v_pk_fma_f32 v[4:5], v[4:5], v[224:225], v[12:13] op_sel:[0,1,0] op_sel_hi:[1,1,1]\n\t"
        "v_pk_fma_f32 v[6:7], v[6:7], v[224:225], v[14:15] op_sel:[0,1,0] op_sel_hi:[1,1,1]\n\t"
        "v_pk_fma_f32 v[112:113], v[112:113], v[224:225], v[0:1] op_sel_hi:[1,0,1]\n\t"
        "v_pk_fma_f32 v[114:115], v[114:115], v[224:225], v[2:3] op_sel_hi:[1,0,1]\n\t"
        "v_pk_fma_f32 v[116:117], v[116:117], v[224:225], v[4:5] op_sel_hi:[1,0,1]\n\t"
        "v_pk_fma_f32 v[118:119], v[118:119], v[224:225], v[6:7] op_sel_hi:[1,0,1]\n\t"
        "v_exp_f32 v208, v112\n\t"
        "v_exp_f32 v209, v113\n\t"
        "v_exp_f32 v210, v114\n\t"
        "v_exp_f32 v211, v115\n\t"
        "v_exp_f32 v212, v116\n\t"
        "v_exp_f32 v213, v117\n\t"
        "v_exp_f32 v214, v118\n\t"
        "v_exp_f32 v215, v119\n\t"
        "buffer_load_dwordx4 v[64:67], v247, s[16:19], s62 offen offset:2048 sc1\n\t"
        "buffer_load_dwordx4 v[68:71], v247, s[16:19], s62 offen offset:3072 sc1\n\t"
        "v_pk_add_f32 v[208:209], v[208:209], 1.0 op_sel_hi:[1,0]\n\t"
        "v_pk_add_f32 v[210:211], v[210:211], 1.0 op_sel_hi:[1,0]\n\t"
        "v_pk_add_f32 v[212:213], v[212:213], 1.0 op_sel_hi:[1,0]\n\t"
        "v_pk_add_f32 v[214:215], v[214:215], 1.0 op_sel_hi:[1,0]\n\t"
        "v_pk_fma_f32 v[208:209], v[208:209], v[208:209], 1.0 op_sel_hi:[1,1,0]\n\t"
        "v_pk_fma_f32 v[210:211], v[210:211], v[210:211], 1.0 op_sel_hi:[1,1,0]\n\t"
        "v_pk_fma_f32 v[212:213], v[212:213], v[212:213], 1.0 op_sel_hi:[1,1,0]\n\t"
        "v_pk_fma_f32 v[214:215], v[214:215], v[214:215], 1.0 op_sel_hi:[1,1,0]\n\t"
        "v_rcp_f32 v208, v208\n\t"
        "v_rcp_f32 v209, v209\n\t"
        "v_rcp_f32 v210, v210\n\t"
        "v_rcp_f32 v211, v211\n\t"
        "v_rcp_f32 v212, v212\n\t"
        "v_rcp_f32 v213, v213\n\t"
        "v_rcp_f32 v214, v214\n\t"
        "v_rcp_f32 v215, v215\n\t"
        "s_add_u32 s62, s62, 4096\n\t"
        "buffer_load_dwordx4 v[72:75], v247, s[16:19], s62 offen offset:0 sc1\n\t"
        "buffer_load_dwordx4 v[76:79], v247, s[16:19], s62 offen offset:1024 sc1\n\t"
        "v_pk_fma_f32 v[208:209], v[208:209], v[226:227], v[226:227] op_sel:[0,0,1] op_sel_hi:[1,0,1]\n\t"
        "v_pk_fma_f32 v[210:211], v[210:211], v[226:227], v[226:227] op_sel:[0,0,1] op_sel_hi:[1,0,1]\n\t"
        "v_pk_fma_f32 v[212:213], v[212:213], v[226:227], v[226:227] op_sel:[0,0,1] op_sel_hi:[1,0,1]\n\t"
        "v_pk_fma_f32 v[214:215], v[214:215], v[226:227], v[226:227] op_sel:[0,0,1] op_sel_hi:[1,0,1]\n\t"
        "v_pk_mul_f32 v[112:113], v[112:113], v[208:209]\n\t"
        "v_pk_mul_f32 v[114:115], v[114:115], v[210:211]\n\t"
        "v_pk_mul_f32 v[116:117], v[116:117], v[212:213]\n\t"
        "v_pk_mul_f32 v[118:119], v[118:119], v[214:215]\n\t"
        "v_cvt_pk_bf16_f32 v0, v112, v113\n\t"
        "v_cvt_pk_bf16_f32 v1, v114, v115\n\t"
        "v_cvt_pk_bf16_f32 v2, v116, v117\n\t"
        "v_cvt_pk_bf16_f32 v3, v118, v119\n\t"
        "buffer_store_dwordx4 v[0:3], v231, s[16:19], s47 offen offset:0 sc1\n\t"
        "ds_read_b128 v[48:51], v230 offset:768\n\t"
        "ds_read_b128 v[52:55], v230 offset:832\n\t"
        "ds_read_b128 v[192:195], v230 offset:896\n\t"
        "ds_read_b128 v[196:199], v230 offset:960\n\t"
        "s_waitcnt lgkmcnt(9)\n\t"
        "v_pk_fma_f32 v[16:17], v[16:17], v[224:225], v[24:25] op_sel:[0,1,0] op_sel_hi:[1,1,1]\n\t"
        "v_pk_fma_f32 v[18:19], v[18:19], v[224:225], v[26:27] op_sel:[0,1,0] op_sel_hi:[1,1,1]\n\t"
        "s_waitcnt lgkmcnt(8)\n\t"
        "v_pk_fma_f32 v[20:21], v[20:21], v[224:225], v[28:29] op_sel:[0,1,0] op_sel_hi:[1,1,1]\n\t"
        "v_pk_fma_f32 v[22:23], v[22:23], v[224:225], v[30:31] op_sel:[0,1,0] op_sel_hi:[1,1,1]\n\t"
        "v_pk_fma_f32 v[120:121], v[120:121], v[224:225], v[16:17] op_sel_hi:[1,0,1]\n\t"
        "v_pk_fma_f32 v[122:123], v[122:123], v[224:225], v[18:19] op_sel_hi:[1,0,1]\n\t"
        "v_pk_fma_f32 v[124:125], v[124:125], v[224:225], v[20:21] op_sel_hi:[1,0,1]\n\t"
        "v_pk_fma_f32 v[126:127], v[126:127], v[224:225], v[22:23] op_sel_hi:[1,0,1]\n\t"
        "v_exp_f32 v208, v120\n\t"
        "v_exp_f32 v209, v121\n\t"
        "v_exp_f32 v210, v122\n\t"
        "v_exp_f32 v211, v123\n\t"
        "v_exp_f32 v212, v124\n\t"
        "v_exp_f32 v213, v125\n\t"
        "v_exp_f32 v214, v126\n\t"
        "v_exp_f32 v215, v127\n\t"
        "buffer_load_dwordx4 v[80:83], v247, s[16:19], s62 offen offset:2048 sc1\n\t"
        "buffer_load_dwordx4 v[84:87], v247, s[16:19], s62 offen offset:3072 sc1\n\t"
        "v_pk_add_f32 v[208:209], v[208:209], 1.0 op_sel_hi:[1,0]\n\t"
        "v_pk_add_f32 v[210:211], v[210:211], 1.0 op_sel_hi:[1,0]\n\t"
        "v_pk_add_f32 v[212:213], v[212:213], 1.0 op_sel_hi:[1,0]\n\t"
        "v_pk_add_f32 v[214:215], v[214:215], 1.0 op_sel_hi:[1,0]\n\t"
        "v_pk_fma_f32 v[208:209], v[208:209], v[208:209], 1.0 op_sel_hi:[1,1,0]\n\t"
        "v_pk_fma_f32 v[210:211], v[210:211], v[210:211], 1.0 op_sel_hi:[1,1,0]\n\t"
        "v_pk_fma_f32 v[212:213], v[212:213], v[212:213], 1.0 op_sel_hi:[1,1,0]\n\t"
        "v_pk_fma_f32 v[214:215], v[214:215], v[214:215], 1.0 op_sel_hi:[1,1,0]\n\t"
        "v_rcp_f32 v208, v208\n\t"
        "v_rcp_f32 v209, v209\n\t"
        "v_rcp_f32 v210, v210\n\t"
        "v_rcp_f32 v211, v211\n\t"
        "v_rcp_f32 v212, v212\n\t"
        "v_rcp_f32 v213, v213\n\t"
        "v_rcp_f32 v214, v214\n\t"
        "v_rcp_f32 v215, v215\n\t"
        "s_add_u32 s62, s62, 4096\n\t"
        "buffer_load_dwordx4 v[88:91], v247, s[16:19], s62 offen offset:0 sc1\n\t"
        "buffer_load_dwordx4 v[92:95], v247, s[16:19], s62 offen offset:1024 sc1\n\t"
        "v_pk_fma_f32 v[208:209], v[208:209], v[226:227], v[226:227] op_sel:[0,0,1] op_sel_hi:[1,0,1]\n\t"
        "v_pk_fma_f32 v[210:211], v[210:211], v[226:227], v[226:227] op_sel:[0,0,1] op_sel_hi:[1,0,1]\n\t"
        "v_pk_fma_f32 v[212:213], v[212:213], v[226:227], v[226:227] op_sel:[0,0,1] op_sel_hi:[1,0,1]\n\t"
        "v_pk_fma_f32 v[214:215], v[214:215], v[226:227], v[226:227] op_sel:[0,0,1] op_sel_hi:[1,0,1]\n\t"
        "v_pk_mul_f32 v[120:121], v[120:121], v[208:209]\n\t"
        "v_pk_mul_f32 v[122:123], v[122:123], v[210:211]\n\t"
        "v_pk_mul_f32 v[124:125], v[124:125], v[212:213]\n\t"
        "v_pk_mul_f32 v[126:127], v[126:127], v[214:215]\n\t"
        "v_cvt_pk_bf16_f32 v16, v120, v121\n\t"
        "v_cvt_pk_bf16_f32 v17, v122, v123\n\t"
        "v_cvt_pk_bf16_f32 v18, v124, v125\n\t"
        "v_cvt_pk_bf16_f32 v19, v126, v127\n\t"
        "buffer_store_dwordx4 v[16:19], v231, s[16:19], s47 offen offset:2048 sc1\n\t"
        "s_waitcnt lgkmcnt(5)\n\t"
        "v_pk_fma_f32 v[32:33], v[32:33], v[224:225], v[40:41] op_sel:[0,1,0] op_sel_hi:[1,1,1]\n\t"
        "v_pk_fma_f32 v[34:35], v[34:35], v[224:225], v[42:43] op_sel:[0,1,0] op_sel_hi:[1,1,1]\n\t"
        "s_waitcnt lgkmcnt(4)\n\t"
        "v_pk_fma_f32 v[36:37], v[36:37], v[224:225], v[44:45] op_sel:[0,1,0] op_sel_hi:[1,1,1]\n\t"
        "v_pk_fma_f32 v[38:39], v[38:39], v[224:225], v[46:47] op_sel:[0,1,0] op_sel_hi:[1,1,1]\n\t"
        "v_pk_fma_f32 v[128:129], v[128:129], v[224:225], v[32:33] op_sel_hi:[1,0,1]\n\t"
        "v_pk_fma_f32 v[130:131], v[130:131], v[224:225], v[34:35] op_sel_hi:[1,0,1]\n\t"
        "v_pk_fma_f32 v[132:133], v[132:133], v[224:225], v[36:37] op_sel_hi:[1,0,1]\n\t"
        "v_pk_fma_f32 v[134:135], v[134:135], v[224:225], v[38:39] op_sel_hi:[1,0,1]\n\t"
        "v_exp_f32 v208, v128\n\t"
        "v_exp_f32 v209, v129\n\t"
        "v_exp_f32 v210, v130\n\t"
        "v_exp_f32 v211, v131\n\t"
        "v_exp_f32 v212, v132\n\t"
        "v_exp_f32 v213, v133\n\t"
        "v_exp_f32 v214, v134\n\t"
        "v_exp_f32 v215, v135\n\t"
        "buffer_load_dwordx4 v[96:99], v247, s[16:19], s62 offen offset:2048 sc1\n\t"
        "buffer_load_dwordx4 v[100:103], v247, s[16:19], s62 offen offset:3072 sc1\n\t"
        "v_pk_add_f32 v[208:209], v[208:209], 1.0 op_sel_hi:[1,0]\n\t"
        "v_pk_add_f32 v[210:211], v[210:211], 1.0 op_sel_hi:[1,0]\n\t"
        "v_pk_add_f32 v[212:213], v[212:213], 1.0 op_sel_hi:[1,0]\n\t"
        "v_pk_add_f32 v[214:215], v[214:215], 1.0 op_sel_hi:[1,0]\n\t"
        "v_pk_fma_f32 v[208:209], v[208:209], v[208:209], 1.0 op_sel_hi:[1,1,0]\n\t"
        "v_pk_fma_f32 v[210:211], v[210:211], v[210:211], 1.0 op_sel_hi:[1,1,0]\n\t"
        "v_pk_fma_f32 v[212:213], v[212:213], v[212:213], 1.0 op_sel_hi:[1,1,0]\n\t"
        "v_pk_fma_f32 v[214:215], v[214:215], v[214:215], 1.0 op_sel_hi:[1,1,0]\n\t"
        "v_rcp_f32 v208, v208\n\t"
        "v_rcp_f32 v209, v209\n\t"
        "v_rcp_f32 v210, v210\n\t"
        "v_rcp_f32 v211, v211\n\t"
        "v_rcp_f32 v212, v212\n\t"
        "v_rcp_f32 v213, v213\n\t"
        "v_rcp_f32 v214, v214\n\t"
        "v_rcp_f32 v215, v215\n\t"
        "s_add_u32 s62, s62, 4096\n\t"
        "buffer_load_dwordx4 v[104:107], v247, s[16:19], s62 offen offset:0 sc1\n\t"
        "buffer_load_dwordx4 v[108:111], v247, s[16:19], s62 offen offset:1024 sc1\n\t"
        "v_pk_fma_f32 v[208:209], v[208:209], v[226:227], v[226:227] op_sel:[0,0,1] op_sel_hi:[1,0,1]\n\t"
        "v_pk_fma_f32 v[210:211], v[210:211], v[226:227], v[226:227] op_sel:[0,0,1] op_sel_hi:[1,0,1]\n\t"
        "v_pk_fma_f32 v[212:213], v[212:213], v[226:227], v[226:227] op_sel:[0,0,1] op_sel_hi:[1,0,1]\n\t"
        "v_pk_fma_f32 v[214:215], v[214:215], v[226:227], v[226:227] op_sel:[0,0,1] op_sel_hi:[1,0,1]\n\t"
        "v_pk_mul_f32 v[128:129], v[128:129], v[208:209]\n\t"
        "v_pk_mul_f32 v[130:131], v[130:131], v[210:211]\n\t"
        "v_pk_mul_f32 v[132:133], v[132:133], v[212:213]\n\t"
        "v_pk_mul_f32 v[134:135], v[134:135], v[214:215]\n\t"
        "v_cvt_pk_bf16_f32 v32, v128, v129\n\t"
        "v_cvt_pk_bf16_f32 v33, v130, v131\n\t"
        "v_cvt_pk_bf16_f32 v34, v132, v133\n\t"
        "v_cvt_pk_bf16_f32 v35, v134, v135\n\t"
        "s_add_u32 s47, s47, 4096\n\t"
        "buffer_store_dwordx4 v[32:35], v231, s[16:19], s47 offen offset:0 sc1\n\t"
        "s_waitcnt lgkmcnt(1)\n\t"
        "v_pk_fma_f32 v[48:49], v[48:49], v[224:225], v[192:193] op_sel:[0,1,0] op_sel_hi:[1,1,1]\n\t"
        "v_pk_fma_f32 v[50:51], v[50:51], v[224:225], v[194:195] op_sel:[0,1,0] op_sel_hi:[1,1,1]\n\t"
        "s_waitcnt lgkmcnt(0)\n\t"
        "v_pk_fma_f32 v[52:53], v[52:53], v[224:225], v[196:197] op_sel:[0,1,0] op_sel_hi:[1,1,1]\n\t"
        "v_pk_fma_f32 v[54:55], v[54:55], v[224:225], v[198:199] op_sel:[0,1,0] op_sel_hi:[1,1,1]\n\t"
        "v_pk_fma_f32 v[136:137], v[136:137], v[224:225], v[48:49] op_sel_hi:[1,0,1]\n\t"
        "v_pk_fma_f32 v[138:139], v[138:139], v[224:225], v[50:51] op_sel_hi:[1,0,1]\n\t"
        "v_pk_fma_f32 v[140:141], v[140:141], v[224:225], v[52:53] op_sel_hi:[1,0,1]\n\t"
        "v_pk_fma_f32 v[142:143], v[142:143], v[224:225], v[54:55] op_sel_hi:[1,0,1]\n\t"
        "v_exp_f32 v208, v136\n\t"
        "v_exp_f32 v209, v137\n\t"
        "v_exp_f32 v210, v138\n\t"
        "v_exp_f32 v211, v139\n\t"
        "v_exp_f32 v212, v140\n\t"
        "v_exp_f32 v213, v141\n\t"
        "v_exp_f32 v214, v142\n\t"
        "v_exp_f32 v215, v143\n\t"
        "buffer_load_dwordx4 v[144:147], v247, s[16:19], s62 offen offset:2048 sc1\n\t"
        "buffer_load_dwordx4 v[148:151], v247, s[16:19], s62 offen offset:3072 sc1\n\t"
        "v_pk_add_f32 v[208:209], v[208:209], 1.0 op_sel_hi:[1,0]\n\t"
        "v_pk_add_f32 v[210:211], v[210:211], 1.0 op_sel_hi:[1,0]\n\t"
        "v_pk_add_f32 v[212:213], v[212:213], 1.0 op_sel_hi:[1,0]\n\t"
        "v_pk_add_f32 v[214:215], v[214:215], 1.0 op_sel_hi:[1,0]\n\t"
        "v_pk_fma_f32 v[208:209], v[208:209], v[208:209], 1.0 op_sel_hi:[1,1,0]\n\t"
        "v_pk_fma_f32 v[210:211], v[210:211], v[210:211], 1.0 op_sel_hi:[1,1,0]\n\t"
        "v_pk_fma_f32 v[212:213], v[212:213], v[212:213], 1.0 op_sel_hi:[1,1,0]\n\t"
        "v_pk_fma_f32 v[214:215], v[214:215], v[214:215], 1.0 op_sel_hi:[1,1,0]\n\t"
        "v_rcp_f32 v208, v208\n\t"
        "v_rcp_f32 v209, v209\n\t"
        "v_rcp_f32 v210, v210\n\t"
        "v_rcp_f32 v211, v211\n\t"
        "v_rcp_f32 v212, v212\n\t"
        "v_rcp_f32 v213, v213\n\t"
        "v_rcp_f32 v214, v214\n\t"
        "v_rcp_f32 v215, v215\n\t"
        "s_add_u32 s62, s62, 4096\n\t"
        "buffer_load_dwordx4 v[152:155], v247, s[16:19], s62 offen offset:0 sc1\n\t"
        "buffer_load_dwordx4 v[156:159], v247, s[16:19], s62 offen offset:1024 sc1\n\t"
        "v_pk_fma_f32 v[208:209], v[208:209], v[226:227], v[226:227] op_sel:[0,0,1] op_sel_hi:[1,0,1]\n\t"
        "v_pk_fma_f32 v[210:211], v[210:211], v[226:227], v[226:227] op_sel:[0,0,1] op_sel_hi:[1,0,1]\n\t"
        "v_pk_fma_f32 v[212:213], v[212:213], v[226:227], v[226:227] op_sel:[0,0,1] op_sel_hi:[1,0,1]\n\t"
        "v_pk_fma_f32 v[214:215], v[214:215], v[226:227], v[226:227] op_sel:[0,0,1] op_sel_hi:[1,0,1]\n\t"
        "v_pk_mul_f32 v[136:137], v[136:137], v[208:209]\n\t"
        "v_pk_mul_f32 v[138:139], v[138:139], v[210:211]\n\t"
        "v_pk_mul_f32 v[140:141], v[140:141], v[212:213]\n\t"
        "v_pk_mul_f32 v[142:143], v[142:143], v[214:215]\n\t"
        "v_cvt_pk_bf16_f32 v48, v136, v137\n\t"
        "v_cvt_pk_bf16_f32 v49, v138, v139\n\t"
        "v_cvt_pk_bf16_f32 v50, v140, v141\n\t"
        "v_cvt_pk_bf16_f32 v51, v142, v143\n\t"
        "buffer_store_dwordx4 v[48:51], v231, s[16:19], s47 offen offset:2048 sc1\n\t"
        "s_waitcnt vmcnt(21)\n\t"
        "v_mfma_f32_32x32x16_bf16 v[0:15], a[0:3], v[56:59], 0\n\t"
        "v_mfma_f32_32x32x16_bf16 v[16:31], a[112:115], v[56:59], 0\n\t"
        "buffer_load_dwordx4 v[160:163], v247, s[16:19], s62 offen offset:2048 sc1\n\t"
        "buffer_load_dwordx4 v[164:167], v247, s[16:19], s62 offen offset:3072 sc1\n\t"
        "s_waitcnt vmcnt(22)\n\t"
        "v_mfma_f32_32x32x16_bf16 v[0:15], a[4:7], v[60:63], v[0:15]\n\t"
        "v_mfma_f32_32x32x16_bf16 v[16:31], a[116:119], v[60:63], v[16:31]\n\t"
        "s_add_u32 s62, s62, 4096\n\t"
        "buffer_load_dwordx4 v[168:171], v247, s[16:19], s62 offen offset:0 sc1\n\t"
        "buffer_load_dwordx4 v[172:175], v247, s[16:19], s62 offen offset:1024 sc1\n\t"
        "s_waitcnt vmcnt(23)\n\t"
        "v_mfma_f32_32x32x16_bf16 v[0:15], a[8:11], v[64:67], v[0:15]\n\t"
        "v_lshlrev_b32 v32, 16, v248\n\t"
        "v_mfma_f32_32x32x16_bf16 v[16:31], a[120:123], v[64:67], v[16:31]\n\t"
        "v_and_b32 v33, 0xffff0000, v248\n\t"
        "buffer_load_dwordx4 v[176:179], v247, s[16:19], s62 offen offset:2048 sc1\n\t"
        "buffer_load_dwordx4 v[180:183], v247, s[16:19], s62 offen offset:3072 sc1\n\t"
        "s_waitcnt vmcnt(24)\n\t"
        "v_mfma_f32_32x32x16_bf16 v[0:15], a[12:15], v[68:71], v[0:15]\n\t"
        "v_lshlrev_b32 v34, 16, v249\n\t"
        "v_mfma_f32_32x32x16_bf16 v[16:31], a[124:127], v[68:71], v[16:31]\n\t"
        "v_and_b32 v35, 0xffff0000, v249\n\t"
        "s_add_u32 s62, s62, 4096\n\t"
        "buffer_load_dwordx4 v[184:187], v247, s[16:19], s62 offen offset:0 sc1\n\t"
        "buffer_load_dwordx4 v[188:191], v247, s[16:19], s62 offen offset:1024 sc1\n\t"
        "s_waitcnt vmcnt(25)\n\t"
        "v_mfma_f32_32x32x16_bf16 v[0:15], a[16:19], v[72:75], v[0:15]\n\t"
        "v_lshlrev_b32 v36, 16, v250\n\t"
        "v_mfma_f32_32x32x16_bf16 v[16:31], a[128:131], v[72:75], v[16:31]\n\t"
        "v_and_b32 v37, 0xffff0000, v250\n\t"
        "buffer_load_dwordx4 v[200:203], v247, s[16:19], s62 offen offset:2048 sc1\n\t"
        "buffer_load_dwordx4 v[204:207], v247, s[16:19], s62 offen offset:3072 sc1\n\t"
        "s_waitcnt vmcnt(26)\n\t"
        "v_mfma_f32_32x32x16_bf16 v[0:15], a[20:23], v[76:79], v[0:15]\n\t"
        "v_lshlrev_b32 v38, 16, v251\n\t"
        "v_mfma_f32_32x32x16_bf16 v[16:31], a[132:135], v[76:79], v[16:31]\n\t"
        "v_and_b32 v39, 0xffff0000, v251\n\t"
        "s_waitcnt vmcnt(24)\n\t"
        "v_mfma_f32_32x32x16_bf16 v[0:15], a[24:27], v[80:83], v[0:15]\n\t"
        "v_mfma_f32_32x32x16_bf16 v[16:31], a[136:139], v[80:83], v[16:31]\n\t"
        "s_mul_i32 s38, s27, 147456\n\t"
        "s_lshl_b32 s39, s24, 7\n\t"
        "s_lshl_b32 s40, s25, 3\n\t"
        "s_add_u32 s38, s38, s39\n\t"
        "s_add_u32 s38, s38, s40\n\t"
        "s_add_u32 s38, s38, 145408\n\t"
        "s_waitcnt vmcnt(10)\n\t"
        "s_barrier\n\t"
        "s_cmp_lg_u32 s26, 0\n\t"
        "s_cbranch_scc1 sig_72_%=\n\t"
        "s_mov_b64 exec, 1\n\t"
        "buffer_atomic_swap_x2 v[234:235], off, s[16:19], s38\n\t"
        "s_mov_b64 exec, -1\n\t"
        "sig_72_%=:\n\t"
        "buffer_load_dwordx4 a[224:227], v231, s[16:19], s82 offen offset:0 sc1\n\t"
        "buffer_load_dwordx4 a[224:227], v231, s[16:19], s82 offen offset:2048 sc1\n\t"
        "buffer_load_dwordx4 a[224:227], v231, s[16:19], s83 offen offset:0 sc1\n\t"
        "buffer_load_dwordx4 a[224:227], v231, s[16:19], s83 offen offset:2048 sc1\n\t"
        "v_mfma_f32_32x32x16_bf16 v[0:15], a[28:31], v[84:87], v[0:15]\n\t"
        "v_mfma_f32_32x32x16_bf16 v[16:31], a[140:143], v[84:87], v[16:31]\n\t"
        "s_cmp_lg_u32 s27, 0\n\t"
        "s_cbranch_scc1 l0_73_%=\n\t"
        "v_cmp_ne_u64 vcc, s[10:11], v[232:233]\n\t"
        "s_nop 0\n\t"
        "s_cbranch_vccnz sp_74_%=\n\t"
        "bk_75_%=:\n\t"
        "s_mul_i32 s35, s25, 86016\n\t"
        "s_mul_i32 s41, s26, 21504\n\t"
        "s_add_u32 s35, s35, s41\n\t"
        "s_add_u32 s35, s35, 368640\n\t"
        "s_mov_b32 m0, s41\n\t"
        "s_nop 0\n\t"
        "buffer_load_dwordx4 v247, s[16:19], s35 offen offset:0 sc1 lds\n\t"
        "buffer_load_dwordx4 v247, s[16:19], s35 offen offset:1024 sc1 lds\n\t"
        "buffer_load_dwordx4 v247, s[16:19], s35 offen offset:2048 sc1 lds\n\t"
        "buffer_load_dwordx4 v247, s[16:19], s35 offen offset:3072 sc1 lds\n\t"
        "s_add_u32 s35, s35, 4096\n\t"
        "s_add_u32 m0, m0, 4096\n\t"
        "s_nop 0\n\t"
        "buffer_load_dwordx4 v247, s[16:19], s35 offen offset:0 sc1 lds\n\t"
        "buffer_load_dwordx4 v247, s[16:19], s35 offen offset:1024 sc1 lds\n\t"
        "buffer_load_dwordx4 v247, s[16:19], s35 offen offset:2048 sc1 lds\n\t"
        "buffer_load_dwordx4 v247, s[16:19], s35 offen offset:3072 sc1 lds\n\t"
        "l0_73_%=:\n\t"
        "v_mfma_f32_32x32x16_bf16 v[0:15], a[32:35], v[88:91], v[0:15]\n\t"
        "v_mfma_f32_32x32x16_bf16 v[16:31], a[144:147], v[88:91], v[16:31]\n\t"
        "v_mfma_f32_32x32x16_bf16 v[0:15], a[36:39], v[92:95], v[0:15]\n\t"
        "v_mfma_f32_32x32x16_bf16 v[16:31], a[148:151], v[92:95], v[16:31]\n\t"
        "v_mfma_f32_32x32x16_bf16 v[0:15], a[40:43], v[96:99], v[0:15]\n\t"
        "v_mfma_f32_32x32x16_bf16 v[16:31], a[152:155], v[96:99], v[16:31]\n\t"
        "v_mfma_f32_32x32x16_bf16 v[0:15], a[44:47], v[100:103], v[0:15]\n\t"
        "v_mfma_f32_32x32x16_bf16 v[16:31], a[156:159], v[100:103], v[16:31]\n\t"
        "v_mfma_f32_32x32x16_bf16 v[0:15], a[48:51], v[104:107], v[0:15]\n\t"
        "v_mfma_f32_32x32x16_bf16 v[16:31], a[160:163], v[104:107], v[16:31]\n\t"
        "s_cmp_lg_u32 s27, 0\n\t"
        "s_cbranch_scc1 l0_76_%=\n\t"
        "s_add_u32 s35, s35, 4096\n\t"
        "s_add_u32 m0, m0, 4096\n\t"
        "s_nop 0\n\t"
        "buffer_load_dwordx4 v247, s[16:19], s35 offen offset:0 sc1 lds\n\t"
        "buffer_load_dwordx4 v247, s[16:19], s35 offen offset:1024 sc1 lds\n\t"
        "buffer_load_dwordx4 v247, s[16:19], s35 offen offset:2048 sc1 lds\n\t"
        "buffer_load_dwordx4 v247, s[16:19], s35 offen offset:3072 sc1 lds\n\t"
        "s_add_u32 s35, s35, 4096\n\t"
        "s_add_u32 m0, m0, 4096\n\t"
        "s_nop 0\n\t"
        "buffer_load_dwordx4 v247, s[16:19], s35 offen offset:0 sc1 lds\n\t"
        "buffer_load_dwordx4 v247, s[16:19], s35 offen offset:1024 sc1 lds\n\t"
        "buffer_load_dwordx4 v247, s[16:19], s35 offen offset:2048 sc1 lds\n\t"
        "buffer_load_dwordx4 v247, s[16:19], s35 offen offset:3072 sc1 lds\n\t"
        "l0_76_%=:\n\t"
        "v_mfma_f32_32x32x16_bf16 v[0:15], a[52:55], v[108:111], v[0:15]\n\t"
        "v_mfma_f32_32x32x16_bf16 v[16:31], a[164:167], v[108:111], v[16:31]\n\t"
        "v_mfma_f32_32x32x16_bf16 v[0:15], a[56:59], v[144:147], v[0:15]\n\t"
        "v_mfma_f32_32x32x16_bf16 v[16:31], a[168:171], v[144:147], v[16:31]\n\t"
        "v_mfma_f32_32x32x16_bf16 v[0:15], a[60:63], v[148:151], v[0:15]\n\t"
        "v_mfma_f32_32x32x16_bf16 v[16:31], a[172:175], v[148:151], v[16:31]\n\t"
        "v_mfma_f32_32x32x16_bf16 v[0:15], a[64:67], v[152:155], v[0:15]\n\t"
        "v_mfma_f32_32x32x16_bf16 v[16:31], a[176:179], v[152:155], v[16:31]\n\t"
        "v_mfma_f32_32x32x16_bf16 v[0:15], a[68:71], v[156:159], v[0:15]\n\t"
        "v_mfma_f32_32x32x16_bf16 v[16:31], a[180:183], v[156:159], v[16:31]\n\t"
        "s_cmp_lg_u32 s27, 0\n\t"
        "s_cbranch_scc1 l0_77_%=\n\t"
        "s_add_u32 s35, s35, 4096\n\t"
        "s_add_u32 m0, m0, 4096\n\t"
        "s_nop 0\n\t"
        "buffer_load_dwordx4 v247, s[16:19], s35 offen offset:0 sc1 lds\n\t"
        "buffer_load_dwordx4 v247, s[16:19], s35 offen offset:1024 sc1 lds\n\t"
        "buffer_load_dwordx4 v247, s[16:19], s35 offen offset:2048 sc1 lds\n\t"
        "buffer_load_dwordx4 v247, s[16:19], s35 offen offset:3072 sc1 lds\n\t"
        "s_add_u32 s35, s35, 4096\n\t"
        "s_add_u32 m0, m0, 4096\n\t"
        "s_nop 0\n\t"
        "buffer_load_dwordx4 v247, s[16:19], s35 offen offset:0 sc1 lds\n\t"
        "l0_77_%=:\n\t"
        "s_waitcnt vmcnt(13)\n\t"
        "v_mfma_f32_32x32x16_bf16 v[0:15], a[72:75], v[160:163], v[0:15]\n\t"
        "v_mfma_f32_32x32x16_bf16 v[16:31], a[184:187], v[160:163], v[16:31]\n\t"
        "s_waitcnt vmcnt(12)\n\t"
        "v_mfma_f32_32x32x16_bf16 v[0:15], a[76:79], v[164:167], v[0:15]\n\t"
        "v_mfma_f32_32x32x16_bf16 v[16:31], a[188:191], v[164:167], v[16:31]\n\t"
        "s_waitcnt vmcnt(11)\n\t"
        "v_mfma_f32_32x32x16_bf16 v[0:15], a[80:83], v[168:171], v[0:15]\n\t"
        "v_mfma_f32_32x32x16_bf16 v[16:31], a[192:195], v[168:171], v[16:31]\n\t"
        "s_waitcnt vmcnt(10)\n\t"
        "v_mfma_f32_32x32x16_bf16 v[0:15], a[84:87], v[172:175], v[0:15]\n\t"
        "v_mfma_f32_32x32x16_bf16 v[16:31], a[196:199], v[172:175], v[16:31]\n\t"
        "s_waitcnt vmcnt(9)\n\t"
        "v_mfma_f32_32x32x16_bf16 v[0:15], a[88:91], v[176:179], v[0:15]\n\t"
        "v_mfma_f32_32x32x16_bf16 v[16:31], a[200:203], v[176:179], v[16:31]\n\t"
        "s_waitcnt vmcnt(8)\n\t"
        "v_mfma_f32_32x32x16_bf16 v[0:15], a[92:95], v[180:183], v[0:15]\n\t"
        "v_mfma_f32_32x32x16_bf16 v[16:31], a[204:207], v[180:183], v[16:31]\n\t"
        "s_waitcnt vmcnt(7)\n\t"
        "v_mfma_f32_32x32x16_bf16 v[0:15], a[96:99], v[184:187], v[0:15]\n\t"
        "v_mfma_f32_32x32x16_bf16 v[16:31], a[208:211], v[184:187], v[16:31]\n\t"
        "s_mul_i32 s9, s27, 147456\n\t"
        "s_add_u32 s9, s9, s14\n\t"
        "s_add_u32 s9, s9, 145408\n\t"
        "buffer_load_dwordx2 v[232:233], v237, s[16:19], s9 offen sc1\n\t"
        "s_waitcnt vmcnt(7)\n\t"
        "v_mfma_f32_32x32x16_bf16 v[0:15], a[100:103], v[188:191], v[0:15]\n\t"
        "v_mfma_f32_32x32x16_bf16 v[16:31], a[212:215], v[188:191], v[16:31]\n\t"
        "s_waitcnt vmcnt(6)\n\t"
        "v_mfma_f32_32x32x16_bf16 v[0:15], a[104:107], v[200:203], v[0:15]\n\t"
        "v_mfma_f32_32x32x16_bf16 v[16:31], a[216:219], v[200:203], v[16:31]\n\t"
        "s_waitcnt vmcnt(5)\n\t"
        "v_mfma_f32_32x32x16_bf16 v[16:31], a[220:223], v[204:207], v[16:31]\n\t"
        "v_mfma_f32_32x32x16_bf16 v[0:15], a[108:111], v[204:207], v[0:15]\n\t"
        "s_nop 10\n\t"
        "ds_write_b128 v242, v[16:19] offset:0\n\t"
        "ds_write_b128 v242, v[20:23] offset:1024\n\t"
        "ds_write_b128 v241, v[24:27] offset:0\n\t"
        "ds_write_b128 v241, v[28:31] offset:1024\n\t"
        "ds_write_b128 v243, v[8:11] offset:0\n\t"
        "ds_write_b128 v243, v[12:15] offset:1024\n\t"
        "s_waitcnt vmcnt(0)\n\t"
        "v_cmp_ne_u64 vcc, s[10:11], v[232:233]\n\t"
        "s_nop 0\n\t"
        "s_cbranch_vccnz sp_78_%=\n\t"
        "bk_79_%=:\n\t"
        "s_add_u32 s62, s61, s28\n\t"
        "s_add_u32 s62, s62, 13205504\n\t"
        "buffer_load_dwordx4 v[56:59], v247, s[16:19], s62 offen offset:0 sc1\n\t"
        "buffer_load_dwordx4 v[60:63], v247, s[16:19], s62 offen offset:1024 sc1\n\t"
        "buffer_load_dwordx4 v[64:67], v247, s[16:19], s62 offen offset:2048 sc1\n\t"
        "buffer_load_dwordx4 v[68:71], v247, s[16:19], s62 offen offset:3072 sc1\n\t"
        "s_add_u32 s62, s62, 4096\n\t"
        "buffer_load_dwordx4 v[72:75], v247, s[16:19], s62 offen offset:0 sc1\n\t"
        "buffer_load_dwordx4 v[76:79], v247, s[16:19], s62 offen offset:1024 sc1\n\t"
        "s_waitcnt lgkmcnt(0)\n\t"
        "s_barrier\n\t"
        "ds_read_b128 v[112:115], v240 offset:0\n\t"
        "ds_read_b128 v[120:123], v240 offset:2048\n\t"
        "ds_read_b128 v[128:131], v240 offset:4096\n\t"
        "ds_read_b128 v[116:119], v240 offset:1024\n\t"
        "ds_read_b128 v[124:127], v240 offset:3072\n\t"
        "ds_read_b128 v[132:135], v240 offset:5120\n\t"
        "s_waitcnt lgkmcnt(5)\n\t"
        "v_pk_add_f32 v[0:1], v[0:1], v[112:113]\n\t"
        "s_waitcnt lgkmcnt(3)\n\t"
        "v_pk_add_f32 v[120:121], v[120:121], v[128:129]\n\t"
        "v_pk_add_f32 v[2:3], v[2:3], v[114:115]\n\t"
        "v_pk_add_f32 v[122:123], v[122:123], v[130:131]\n\t"
        "s_waitcnt lgkmcnt(2)\n\t"
        "v_pk_add_f32 v[4:5], v[4:5], v[116:117]\n\t"
        "s_waitcnt lgkmcnt(0)\n\t"
        "v_pk_add_f32 v[124:125], v[124:125], v[132:133]\n\t"
        "v_pk_add_f32 v[6:7], v[6:7], v[118:119]\n\t"
        "v_pk_add_f32 v[126:127], v[126:127], v[134:135]\n\t"
        "v_pk_add_f32 v[0:1], v[0:1], v[120:121]\n\t"
        "v_pk_add_f32 v[2:3], v[2:3], v[122:123]\n\t"
        "v_pk_add_f32 v[4:5], v[4:5], v[124:125]\n\t"
        "v_pk_add_f32 v[6:7], v[6:7], v[126:127]\n\t"
        "buffer_load_dwordx4 v[80:83], v247, s[16:19], s62 offen offset:2048 sc1\n\t"
        "buffer_load_dwordx4 v[84:87], v247, s[16:19], s62 offen offset:3072 sc1\n\t"
        "s_add_u32 s62, s62, 4096\n\t"
        "buffer_load_dwordx4 v[88:91], v247, s[16:19], s62 offen offset:0 sc1\n\t"
        "buffer_load_dwordx4 v[92:95], v247, s[16:19], s62 offen offset:1024 sc1\n\t"
        "buffer_load_dwordx4 v[96:99], v247, s[16:19], s62 offen offset:2048 sc1\n\t"
        "buffer_load_dwordx4 v[100:103], v247, s[16:19], s62 offen offset:3072 sc1\n\t"
        "s_cmp_lg_u32 s27, 0\n\t"
        "s_cbranch_scc1 fin_80_%=\n\t"
        "s_add_u32 s47, s57, s28\n\t"
        "s_add_u32 s47, s47, 20316160\n\t"
        "s_add_u32 s33, s58, s28\n\t"
        "s_add_u32 s33, s33, 22151168\n\t"
        "s_mov_b32 s82, s47\n\t"
        "s_mov_b32 s83, s33\n\t"
        "v_pk_add_f32 v[0:1], v[0:1], v[32:33]\n\t"
        "v_pk_add_f32 v[2:3], v[2:3], v[34:35]\n\t"
        "v_pk_add_f32 v[4:5], v[4:5], v[36:37]\n\t"
        "v_pk_add_f32 v[6:7], v[6:7], v[38:39]\n\t"
        "v_cvt_pk_bf16_f32 v248, v0, v1\n\t"
        "v_cvt_pk_bf16_f32 v249, v2, v3\n\t"
        "v_cvt_pk_bf16_f32 v250, v4, v5\n\t"
        "v_cvt_pk_bf16_f32 v251, v6, v7\n\t"
        "buffer_store_dwordx4 v[248:251], v247, s[16:19], s47 offen sc1\n\t"
        "v_pk_add_f32 v[40:41], v[0:1], v[2:3]\n\t"
        "v_pk_mul_f32 v[42:43], v[0:1], v[0:1]\n\t"
        "v_pk_add_f32 v[44:45], v[4:5], v[6:7]\n\t"
        "v_pk_mul_f32 v[46:47], v[2:3], v[2:3]\n\t"
        "v_pk_fma_f32 v[42:43], v[4:5], v[4:5], v[42:43]\n\t"
        "v_pk_fma_f32 v[46:47], v[6:7], v[6:7], v[46:47]\n\t"
        "v_pk_add_f32 v[40:41], v[40:41], v[44:45]\n\t"
        "v_pk_add_f32 v[42:43], v[42:43], v[46:47]\n\t"
        "v_add_f32 v40, v40, v41\n\t"
        "v_add_f32 v41, v42, v43\n\t"
        "v_mov_b32 v42, v40\n\t"
        "v_mov_b32 v43, v41\n\t"
        "s_nop 0\n\t"
        "v_permlane32_swap_b32 v40, v42\n\t"
        "v_permlane32_swap_b32 v41, v43\n\t"
        "v_pk_add_f32 v[40:41], v[40:41], v[42:43]\n\t"
        "v_lshlrev_b32 v44, 1, v239\n\t"
        "s_mov_b64 exec, 0xffffffff\n\t"
        "buffer_store_dwordx2 v[40:41], v44, s[16:19], s33 offen sc1\n\t"
        "s_mov_b64 exec, -1\n\t"
        "s_branch join_81_%=\n\t"
        "fin_80_%=:\n\t"
        "v_pk_add_f32 v[0:1], v[0:1], v[32:33]\n\t"
        "v_pk_add_f32 v[2:3], v[2:3], v[34:35]\n\t"
        "v_pk_add_f32 v[4:5], v[4:5], v[36:37]\n\t"
        "v_pk_add_f32 v[6:7], v[6:7], v[38:39]\n\t"
        "v_cvt_pk_bf16_f32 v248, v0, v1\n\t"
        "v_cvt_pk_bf16_f32 v249, v2, v3\n\t"
        "v_cvt_pk_bf16_f32 v250, v4, v5\n\t"
        "v_cvt_pk_bf16_f32 v251, v6, v7\n\t"
        "v_and_b32 v40, 0x7c, v239\n\t"
        "v_mul_u32_u24 v40, 224, v40\n\t"
        "s_add_u32 s47, s15, 0\n\t"
        "v_add_u32 v40, v40, v244\n\t"
        "v_permlane32_swap_b32 v248, v250\n\t"
        "v_permlane32_swap_b32 v249, v251\n\t"
        "buffer_store_dwordx4 v[248:251], v40, s[20:23], s47 offen\n\t"
        "join_81_%=:\n\t"
        "s_waitcnt vmcnt(11)\n\t"
        "v_mfma_f32_32x32x16_bf16 v[0:15], a[0:3], v[56:59], 0\n\t"
        "v_mfma_f32_32x32x16_bf16 v[16:31], a[112:115], v[56:59], 0\n\t"
        "s_add_u32 s62, s62, 4096\n\t"
        "buffer_load_dwordx4 v[104:107], v247, s[16:19], s62 offen offset:0 sc1\n\t"
        "s_waitcnt vmcnt(11)\n\t"
        "v_mfma_f32_32x32x16_bf16 v[0:15], a[4:7], v[60:63], v[0:15]\n\t"
        "v_mfma_f32_32x32x16_bf16 v[16:31], a[116:119], v[60:63], v[16:31]\n\t"
        "buffer_load_dwordx4 v[108:111], v247, s[16:19], s62 offen offset:1024 sc1\n\t"
        "s_cmp_lg_u32 s27, 0\n\t"
        "s_cbranch_scc1 l0_82_%=\n\t"
        "s_mul_i32 s38, s27, 147456\n\t"
        "s_lshl_b32 s39, s24, 7\n\t"
        "s_lshl_b32 s40, s25, 3\n\t"
        "s_add_u32 s38, s38, s39\n\t"
        "s_add_u32 s38, s38, s40\n\t"
        "s_add_u32 s38, s38, 147456\n\t"
        "s_waitcnt vmcnt(2)\n\t"
        "s_barrier\n\t"
        "s_cmp_lg_u32 s26, 0\n\t"
        "s_cbranch_scc1 sig_83_%=\n\t"
        "s_mov_b64 exec, 1\n\t"
        "buffer_atomic_swap_x2 v[234:235], off, s[16:19], s38\n\t"
        "s_mov_b64 exec, -1\n\t"
        "sig_83_%=:\n\t"
        "l0_82_%=:\n\t"
        "s_waitcnt vmcnt(11)\n\t"
        "v_mfma_f32_32x32x16_bf16 v[0:15], a[8:11], v[64:67], v[0:15]\n\t"
        "v_lshlrev_b32 v32, 16, v252\n\t"
        "v_mfma_f32_32x32x16_bf16 v[16:31], a[120:123], v[64:67], v[16:31]\n\t"
        "v_and_b32 v33, 0xffff0000, v252\n\t"
        "buffer_load_dwordx4 v[144:147], v247, s[16:19], s62 offen offset:2048 sc1\n\t"
        "s_waitcnt vmcnt(11)\n\t"
        "v_mfma_f32_32x32x16_bf16 v[0:15], a[12:15], v[68:71], v[0:15]\n\t"
        "v_lshlrev_b32 v34, 16, v253\n\t"
        "v_mfma_f32_32x32x16_bf16 v[16:31], a[124:127], v[68:71], v[16:31]\n\t"
        "v_and_b32 v35, 0xffff0000, v253\n\t"
        "buffer_load_dwordx4 v[148:151], v247, s[16:19], s62 offen offset:3072 sc1\n\t"
        "s_waitcnt vmcnt(11)\n\t"
        "v_mfma_f32_32x32x16_bf16 v[0:15], a[16:19], v[72:75], v[0:15]\n\t"
        "v_lshlrev_b32 v36, 16, v254\n\t"
        "v_mfma_f32_32x32x16_bf16 v[16:31], a[128:131], v[72:75], v[16:31]\n\t"
        "v_and_b32 v37, 0xffff0000, v254\n\t"
        "s_add_u32 s62, s62, 4096\n\t"
        "buffer_load_dwordx4 v[152:155], v247, s[16:19], s62 offen offset:0 sc1\n\t"
        "s_waitcnt vmcnt(11)\n\t"
        "v_mfma_f32_32x32x16_bf16 v[0:15], a[20:23], v[76:79], v[0:15]\n\t"
        "v_lshlrev_b32 v38, 16, v255\n\t"
        "v_mfma_f32_32x32x16_bf16 v[16:31], a[132:135], v[76:79], v[16:31]\n\t"
        "v_and_b32 v39, 0xffff0000, v255\n\t"
        "buffer_load_dwordx4 v[156:159], v247, s[16:19], s62 offen offset:1024 sc1\n\t"
        "s_waitcnt vmcnt(11)\n\t"
        "v_mfma_f32_32x32x16_bf16 v[0:15], a[24:27], v[80:83], v[0:15]\n\t"
        "v_mfma_f32_32x32x16_bf16 v[16:31], a[136:139], v[80:83], v[16:31]\n\t"
        "buffer_load_dwordx4 v[160:163], v247, s[16:19], s62 offen offset:2048 sc1\n\t"
        "s_waitcnt vmcnt(11)\n\t"
        "v_mfma_f32_32x32x16_bf16 v[0:15], a[28:31], v[84:87], v[0:15]\n\t"
        "v_mfma_f32_32x32x16_bf16 v[16:31], a[140:143], v[84:87], v[16:31]\n\t"
        "buffer_load_dwordx4 v[164:167], v247, s[16:19], s62 offen offset:3072 sc1\n\t"
        "s_waitcnt vmcnt(11)\n\t"
        "v_mfma_f32_32x32x16_bf16 v[0:15], a[32:35], v[88:91], v[0:15]\n\t"
        "v_mfma_f32_32x32x16_bf16 v[16:31], a[144:147], v[88:91], v[16:31]\n\t"
        "s_add_u32 s62, s62, 4096\n\t"
        "buffer_load_dwordx4 v[168:171], v247, s[16:19], s62 offen offset:0 sc1\n\t"
        "s_waitcnt vmcnt(11)\n\t"
        "v_mfma_f32_32x32x16_bf16 v[0:15], a[36:39], v[92:95], v[0:15]\n\t"
        "v_mfma_f32_32x32x16_bf16 v[16:31], a[148:151], v[92:95], v[16:31]\n\t"
        "buffer_load_dwordx4 v[172:175], v247, s[16:19], s62 offen offset:1024 sc1\n\t"
        "s_waitcnt vmcnt(11)\n\t"
        "v_mfma_f32_32x32x16_bf16 v[0:15], a[40:43], v[96:99], v[0:15]\n\t"
        "v_mfma_f32_32x32x16_bf16 v[16:31], a[152:155], v[96:99], v[16:31]\n\t"
        "buffer_load_dwordx4 v[176:179], v247, s[16:19], s62 offen offset:2048 sc1\n\t"
        "s_waitcnt vmcnt(11)\n\t"
        "v_mfma_f32_32x32x16_bf16 v[0:15], a[44:47], v[100:103], v[0:15]\n\t"
        "v_mfma_f32_32x32x16_bf16 v[16:31], a[156:159], v[100:103], v[16:31]\n\t"
        "buffer_load_dwordx4 v[180:183], v247, s[16:19], s62 offen offset:3072 sc1\n\t"
        "s_waitcnt vmcnt(11)\n\t"
        "v_mfma_f32_32x32x16_bf16 v[0:15], a[48:51], v[104:107], v[0:15]\n\t"
        "v_mfma_f32_32x32x16_bf16 v[16:31], a[160:163], v[104:107], v[16:31]\n\t"
        "s_add_u32 s62, s62, 4096\n\t"
        "buffer_load_dwordx4 v[184:187], v247, s[16:19], s62 offen offset:0 sc1\n\t"
        "s_waitcnt vmcnt(11)\n\t"
        "v_mfma_f32_32x32x16_bf16 v[0:15], a[52:55], v[108:111], v[0:15]\n\t"
        "v_mfma_f32_32x32x16_bf16 v[16:31], a[164:167], v[108:111], v[16:31]\n\t"
        "buffer_load_dwordx4 v[188:191], v247, s[16:19], s62 offen offset:1024 sc1\n\t"
        "s_waitcnt vmcnt(11)\n\t"
        "v_mfma_f32_32x32x16_bf16 v[0:15], a[56:59], v[144:147], v[0:15]\n\t"
        "v_mfma_f32_32x32x16_bf16 v[16:31], a[168:171], v[144:147], v[16:31]\n\t"
        "buffer_load_dwordx4 v[200:203], v247, s[16:19], s62 offen offset:2048 sc1\n\t"
        "s_waitcnt vmcnt(11)\n\t"
        "v_mfma_f32_32x32x16_bf16 v[0:15], a[60:63], v[148:151], v[0:15]\n\t"
        "v_mfma_f32_32x32x16_bf16 v[16:31], a[172:175], v[148:151], v[16:31]\n\t"
        "buffer_load_dwordx4 v[204:207], v247, s[16:19], s62 offen offset:3072 sc1\n\t"
        "s_waitcnt vmcnt(11)\n\t"
        "v_mfma_f32_32x32x16_bf16 v[0:15], a[64:67], v[152:155], v[0:15]\n\t"
        "v_mfma_f32_32x32x16_bf16 v[16:31], a[176:179], v[152:155], v[16:31]\n\t"
        "s_waitcnt vmcnt(10)\n\t"
        "v_mfma_f32_32x32x16_bf16 v[0:15], a[68:71], v[156:159], v[0:15]\n\t"
        "v_mfma_f32_32x32x16_bf16 v[16:31], a[180:183], v[156:159], v[16:31]\n\t"
        "s_waitcnt vmcnt(9)\n\t"
        "v_mfma_f32_32x32x16_bf16 v[0:15], a[72:75], v[160:163], v[0:15]\n\t"
        "v_mfma_f32_32x32x16_bf16 v[16:31], a[184:187], v[160:163], v[16:31]\n\t"
        "s_waitcnt vmcnt(8)\n\t"
        "v_mfma_f32_32x32x16_bf16 v[0:15], a[76:79], v[164:167], v[0:15]\n\t"
        "v_mfma_f32_32x32x16_bf16 v[16:31], a[188:191], v[164:167], v[16:31]\n\t"
        "s_waitcnt vmcnt(7)\n\t"
        "v_mfma_f32_32x32x16_bf16 v[0:15], a[80:83], v[168:171], v[0:15]\n\t"
        "v_mfma_f32_32x32x16_bf16 v[16:31], a[192:195], v[168:171], v[16:31]\n\t"
        "s_waitcnt vmcnt(6)\n\t"
        "v_mfma_f32_32x32x16_bf16 v[0:15], a[84:87], v[172:175], v[0:15]\n\t"
        "v_mfma_f32_32x32x16_bf16 v[16:31], a[196:199], v[172:175], v[16:31]\n\t"
        "s_waitcnt vmcnt(5)\n\t"
        "v_mfma_f32_32x32x16_bf16 v[0:15], a[88:91], v[176:179], v[0:15]\n\t"
        "v_mfma_f32_32x32x16_bf16 v[16:31], a[200:203], v[176:179], v[16:31]\n\t"
        "s_cmp_lg_u32 s27, 0\n\t"
        "s_cbranch_scc1 l0_84_%=\n\t"
        "s_mul_i32 s9, s27, 147456\n\t"
        "s_add_u32 s9, s9, s14\n\t"
        "s_add_u32 s9, s9, 147456\n\t"
        "buffer_load_dwordx2 v[232:233], v237, s[16:19], s9 offen sc1\n\t"
        "l0_84_%=:\n\t"
        "s_waitcnt vmcnt(4)\n\t"
        "v_mfma_f32_32x32x16_bf16 v[0:15], a[92:95], v[180:183], v[0:15]\n\t"
        "v_mfma_f32_32x32x16_bf16 v[16:31], a[204:207], v[180:183], v[16:31]\n\t"
        "s_waitcnt vmcnt(3)\n\t"
        "v_mfma_f32_32x32x16_bf16 v[0:15], a[96:99], v[184:187], v[0:15]\n\t"
        "v_mfma_f32_32x32x16_bf16 v[16:31], a[208:211], v[184:187], v[16:31]\n\t"
        "s_waitcnt vmcnt(2)\n\t"
        "v_mfma_f32_32x32x16_bf16 v[0:15], a[100:103], v[188:191], v[0:15]\n\t"
        "v_mfma_f32_32x32x16_bf16 v[16:31], a[212:215], v[188:191], v[16:31]\n\t"
        "s_waitcnt vmcnt(1)\n\t"
        "v_mfma_f32_32x32x16_bf16 v[0:15], a[104:107], v[200:203], v[0:15]\n\t"
        "v_mfma_f32_32x32x16_bf16 v[16:31], a[216:219], v[200:203], v[16:31]\n\t"
        "s_waitcnt vmcnt(0)\n\t"
        "v_mfma_f32_32x32x16_bf16 v[16:31], a[220:223], v[204:207], v[16:31]\n\t"
        "v_mfma_f32_32x32x16_bf16 v[0:15], a[108:111], v[204:207], v[0:15]\n\t"
        "s_cmp_lg_u32 s27, 0\n\t"
        "s_cbranch_scc1 l0_85_%=\n\t"
        "s_waitcnt vmcnt(0)\n\t"
        "v_cmp_ne_u64 vcc, s[10:11], v[232:233]\n\t"
        "s_nop 0\n\t"
        "s_cbranch_vccnz sp_86_%=\n\t"
        "bk_87_%=:\n\t"
        "s_lshl_b32 s38, s25, 2\n\t"
        "s_add_u32 s38, s38, s26\n\t"
        "s_cmp_lg_u32 s38, 3\n\t"
        "s_cbranch_scc1 rl_88_%=\n\t"
        "s_add_u32 s38, s9, 4096\n\t"
        "s_mov_b64 exec, 1\n\t"
        "buffer_atomic_swap_x2 v[234:235], off, s[16:19], s38\n\t"
        "s_mov_b64 exec, -1\n\t"
        "rl_88_%=:\n\t"
        "s_add_u32 s28, s28, 20316160\n\t"
        "s_add_u32 s62, s29, s28\n\t"
        "buffer_load_dwordx4 v[56:59], v231, s[16:19], s62 offen offset:0 sc1\n\t"
        "buffer_load_dwordx4 v[60:63], v231, s[16:19], s62 offen offset:2048 sc1\n\t"
        "s_add_u32 s62, s62, 4096\n\t"
        "buffer_load_dwordx4 v[64:67], v231, s[16:19], s62 offen offset:0 sc1\n\t"
        "buffer_load_dwordx4 v[68:71], v231, s[16:19], s62 offen offset:2048 sc1\n\t"
        "s_add_u32 s62, s62, 4096\n\t"
        "buffer_load_dwordx4 v[72:75], v231, s[16:19], s62 offen offset:0 sc1\n\t"
        "buffer_load_dwordx4 v[76:79], v231, s[16:19], s62 offen offset:2048 sc1\n\t"
        "s_sub_u32 s28, s28, 20316160\n\t"
        "l0_85_%=:\n\t"
        "s_barrier\n\t"
        "ds_write_b128 v242, v[16:19] offset:0\n\t"
        "ds_write_b128 v242, v[20:23] offset:1024\n\t"
        "ds_write_b128 v241, v[24:27] offset:0\n\t"
        "ds_write_b128 v241, v[28:31] offset:1024\n\t"
        "ds_write_b128 v243, v[8:11] offset:0\n\t"
        "ds_write_b128 v243, v[12:15] offset:1024\n\t"
        "s_waitcnt lgkmcnt(0)\n\t"
        "s_barrier\n\t"
        "ds_read_b128 v[112:115], v240 offset:0\n\t"
        "ds_read_b128 v[120:123], v240 offset:2048\n\t"
        "ds_read_b128 v[128:131], v240 offset:4096\n\t"
        "ds_read_b128 v[116:119], v240 offset:1024\n\t"
        "ds_read_b128 v[124:127], v240 offset:3072\n\t"
        "ds_read_b128 v[132:135], v240 offset:5120\n\t"
        "s_waitcnt lgkmcnt(5)\n\t"
        "v_pk_add_f32 v[0:1], v[0:1], v[112:113]\n\t"
        "s_waitcnt lgkmcnt(3)\n\t"
        "v_pk_add_f32 v[120:121], v[120:121], v[128:129]\n\t"
        "v_pk_add_f32 v[2:3], v[2:3], v[114:115]\n\t"
        "v_pk_add_f32 v[122:123], v[122:123], v[130:131]\n\t"
        "s_waitcnt lgkmcnt(2)\n\t"
        "v_pk_add_f32 v[4:5], v[4:5], v[116:117]\n\t"
        "s_waitcnt lgkmcnt(0)\n\t"
        "v_pk_add_f32 v[124:125], v[124:125], v[132:133]\n\t"
        "v_pk_add_f32 v[6:7], v[6:7], v[118:119]\n\t"
        "v_pk_add_f32 v[126:127], v[126:127], v[134:135]\n\t"
        "v_pk_add_f32 v[0:1], v[0:1], v[120:121]\n\t"
        "v_pk_add_f32 v[2:3], v[2:3], v[122:123]\n\t"
        "v_pk_add_f32 v[4:5], v[4:5], v[124:125]\n\t"
        "v_pk_add_f32 v[6:7], v[6:7], v[126:127]\n\t"
        "s_cmp_lg_u32 s27, 0\n\t"
        "s_cbranch_scc1 final_3_%=\n\t"
        "s_add_u32 s47, s57, s28\n\t"
        "s_add_u32 s47, s47, 20373504\n\t"
        "s_add_u32 s33, s58, s28\n\t"
        "s_add_u32 s33, s33, 22165504\n\t"
        "s_mov_b32 s82, s47\n\t"
        "s_mov_b32 s83, s33\n\t"
        "v_pk_add_f32 v[0:1], v[0:1], v[32:33]\n\t"
        "v_pk_add_f32 v[2:3], v[2:3], v[34:35]\n\t"
        "v_pk_add_f32 v[4:5], v[4:5], v[36:37]\n\t"
        "v_pk_add_f32 v[6:7], v[6:7], v[38:39]\n\t"
        "v_cvt_pk_bf16_f32 v252, v0, v1\n\t"
        "v_cvt_pk_bf16_f32 v253, v2, v3\n\t"
        "v_cvt_pk_bf16_f32 v254, v4, v5\n\t"
        "v_cvt_pk_bf16_f32 v255, v6, v7\n\t"
        "buffer_store_dwordx4 v[252:255], v247, s[16:19], s47 offen sc1\n\t"
        "v_pk_add_f32 v[40:41], v[0:1], v[2:3]\n\t"
        "v_pk_mul_f32 v[42:43], v[0:1], v[0:1]\n\t"
        "v_pk_add_f32 v[44:45], v[4:5], v[6:7]\n\t"
        "v_pk_mul_f32 v[46:47], v[2:3], v[2:3]\n\t"
        "v_pk_fma_f32 v[42:43], v[4:5], v[4:5], v[42:43]\n\t"
        "v_pk_fma_f32 v[46:47], v[6:7], v[6:7], v[46:47]\n\t"
        "v_pk_add_f32 v[40:41], v[40:41], v[44:45]\n\t"
        "v_pk_add_f32 v[42:43], v[42:43], v[46:47]\n\t"
        "v_add_f32 v40, v40, v41\n\t"
        "v_add_f32 v41, v42, v43\n\t"
        "v_mov_b32 v42, v40\n\t"
        "v_mov_b32 v43, v41\n\t"
        "s_nop 0\n\t"
        "v_permlane32_swap_b32 v40, v42\n\t"
        "v_permlane32_swap_b32 v41, v43\n\t"
        "v_pk_add_f32 v[40:41], v[40:41], v[42:43]\n\t"
        "v_lshlrev_b32 v44, 1, v239\n\t"
        "s_mov_b64 exec, 0xffffffff\n\t"
        "buffer_store_dwordx2 v[40:41], v44, s[16:19], s33 offen sc1\n\t"
        "s_mov_b64 exec, -1\n\t"
        "next_4_%=:\n\t"
        "s_mov_b32 s27, 1\n\t"
        "s_mov_b64 s[84:85], 0\n\t"
        "s_add_u32 s28, s28, 20316160\n\t"
        "s_branch layer_1_%=\n\t"
        "idle_2_%=:\n\t"
        "s_memtime s[6:7]\n\t"
        "s_mov_b32 s82, 368640\n\t"
        "s_mov_b32 s83, 368640\n\t"
        "s_mul_i32 s38, s27, 147456\n\t"
        "s_lshl_b32 s39, s25, 11\n\t"
        "s_lshl_b32 s40, s24, 3\n\t"
        "v_lshlrev_b32 v236, 4, v238\n\t"
        "s_add_u32 s38, s38, s39\n\t"
        "s_add_u32 s38, s38, s40\n\t"
        "s_add_u32 s38, s38, 106496\n\t"
        "s_waitcnt vmcnt(0)\n\t"
        "s_barrier\n\t"
        "s_cmp_lg_u32 s26, 0\n\t"
        "s_cbranch_scc1 sig_91_%=\n\t"
        "s_mov_b64 exec, 0xffff\n\t"
        "buffer_atomic_swap_x2 v[234:235], v236, s[16:19], s38 offen\n\t"
        "s_mov_b64 exec, -1\n\t"
        "sig_91_%=:\n\t"
        "s_cmp_lg_u32 s27, 0\n\t"
        "s_cbranch_scc1 kfin_%=\n\t"
        "v_lshrrev_b32 v0, 2, v239\n\t"
        "v_min_u32 v0, 47, v0\n\t"
        "v_mul_u32_u24 v1, 43, v0\n\t"
        "v_lshrrev_b32 v1, 7, v1\n\t"
        "v_mul_u32_u24 v2, 104, v1\n\t"
        "v_lshl_add_u32 v236, v0, 3, v2\n\t"
        "s_lshl_b32 s9, s24, 11\n\t"
        "s_add_u32 s9, s9, 303136\n\t"
        "s_mov_b32 s42, 400000\n\t"
        "pl_92_%=:\n\t"
        "buffer_load_dwordx2 v[232:233], v236, s[16:19], s9 offen sc1\n\t"
        "s_waitcnt vmcnt(0)\n\t"
        "v_cmp_ne_u64 vcc, s[10:11], v[232:233]\n\t"
        "s_nop 0\n\t"
        "s_cbranch_vccz pd_93_%=\n\t"
        "s_sub_u32 s42, s42, 1\n\t"
        "s_cbranch_scc0 pl_92_%=\n\t"
        "pd_93_%=:\n\t"
        "s_lshr_b32 s38, s46, 3\n\t"
        "s_and_b32 s38, s38, 3\n\t"
        "s_lshl_b32 s38, s38, 2\n\t"
        "s_add_u32 s38, s38, s26\n\t"
        "s_mul_i32 s47, s38, 86016\n\t"
        "s_add_u32 s47, s47, 368640\n\t"
        "s_mov_b32 s42, 42\n\t"
        "iw_94_%=:\n\t"
        "buffer_load_dwordx4 a[224:227], v247, s[16:19], s47 offen sc1\n\t"
        "buffer_load_dwordx4 a[224:227], v247, s[16:19], s47 offen offset:1024 sc1\n\t"
        "s_waitcnt vmcnt(24)\n\t"
        "s_add_u32 s47, s47, 2048\n\t"
        "s_sub_u32 s42, s42, 1\n\t"
        "s_cmp_lg_u32 s42, 0\n\t"
        "s_cbranch_scc1 iw_94_%=\n\t"
        "s_mul_i32 s35, s25, 86016\n\t"
        "s_mul_i32 s41, s26, 21504\n\t"
        "s_add_u32 s35, s35, s41\n\t"
        "s_add_u32 s35, s35, 368640\n\t"
        "s_mov_b32 m0, s41\n\t"
        "s_nop 0\n\t"
        "buffer_load_dwordx4 v247, s[16:19], s35 offen offset:0 sc1 lds\n\t"
        "buffer_load_dwordx4 v247, s[16:19], s35 offen offset:1024 sc1 lds\n\t"
        "buffer_load_dwordx4 v247, s[16:19], s35 offen offset:2048 sc1 lds\n\t"
        "buffer_load_dwordx4 v247, s[16:19], s35 offen offset:3072 sc1 lds\n\t"
        "s_add_u32 s35, s35, 4096\n\t"
        "s_add_u32 m0, m0, 4096\n\t"
        "s_nop 0\n\t"
        "buffer_load_dwordx4 v247, s[16:19], s35 offen offset:0 sc1 lds\n\t"
        "buffer_load_dwordx4 v247, s[16:19], s35 offen offset:1024 sc1 lds\n\t"
        "buffer_load_dwordx4 v247, s[16:19], s35 offen offset:2048 sc1 lds\n\t"
        "buffer_load_dwordx4 v247, s[16:19], s35 offen offset:3072 sc1 lds\n\t"
        "s_add_u32 s35, s35, 4096\n\t"
        "s_add_u32 m0, m0, 4096\n\t"
        "s_nop 0\n\t"
        "buffer_load_dwordx4 v247, s[16:19], s35 offen offset:0 sc1 lds\n\t"
        "buffer_load_dwordx4 v247, s[16:19], s35 offen offset:1024 sc1 lds\n\t"
        "buffer_load_dwordx4 v247, s[16:19], s35 offen offset:2048 sc1 lds\n\t"
        "buffer_load_dwordx4 v247, s[16:19], s35 offen offset:3072 sc1 lds\n\t"
        "s_add_u32 s35, s35, 4096\n\t"
        "s_add_u32 m0, m0, 4096\n\t"
        "s_nop 0\n\t"
        "buffer_load_dwordx4 v247, s[16:19], s35 offen offset:0 sc1 lds\n\t"
        "buffer_load_dwordx4 v247, s[16:19], s35 offen offset:1024 sc1 lds\n\t"
        "buffer_load_dwordx4 v247, s[16:19], s35 offen offset:2048 sc1 lds\n\t"
        "buffer_load_dwordx4 v247, s[16:19], s35 offen offset:3072 sc1 lds\n\t"
        "s_add_u32 s35, s35, 4096\n\t"
        "s_add_u32 m0, m0, 4096\n\t"
        "s_nop 0\n\t"
        "buffer_load_dwordx4 v247, s[16:19], s35 offen offset:0 sc1 lds\n\t"
        "buffer_load_dwordx4 v247, s[16:19], s35 offen offset:1024 sc1 lds\n\t"
        "buffer_load_dwordx4 v247, s[16:19], s35 offen offset:2048 sc1 lds\n\t"
        "buffer_load_dwordx4 v247, s[16:19], s35 offen offset:3072 sc1 lds\n\t"
        "s_add_u32 s35, s35, 4096\n\t"
        "s_add_u32 m0, m0, 4096\n\t"
        "s_nop 0\n\t"
        "buffer_load_dwordx4 v247, s[16:19], s35 offen offset:0 sc1 lds\n\t"
        "s_waitcnt vmcnt(0)\n\t"
        "s_barrier\n\t"
        "it_95_%=:\n\t"
        "s_sleep 8\n\t"
        "s_memtime s[48:49]\n\t"
        "s_waitcnt lgkmcnt(0)\n\t"
        "s_sub_u32 s48, s48, s6\n\t"
        "s_cmp_lt_u32 s48, 15000\n\t"
        "s_cbranch_scc1 it_95_%=\n\t"
        "s_cmp_lg_u32 s26, 0\n\t"
        "s_cbranch_scc1 one_96_%=\n\t"
        "s_mul_i32 s9, s27, 147456\n\t"
        "s_add_u32 s9, s9, s14\n\t"
        "s_add_u32 s9, s9, 151552\n\t"
        "v_mov_b32 v236, 0\n\t"
        "s_mov_b32 s42, 400000\n\t"
        "pl_97_%=:\n\t"
        "buffer_load_dwordx2 v[232:233], v236, s[16:19], s9 offen sc1\n\t"
        "s_waitcnt vmcnt(0)\n\t"
        "v_cmp_ne_u64 vcc, s[10:11], v[232:233]\n\t"
        "s_nop 0\n\t"
        "s_cbranch_vccz pd_98_%=\n\t"
        "s_sleep 3\n\t"
        "s_sub_u32 s42, s42, 1\n\t"
        "s_cbranch_scc0 pl_97_%=\n\t"
        "pd_98_%=:\n\t"
        "one_96_%=:\n\t"
        "s_barrier\n\t"
        "s_add_u32 s28, s28, 20316160\n\t"
        "s_add_u32 s62, s29, s28\n\t"
        "buffer_load_dwordx4 v[56:59], v231, s[16:19], s62 offen offset:0 sc1\n\t"
        "buffer_load_dwordx4 v[60:63], v231, s[16:19], s62 offen offset:2048 sc1\n\t"
        "s_add_u32 s62, s62, 4096\n\t"
        "buffer_load_dwordx4 v[64:67], v231, s[16:19], s62 offen offset:0 sc1\n\t"
        "buffer_load_dwordx4 v[68:71], v231, s[16:19], s62 offen offset:2048 sc1\n\t"
        "s_add_u32 s62, s62, 4096\n\t"
        "buffer_load_dwordx4 v[72:75], v231, s[16:19], s62 offen offset:0 sc1\n\t"
        "buffer_load_dwordx4 v[76:79], v231, s[16:19], s62 offen offset:2048 sc1\n\t"
        "s_sub_u32 s28, s28, 20316160\n\t"
        "s_waitcnt vmcnt(0)\n\t"
        "s_branch next_4_%=\n\t"
        "fbk_51_%=:\n\t"
        "s_mov_b32 s43, 1\n\t"
        "s_waitcnt vmcnt(0) lgkmcnt(0)\n\t"
        "s_lshl_b32 s36, s24, 18\n\t"
        "s_add_u32 s36, s36, s28\n\t"
        "s_add_u32 s36, s36, 4390912\n\t"
        "s_mov_b32 s41, s36\n\t"
        "s_mov_b32 s42, 64\n\t"
        "v_mov_b32 v0, 0xff61b1e6\n\t"
        "fb1_99_%=:\n\t"
        "buffer_load_dwordx4 v[16:19], v247, s[16:19], s41 offen sc1\n\t"
        "buffer_load_dwordx4 v[20:23], v247, s[16:19], s41 offen offset:1024 sc1\n\t"
        "s_add_u32 s41, s41, 4096\n\t"
        "s_waitcnt vmcnt(1)\n\t"
        "v_mfma_f32_32x32x16_bf16 v[160:175], v[16:19], v[64:67], 0\n\t"
        "s_waitcnt vmcnt(0)\n\t"
        "v_mfma_f32_32x32x16_bf16 v[160:175], v[20:23], v[68:71], v[160:175]\n\t"
        "s_nop 11\n\t"
        "v_max3_f32 v0, v0, v160, v161\n\t"
        "v_max3_f32 v0, v0, v162, v163\n\t"
        "v_max3_f32 v0, v0, v164, v165\n\t"
        "v_max3_f32 v0, v0, v166, v167\n\t"
        "v_max3_f32 v0, v0, v168, v169\n\t"
        "v_max3_f32 v0, v0, v170, v171\n\t"
        "v_max3_f32 v0, v0, v172, v173\n\t"
        "v_max3_f32 v0, v0, v174, v175\n\t"
        "s_sub_u32 s42, s42, 1\n\t"
        "s_cmp_lg_u32 s42, 0\n\t"
        "s_cbranch_scc1 fb1_99_%=\n\t"
        "v_mov_b32 v1, v0\n\t"
        "s_nop 1\n\t"
        "v_permlane32_swap_b32 v0, v1\n\t"
        "v_max_f32 v0, v0, v1\n\t"
        "v_sub_f32 v0, 0, v0\n\t"
        "v_cvt_pk_bf16_f32 v2, v0, v0\n\t"
        "v_and_b32 v3, 0xffff0000, v2\n\t"
        "v_sub_f32 v0, v0, v3\n\t"
        "v_cvt_pk_bf16_f32 v4, v0, v0\n\t"
        "v_and_b32 v5, 0xffff0000, v4\n\t"
        "v_sub_f32 v0, v0, v5\n\t"
        "v_mov_b32 v6, 0\n\t"
        "v_cvt_pk_bf16_f32 v6, v0, v6\n\t"
        "v_and_b32 v2, 0xffff, v2\n\t"
        "v_or_b32 v2, v2, v5\n\t"
        "v_cmp_lt_u32 vcc, 15, v244\n\t"
        "s_nop 1\n\t"
        "v_cndmask_b32 v70, v70, v2, vcc\n\t"
        "v_cndmask_b32 v71, v71, v6, vcc\n\t"
        "v_mov_b32 v176, 0\n\t"
        "v_mov_b32 v177, 0\n\t"
        "v_mov_b32 v178, 0\n\t"
        "v_mov_b32 v179, 0\n\t"
        "v_mov_b32 v180, 0\n\t"
        "v_mov_b32 v181, 0\n\t"
        "v_mov_b32 v182, 0\n\t"
        "v_mov_b32 v183, 0\n\t"
        "v_mov_b32 v184, 0\n\t"
        "v_mov_b32 v185, 0\n\t"
        "v_mov_b32 v186, 0\n\t"
        "v_mov_b32 v187, 0\n\t"
        "v_mov_b32 v188, 0\n\t"
        "v_mov_b32 v189, 0\n\t"
        "v_mov_b32 v190, 0\n\t"
        "v_mov_b32 v191, 0\n\t"
        "s_mov_b32 s41, s36\n\t"
        "s_mov_b32 s42, 64\n\t"
        "fb2_100_%=:\n\t"
        "buffer_load_dwordx4 v[16:19], v247, s[16:19], s41 offen offset:0 sc1\n\t"
        "buffer_load_dwordx4 v[20:23], v247, s[16:19], s41 offen offset:1024 sc1\n\t"
        "buffer_load_dwordx4 v[24:27], v246, s[16:19], s41 offen offset:2048 sc1\n\t"
        "buffer_load_dwordx4 v[28:31], v246, s[16:19], s41 offen offset:3072 sc1\n\t"
        "s_add_u32 s41, s41, 4096\n\t"
        "s_waitcnt vmcnt(3)\n\t"
        "v_mfma_f32_32x32x16_bf16 v[160:175], v[16:19], v[64:67], 0\n\t"
        "s_waitcnt vmcnt(2)\n\t"
        "v_mfma_f32_32x32x16_bf16 v[160:175], v[20:23], v[68:71], v[160:175]\n\t"
        "s_nop 11\n\t"
        "v_exp_f32 v160, v160\n\t"
        "v_exp_f32 v161, v161\n\t"
        "v_exp_f32 v162, v162\n\t"
        "v_exp_f32 v163, v163\n\t"
        "v_exp_f32 v164, v164\n\t"
        "v_exp_f32 v165, v165\n\t"
        "v_exp_f32 v166, v166\n\t"
        "v_exp_f32 v167, v167\n\t"
        "v_exp_f32 v168, v168\n\t"
        "v_exp_f32 v169, v169\n\t"
        "v_exp_f32 v170, v170\n\t"
        "v_exp_f32 v171, v171\n\t"
        "v_exp_f32 v172, v172\n\t"
        "v_exp_f32 v173, v173\n\t"
        "v_exp_f32 v174, v174\n\t"
        "v_exp_f32 v175, v175\n\t"
        "v_cvt_pk_bf16_f32 v192, v160, v161\n\t"
        "v_cvt_pk_bf16_f32 v193, v162, v163\n\t"
        "v_cvt_pk_bf16_f32 v194, v164, v165\n\t"
        "v_cvt_pk_bf16_f32 v195, v166, v167\n\t"
        "v_cvt_pk_bf16_f32 v196, v168, v169\n\t"
        "v_cvt_pk_bf16_f32 v197, v170, v171\n\t"
        "v_cvt_pk_bf16_f32 v198, v172, v173\n\t"
        "v_cvt_pk_bf16_f32 v199, v174, v175\n\t"
        "s_waitcnt vmcnt(1)\n\t"
        "v_mfma_f32_32x32x16_bf16 v[176:191], v[24:27], v[192:195], v[176:191]\n\t"
        "s_waitcnt vmcnt(0)\n\t"
        "v_mfma_f32_32x32x16_bf16 v[176:191], v[28:31], v[196:199], v[176:191]\n\t"
        "s_sub_u32 s42, s42, 1\n\t"
        "s_cmp_lg_u32 s42, 0\n\t"
        "s_nop 11\n\t"
        "s_cbranch_scc1 fb2_100_%=\n\t"
        "v_cndmask_b32 v160, v188, v180, s[52:53]\n\t"
        "v_mov_b32 v0, v176\n\t"
        "v_mov_b32 v1, v177\n\t"
        "v_mov_b32 v2, v178\n\t"
        "v_mov_b32 v3, v179\n\t"
        "v_mov_b32 v4, v180\n\t"
        "v_mov_b32 v5, v181\n\t"
        "v_mov_b32 v6, v182\n\t"
        "v_mov_b32 v7, v183\n\t"
        "v_mov_b32 v184, v160\n\t"
        "s_nop 1\n\t"
        "v_permlane32_swap_b32 v160, v184\n\t"
        "s_branch norm_50_%=\n\t"
        "fbk_57_%=:\n\t"
        "s_mov_b32 s43, 1\n\t"
        "s_waitcnt vmcnt(0) lgkmcnt(0)\n\t"
        "s_lshl_b32 s36, s24, 18\n\t"
        "s_add_u32 s36, s36, s28\n\t"
        "s_add_u32 s36, s36, 4390912\n\t"
        "s_mov_b32 s41, s36\n\t"
        "s_mov_b32 s42, 64\n\t"
        "v_mov_b32 v32, 0xff61b1e6\n\t"
        "fb1_101_%=:\n\t"
        "buffer_load_dwordx4 v[48:51], v247, s[16:19], s41 offen sc1\n\t"
        "buffer_load_dwordx4 v[52:55], v247, s[16:19], s41 offen offset:1024 sc1\n\t"
        "s_add_u32 s41, s41, 4096\n\t"
        "s_waitcnt vmcnt(1)\n\t"
        "v_mfma_f32_32x32x16_bf16 v[160:175], v[48:51], v[80:83], 0\n\t"
        "s_waitcnt vmcnt(0)\n\t"
        "v_mfma_f32_32x32x16_bf16 v[160:175], v[52:55], v[84:87], v[160:175]\n\t"
        "s_nop 11\n\t"
        "v_max3_f32 v32, v32, v160, v161\n\t"
        "v_max3_f32 v32, v32, v162, v163\n\t"
        "v_max3_f32 v32, v32, v164, v165\n\t"
        "v_max3_f32 v32, v32, v166, v167\n\t"
        "v_max3_f32 v32, v32, v168, v169\n\t"
        "v_max3_f32 v32, v32, v170, v171\n\t"
        "v_max3_f32 v32, v32, v172, v173\n\t"
        "v_max3_f32 v32, v32, v174, v175\n\t"
        "s_sub_u32 s42, s42, 1\n\t"
        "s_cmp_lg_u32 s42, 0\n\t"
        "s_cbranch_scc1 fb1_101_%=\n\t"
        "v_mov_b32 v33, v32\n\t"
        "s_nop 1\n\t"
        "v_permlane32_swap_b32 v32, v33\n\t"
        "v_max_f32 v32, v32, v33\n\t"
        "v_sub_f32 v32, 0, v32\n\t"
        "v_cvt_pk_bf16_f32 v34, v32, v32\n\t"
        "v_and_b32 v35, 0xffff0000, v34\n\t"
        "v_sub_f32 v32, v32, v35\n\t"
        "v_cvt_pk_bf16_f32 v36, v32, v32\n\t"
        "v_and_b32 v37, 0xffff0000, v36\n\t"
        "v_sub_f32 v32, v32, v37\n\t"
        "v_mov_b32 v38, 0\n\t"
        "v_cvt_pk_bf16_f32 v38, v32, v38\n\t"
        "v_and_b32 v34, 0xffff, v34\n\t"
        "v_or_b32 v34, v34, v37\n\t"
        "v_cmp_lt_u32 vcc, 15, v244\n\t"
        "s_nop 1\n\t"
        "v_cndmask_b32 v86, v86, v34, vcc\n\t"
        "v_cndmask_b32 v87, v87, v38, vcc\n\t"
        "v_mov_b32 v176, 0\n\t"
        "v_mov_b32 v177, 0\n\t"
        "v_mov_b32 v178, 0\n\t"
        "v_mov_b32 v179, 0\n\t"
        "v_mov_b32 v180, 0\n\t"
        "v_mov_b32 v181, 0\n\t"
        "v_mov_b32 v182, 0\n\t"
        "v_mov_b32 v183, 0\n\t"
        "v_mov_b32 v184, 0\n\t"
        "v_mov_b32 v185, 0\n\t"
        "v_mov_b32 v186, 0\n\t"
        "v_mov_b32 v187, 0\n\t"
        "v_mov_b32 v188, 0\n\t"
        "v_mov_b32 v189, 0\n\t"
        "v_mov_b32 v190, 0\n\t"
        "v_mov_b32 v191, 0\n\t"
        "s_mov_b32 s41, s36\n\t"
        "s_mov_b32 s42, 64\n\t"
        "fb2_102_%=:\n\t"
        "buffer_load_dwordx4 v[48:51], v247, s[16:19], s41 offen offset:0 sc1\n\t"
        "buffer_load_dwordx4 v[52:55], v247, s[16:19], s41 offen offset:1024 sc1\n\t"
        "buffer_load_dwordx4 v[56:59], v246, s[16:19], s41 offen offset:2048 sc1\n\t"
        "buffer_load_dwordx4 v[60:63], v246, s[16:19], s41 offen offset:3072 sc1\n\t"
        "s_add_u32 s41, s41, 4096\n\t"
        "s_waitcnt vmcnt(3)\n\t"
        "v_mfma_f32_32x32x16_bf16 v[160:175], v[48:51], v[80:83], 0\n\t"
        "s_waitcnt vmcnt(2)\n\t"
        "v_mfma_f32_32x32x16_bf16 v[160:175], v[52:55], v[84:87], v[160:175]\n\t"
        "s_nop 11\n\t"
        "v_exp_f32 v160, v160\n\t"
        "v_exp_f32 v161, v161\n\t"
        "v_exp_f32 v162, v162\n\t"
        "v_exp_f32 v163, v163\n\t"
        "v_exp_f32 v164, v164\n\t"
        "v_exp_f32 v165, v165\n\t"
        "v_exp_f32 v166, v166\n\t"
        "v_exp_f32 v167, v167\n\t"
        "v_exp_f32 v168, v168\n\t"
        "v_exp_f32 v169, v169\n\t"
        "v_exp_f32 v170, v170\n\t"
        "v_exp_f32 v171, v171\n\t"
        "v_exp_f32 v172, v172\n\t"
        "v_exp_f32 v173, v173\n\t"
        "v_exp_f32 v174, v174\n\t"
        "v_exp_f32 v175, v175\n\t"
        "v_cvt_pk_bf16_f32 v192, v160, v161\n\t"
        "v_cvt_pk_bf16_f32 v193, v162, v163\n\t"
        "v_cvt_pk_bf16_f32 v194, v164, v165\n\t"
        "v_cvt_pk_bf16_f32 v195, v166, v167\n\t"
        "v_cvt_pk_bf16_f32 v196, v168, v169\n\t"
        "v_cvt_pk_bf16_f32 v197, v170, v171\n\t"
        "v_cvt_pk_bf16_f32 v198, v172, v173\n\t"
        "v_cvt_pk_bf16_f32 v199, v174, v175\n\t"
        "s_waitcnt vmcnt(1)\n\t"
        "v_mfma_f32_32x32x16_bf16 v[176:191], v[56:59], v[192:195], v[176:191]\n\t"
        "s_waitcnt vmcnt(0)\n\t"
        "v_mfma_f32_32x32x16_bf16 v[176:191], v[60:63], v[196:199], v[176:191]\n\t"
        "s_sub_u32 s42, s42, 1\n\t"
        "s_cmp_lg_u32 s42, 0\n\t"
        "s_nop 11\n\t"
        "s_cbranch_scc1 fb2_102_%=\n\t"
        "v_cndmask_b32 v160, v188, v180, s[52:53]\n\t"
        "v_mov_b32 v32, v176\n\t"
        "v_mov_b32 v33, v177\n\t"
        "v_mov_b32 v34, v178\n\t"
        "v_mov_b32 v35, v179\n\t"
        "v_mov_b32 v36, v180\n\t"
        "v_mov_b32 v37, v181\n\t"
        "v_mov_b32 v38, v182\n\t"
        "v_mov_b32 v39, v183\n\t"
        "v_mov_b32 v184, v160\n\t"
        "s_nop 1\n\t"
        "v_permlane32_swap_b32 v160, v184\n\t"
        "s_branch norm_56_%=\n\t"
        "sp_16_%=:\n\t"
        "s_mov_b32 s42, 400000\n\t"
        "sl_103_%=:\n\t"
        "buffer_load_dwordx2 v[232:233], v238, s[16:19], s9 offen sc1\n\t"
        "s_waitcnt vmcnt(0)\n\t"
        "v_cmp_ne_u64 vcc, s[10:11], v[232:233]\n\t"
        "s_nop 0\n\t"
        "s_cbranch_vccz bk_17_%=\n\t"
        "s_sub_u32 s42, s42, 1\n\t"
        "s_cbranch_scc0 sl_103_%=\n\t"
        "s_branch bk_17_%=\n\t"
        "sp_28_%=:\n\t"
        "s_mov_b32 s42, 400000\n\t"
        "sl_104_%=:\n\t"
        "buffer_load_dwordx2 v[232:233], v237, s[16:19], s9 offen sc1\n\t"
        "s_waitcnt vmcnt(0)\n\t"
        "v_cmp_ne_u64 vcc, s[10:11], v[232:233]\n\t"
        "s_nop 0\n\t"
        "s_cbranch_vccz bk_29_%=\n\t"
        "s_sub_u32 s42, s42, 1\n\t"
        "s_cbranch_scc0 sl_104_%=\n\t"
        "s_branch bk_29_%=\n\t"
        "sp_31_%=:\n\t"
        "s_mov_b32 s42, 400000\n\t"
        "sl_105_%=:\n\t"
        "buffer_load_dwordx2 v[232:233], v238, s[16:19], s9 offen sc1\n\t"
        "s_waitcnt vmcnt(0)\n\t"
        "v_cmp_ne_u64 vcc, s[10:11], v[232:233]\n\t"
        "s_nop 0\n\t"
        "s_cbranch_vccz bk_32_%=\n\t"
        "s_sub_u32 s42, s42, 1\n\t"
        "s_cbranch_scc0 sl_105_%=\n\t"
        "s_branch bk_32_%=\n\t"
        "sp_36_%=:\n\t"
        "s_mov_b32 s42, 400000\n\t"
        "sl_106_%=:\n\t"
        "buffer_load_dwordx2 v[232:233], v238, s[16:19], s9 offen sc1\n\t"
        "s_waitcnt vmcnt(0)\n\t"
        "v_cmp_ne_u64 vcc, s[10:11], v[232:233]\n\t"
        "s_nop 0\n\t"
        "s_cbranch_vccz bk_37_%=\n\t"
        "s_sub_u32 s42, s42, 1\n\t"
        "s_cbranch_scc0 sl_106_%=\n\t"
        "s_branch bk_37_%=\n\t"
        "sp_40_%=:\n\t"
        "s_mov_b32 s42, 400000\n\t"
        "sl_107_%=:\n\t"
        "buffer_load_dwordx2 v[232:233], v237, s[16:19], s9 offen sc1\n\t"
        "s_waitcnt vmcnt(0)\n\t"
        "v_cmp_ne_u64 vcc, s[10:11], v[232:233]\n\t"
        "s_nop 0\n\t"
        "s_cbranch_vccz bk_41_%=\n\t"
        "s_sub_u32 s42, s42, 1\n\t"
        "s_cbranch_scc0 sl_107_%=\n\t"
        "s_branch bk_41_%=\n\t"
        "sp_53_%=:\n\t"
        "s_mov_b32 s42, 400000\n\t"
        "sl_108_%=:\n\t"
        "buffer_load_dwordx2 v[232:233], v238, s[16:19], s9 offen sc1\n\t"
        "s_waitcnt vmcnt(0)\n\t"
        "v_cmp_ne_u64 vcc, s[10:11], v[232:233]\n\t"
        "s_nop 0\n\t"
        "s_cbranch_vccz bk_54_%=\n\t"
        "s_sub_u32 s42, s42, 1\n\t"
        "s_cbranch_scc0 sl_108_%=\n\t"
        "s_branch bk_54_%=\n\t"
        "sp_63_%=:\n\t"
        "s_mov_b32 s42, 400000\n\t"
        "sl_109_%=:\n\t"
        "buffer_load_dwordx2 v[232:233], v237, s[16:19], s9 offen sc1\n\t"
        "s_waitcnt vmcnt(0)\n\t"
        "v_cmp_ne_u64 vcc, s[10:11], v[232:233]\n\t"
        "s_nop 0\n\t"
        "s_cbranch_vccz bk_64_%=\n\t"
        "s_sub_u32 s42, s42, 1\n\t"
        "s_cbranch_scc0 sl_109_%=\n\t"
        "s_branch bk_64_%=\n\t"
        "sp_66_%=:\n\t"
        "s_mov_b32 s42, 400000\n\t"
        "sl_110_%=:\n\t"
        "buffer_load_dwordx2 v[232:233], v237, s[16:19], s9 offen sc1\n\t"
        "s_waitcnt vmcnt(0)\n\t"
        "v_cmp_ne_u64 vcc, s[10:11], v[232:233]\n\t"
        "s_nop 0\n\t"
        "s_cbranch_vccz bk_67_%=\n\t"
        "s_sub_u32 s42, s42, 1\n\t"
        "s_cbranch_scc0 sl_110_%=\n\t"
        "s_branch bk_67_%=\n\t"
        "sp_69_%=:\n\t"
        "s_mov_b32 s42, 400000\n\t"
        "sl_111_%=:\n\t"
        "buffer_load_dwordx2 v[232:233], v237, s[16:19], s9 offen sc1\n\t"
        "s_waitcnt vmcnt(0)\n\t"
        "v_cmp_ne_u64 vcc, s[10:11], v[232:233]\n\t"
        "s_nop 0\n\t"
        "s_cbranch_vccz bk_70_%=\n\t"
        "s_sub_u32 s42, s42, 1\n\t"
        "s_cbranch_scc0 sl_111_%=\n\t"
        "s_branch bk_70_%=\n\t"
        "sp_74_%=:\n\t"
        "s_mov_b32 s42, 400000\n\t"
        "sl_112_%=:\n\t"
        "buffer_load_dwordx2 v[232:233], v236, s[16:19], s9 offen sc1\n\t"
        "s_waitcnt vmcnt(0)\n\t"
        "v_cmp_ne_u64 vcc, s[10:11], v[232:233]\n\t"
        "s_nop 0\n\t"
        "s_cbranch_vccz bk_75_%=\n\t"
        "s_sub_u32 s42, s42, 1\n\t"
        "s_cbranch_scc0 sl_112_%=\n\t"
        "s_branch bk_75_%=\n\t"
        "sp_78_%=:\n\t"
        "s_mov_b32 s42, 400000\n\t"
        "sl_113_%=:\n\t"
        "buffer_load_dwordx2 v[232:233], v237, s[16:19], s9 offen sc1\n\t"
        "s_waitcnt vmcnt(0)\n\t"
        "v_cmp_ne_u64 vcc, s[10:11], v[232:233]\n\t"
        "s_nop 0\n\t"
        "s_cbranch_vccz bk_79_%=\n\t"
        "s_sub_u32 s42, s42, 1\n\t"
        "s_cbranch_scc0 sl_113_%=\n\t"
        "s_branch bk_79_%=\n\t"
        "sp_86_%=:\n\t"
        "s_mov_b32 s42, 400000\n\t"
        "sl_114_%=:\n\t"
        "buffer_load_dwordx2 v[232:233], v237, s[16:19], s9 offen sc1\n\t"
        "s_waitcnt vmcnt(0)\n\t"
        "v_cmp_ne_u64 vcc, s[10:11], v[232:233]\n\t"
        "s_nop 0\n\t"
        "s_cbranch_vccz bk_87_%=\n\t"
        "s_sub_u32 s42, s42, 1\n\t"
        "s_cbranch_scc0 sl_114_%=\n\t"
        "s_branch bk_87_%=\n\t"
        "final_3_%=:\n\t"
        "v_pk_add_f32 v[0:1], v[0:1], v[32:33]\n\t"
        "v_pk_add_f32 v[2:3], v[2:3], v[34:35]\n\t"
        "v_pk_add_f32 v[4:5], v[4:5], v[36:37]\n\t"
        "v_pk_add_f32 v[6:7], v[6:7], v[38:39]\n\t"
        "v_cvt_pk_bf16_f32 v252, v0, v1\n\t"
        "v_cvt_pk_bf16_f32 v253, v2, v3\n\t"
        "v_cvt_pk_bf16_f32 v254, v4, v5\n\t"
        "v_cvt_pk_bf16_f32 v255, v6, v7\n\t"
        "v_and_b32 v40, 0x7c, v239\n\t"
        "v_mul_u32_u24 v40, 224, v40\n\t"
        "s_add_u32 s47, s15, 57344\n\t"
        "v_add_u32 v40, v40, v244\n\t"
        "v_permlane32_swap_b32 v252, v254\n\t"
        "v_permlane32_swap_b32 v253, v255\n\t"
        "buffer_store_dwordx4 v[252:255], v40, s[20:23], s47 offen\n\t"
        "kfin_%=:\n\t"
        "s_waitcnt vmcnt(0) lgkmcnt(0)\n\t"
        "kend_%=:\n\t"
        :
        : "s"(__builtin_amdgcn_kernarg_segment_ptr()), "s"(blockIdx.x), "v"(threadIdx.x)
        : "v1", "v2", "v3", "v4", "v5", "v6", "v7", "v8", "v9", "v10", "v11", "v12", "v13", "v14", "v15", "v16", "v17", "v18", "v19", "v20", "v21", "v22", "v23", "v24", "v25", "v26", "v27", "v28", "v29", "v30", "v31", "v32", "v33", "v34", "v35", "v36", "v37", "v38", "v39", "v40", "v41", "v42", "v43", "v44", "v45", "v46", "v47", "v48", "v49", "v50", "v51", "v52", "v53", "v54", "v55", "v56", "v57", "v58", "v59", "v60", "v61", "v62", "v63", "v64", "v65", "v66", "v67", "v68", "v69", "v70", "v71", "v72", "v73", "v74", "v75", "v76", "v77", "v78", "v79", "v80", "v81", "v82", "v83", "v84", "v85", "v86", "v87", "v88", "v89", "v90", "v91", "v92", "v93", "v94", "v95", "v96", "v97", "v98", "v99", "v100", "v101", "v102", "v103", "v104", "v105", "v106", "v107", "v108", "v109", "v110", "v111", "v112", "v113", "v114", "v115", "v116", "v117", "v118", "v119", "v120", "v121", "v122", "v123", "v124", "v125", "v126", "v127", "v128", "v129", "v130", "v131", "v132", "v133", "v134", "v135", "v136", "v137", "v138", "v139", "v140", "v141", "v142", "v143", "v144", "v145", "v146", "v147", "v148", "v149", "v150", "v151", "v152", "v153", "v154", "v155", "v156", "v157", "v158", "v159", "v160", "v161", "v162", "v163", "v164", "v165", "v166", "v167", "v168", "v169", "v170", "v171", "v172", "v173", "v174", "v175", "v176", "v177", "v178", "v179", "v180", "v181", "v182", "v183", "v184", "v185", "v186", "v187", "v188", "v189", "v190", "v191", "v192", "v193", "v194", "v195", "v196", "v197", "v198", "v199", "v200", "v201", "v202", "v203", "v204", "v205", "v206", "v207", "v208", "v209", "v210", "v211", "v212", "v213", "v214", "v215", "v216", "v217", "v218", "v219", "v220", "v221", "v222", "v223", "v224", "v225", "v226", "v227", "v228", "v229", "v230", "v231", "v232", "v233", "v234", "v235", "v236", "v237", "v238", "v239", "v240", "v241", "v242", "v243", "v244", "v245", "v246", "v247", "v248", "v249", "v250", "v251", "v252", "v253", "v254", "v255", "a0", "a1", "a2", "a3", "a4", "a5", "a6", "a7", "a8", "a9", "a10", "a11", "a12", "a13", "a14", "a15", "a16", "a17", "a18", "a19", "a20", "a21", "a22", "a23", "a24", "a25", "a26", "a27", "a28", "a29", "a30", "a31", "a32", "a33", "a34", "a35", "a36", "a37", "a38", "a39", "a40", "a41", "a42", "a43", "a44", "a45", "a46", "a47", "a48", "a49", "a50", "a51", "a52", "a53", "a54", "a55", "a56", "a57", "a58", "a59", "a60", "a61", "a62", "a63", "a64", "a65", "a66", "a67", "a68", "a69", "a70", "a71", "a72", "a73", "a74", "a75", "a76", "a77", "a78", "a79", "a80", "a81", "a82", "a83", "a84", "a85", "a86", "a87", "a88", "a89", "a90", "a91", "a92", "a93", "a94", "a95", "a96", "a97", "a98", "a99", "a100", "a101", "a102", "a103", "a104", "a105", "a106", "a107", "a108", "a109", "a110", "a111", "a112", "a113", "a114", "a115", "a116", "a117", "a118", "a119", "a120", "a121", "a122", "a123", "a124", "a125", "a126", "a127", "a128", "a129", "a130", "a131", "a132", "a133", "a134", "a135", "a136", "a137", "a138", "a139", "a140", "a141", "a142", "a143", "a144", "a145", "a146", "a147", "a148", "a149", "a150", "a151", "a152", "a153", "a154", "a155", "a156", "a157", "a158", "a159", "a160", "a161", "a162", "a163", "a164", "a165", "a166", "a167", "a168", "a169", "a170", "a171", "a172", "a173", "a174", "a175", "a176", "a177", "a178", "a179", "a180", "a181", "a182", "a183", "a184", "a185", "a186", "a187", "a188", "a189", "a190", "a191", "a192", "a193", "a194", "a195", "a196", "a197", "a198", "a199", "a200", "a201", "a202", "a203", "a204", "a205", "a206", "a207", "a208", "a209", "a210", "a211", "a212", "a213", "a214", "a215", "a216", "a217", "a218", "a219", "a220", "a221", "a222", "a223", "a224", "a225", "a226", "a227", "s3", "s4", "s5", "s6", "s7", "s8", "s9", "s10", "s11", "s12", "s13", "s14", "s15", "s16", "s17", "s18", "s19", "s20", "s21", "s22", "s23", "s24", "s25", "s26", "s27", "s28", "s29", "s30", "s31", "s33", "s34", "s35", "s36", "s37", "s38", "s39", "s40", "s41", "s42", "s43", "s44", "s45", "s46", "s47", "s48", "s49", "s50", "s51", "s52", "s53", "s54", "s55", "s56", "s57", "s58", "s59", "s60", "s61", "s62", "s63", "s64", "s65", "s66", "s67", "s68", "s69", "s70", "s71", "s72", "s73", "s74", "s75", "s76", "s77", "s78", "s79", "s80", "s81", "s82", "s83", "s84", "s85", "s86", "s87", "s88", "s89", "s90", "s91", "s92", "s93", "s94", "s95", "s96", "s97", "s98", "s99", "vcc", "scc", "memory"
    );
}

extern "C" void kernel_launch(void* const* d_in, const int* in_sizes, int n_in, void* d_out, int out_size, void* d_ws, size_t ws_size, hipStream_t stream) {
    (void)hipFuncSetAttribute((const void*)k_main, hipFuncAttributeMaxDynamicSharedMemorySize, 160 * 1024);
    k_main<<<256, 256, 160 * 1024, stream>>>(d_in[0], d_in[1], d_in[2], d_in[4], d_in[5], d_in[6], d_out, d_ws);
}
```
